```python
import torch

def _softmax(x, axis=-1):
    return torch.softmax(x, dim=axis)
def _gelu(x):
    return torch.nn.functional.gelu(x, approximate="tanh")
def _silu(x):
    return torch.nn.functional.silu(x)
def _sigmoid(x):
    return torch.sigmoid(x)
def _layernorm(x, axis=-1, eps=1e-5):
    m = x.mean(dim=axis, keepdim=True)
    v = x.var(dim=axis, keepdim=True, unbiased=False)
    return (x - m) / torch.sqrt(v + eps)
def _scatter_add(tgt, idx, src, axis=0):
    return tgt.clone().index_add_(axis, idx, src)
def _segment_sum(src, seg, n):
    out = torch.zeros((n,) + src.shape[1:], dtype=src.dtype)
    return out.index_add_(0, seg, src)
def _pad(x, pw):
    flat = tuple(v for pair in reversed(pw) for v in pair)
    return torch.nn.functional.pad(x, flat)

kernel_name = 'interleave_layers__mha_block_cached__dense_activation_gelu__decode'

def setup_inputs():

    _w = (lambda a: (lambda t: t.to(torch.bfloat16) if t.is_floating_point() else t)(torch.as_tensor(a)))
    def _A_setup():
        B, Sq, D = 1, 1, 512
        H, Sk, Dh = 32, 4096, 128
        return {
            "x": _w((lambda *s: torch.randn(*s, dtype=torch.bfloat16) * 0.05)(B, Sq, D)),
            "k_cache": _w((lambda *s: torch.randn(*s, dtype=torch.bfloat16) * 0.8)(B, H, Sk, Dh)),
            "v_cache": _w((lambda *s: torch.randn(*s, dtype=torch.bfloat16) * 1.6)(B, H, Sk, Dh)),
            "wq": _w((lambda *s: torch.randn(*s, dtype=torch.bfloat16) * 0.8)(D, H * Dh) * (D ** -0.5)),
            "wo": _w((lambda *s: torch.randn(*s, dtype=torch.bfloat16) * 1.6)(H * Dh, D) * ((H * Dh) ** -0.5)),
        }
    _A_in = _A_setup()

    def _B_setup():
        B, S, D, F = 1, 1, 512, 1376
        return {
            "x": _w((lambda *s: torch.randn(*s, dtype=torch.bfloat16) * 0.05)(B, S, D)),
            "w1": _w((lambda *s: torch.randn(*s, dtype=torch.bfloat16) * 0.4)(D, F) * (D ** -0.5)),
            "b1": _w((lambda *s: torch.randn(*s, dtype=torch.bfloat16) * 0.4)(F) * 0.01),
            "w2": _w((lambda *s: torch.randn(*s, dtype=torch.bfloat16) * 0.4)(F, D) * (F ** -0.5)),
            "b2": _w((lambda *s: torch.randn(*s, dtype=torch.bfloat16) * 0.4)(D) * 0.01),
        }
    _B_in = _B_setup()

    return {
        "A_x": _A_in["x"],
        "A_k_cache": _A_in["k_cache"],
        "A_v_cache": _A_in["v_cache"],
        "A_wq": _A_in["wq"],
        "A_wo": _A_in["wo"],
        "B_x": _B_in["x"],
        "B_w1": _B_in["w1"],
        "B_b1": _B_in["b1"],
        "B_w2": _B_in["w2"],
        "B_b2": _B_in["b2"],
    }

def reference(A_x, A_k_cache, A_v_cache, A_wq, A_wo, B_x, B_w1, B_b1, B_w2, B_b2):
    def _A_ref(x, k_cache, v_cache, wq, wo):
        B, Sq, D = x.shape
        H, Dh = 32, 128
        q = (lambda x, *d: x.permute(*d))((x @ wq).reshape(B, Sq, H, Dh), 0, 2, 1, 3)
        scores = torch.einsum("bhqd,bhkd->bhqk", q, k_cache) * (Dh ** -0.5)
        p = _softmax(scores, axis=-1)
        o = torch.einsum("bhqk,bhkd->bhqd", p, v_cache)
        return (lambda x, *d: x.permute(*d))(o, 0, 2, 1, 3).reshape(B, Sq, H * Dh) @ wo

    def _B_ref(x, w1, b1, w2, b2):
        h = torch.einsum("bsd,df->bsf", x, w1) + b1
        h = _gelu(h)
        return torch.einsum("bsf,fd->bsd", h, w2) + b2

    x = A_x
    x = x + _A_ref(_layernorm(x, axis=-1), A_k_cache, A_v_cache, A_wq, A_wo)
    x = x + _B_ref(_layernorm(x, axis=-1), B_w1, B_b1, B_w2, B_b2)
    x = x + _A_ref(_layernorm(x, axis=-1), A_k_cache, A_v_cache, A_wq, A_wo)
    x = x + _B_ref(_layernorm(x, axis=-1), B_w1, B_b1, B_w2, B_b2)
    return x
```

```cpp
#include <hip/hip_runtime.h>
#include <hip/hip_bf16.h>
#include <stdint.h>

typedef unsigned short u16;
typedef unsigned u32x4 __attribute__((ext_vector_type(4)));
typedef __bf16 bf16x2 __attribute__((ext_vector_type(2)));

constexpr int D = 512, H = 32, SK = 4096, DH = 128, F = 1376;
constexpr int NSPLIT = 8, KPS = SK / NSPLIT;

__device__ __forceinline__ float bf2f(u16 b) { return __builtin_bit_cast(float, (unsigned)b << 16); }
__device__ __forceinline__ float lo(unsigned w) { return __builtin_bit_cast(float, w << 16); }
__device__ __forceinline__ float hi(unsigned w) { return __builtin_bit_cast(float, w & 0xffff0000u); }
__device__ __forceinline__ u16 f2bf(float f) { unsigned u = __builtin_bit_cast(unsigned, f); return (u16)((u + 0x7fffu + ((u >> 16) & 1u)) >> 16); }
__device__ __forceinline__ float wsum(float v) { for (int o = 32; o > 0; o >>= 1) v += __shfl_xor(v, o); return v; }
__device__ __forceinline__ float wmax(float v) { for (int o = 32; o > 0; o >>= 1) v = fmaxf(v, __shfl_xor(v, o)); return v; }

__device__ void ln512(const float* xin, float* xn, float* red) {
  int t = threadIdx.x;
  float a = xin[t], b = xin[t + 256];
  float s = wsum(a + b);
  if ((t & 63) == 0) red[t >> 6] = s;
  __syncthreads();
  float m = (red[0] + red[1] + red[2] + red[3]) * (1.f / 512);
  __syncthreads();
  float da = a - m, db = b - m;
  s = wsum(da * da + db * db);
  if ((t & 63) == 0) red[t >> 6] = s;
  __syncthreads();
  float v = (red[0] + red[1] + red[2] + red[3]) * (1.f / 512);
  float r = 1.f / sqrtf(v + 1e-5f);
  xn[t] = da * r; xn[t + 256] = db * r;
  __syncthreads();
}

__global__ void __launch_bounds__(256) k_q(const float* x, const u16* wq, float* q) {
  __shared__ float xs[512], xn[512], red[4], part[4][128];
  int t = threadIdx.x, h = blockIdx.x;
  xs[t] = x[t]; xs[t + 256] = x[t + 256];
  __syncthreads();
  ln512(xs, xn, red);
  int w = t >> 6, l = t & 63;
  float a0 = 0, a1 = 0;
  for (int r = w * 128; r < w * 128 + 128; ++r) {
    unsigned v = *(const unsigned*)(wq + (size_t)r * (H * DH) + h * DH + 2 * l);
    a0 += xn[r] * lo(v); a1 += xn[r] * hi(v);
  }
  part[w][2 * l] = a0; part[w][2 * l + 1] = a1;
  __syncthreads();
  if (t < 128) q[h * DH + t] = part[0][t] + part[1][t] + part[2][t] + part[3][t];
}

__global__ void __launch_bounds__(256) k_init(const u16* x, float* xf) { int t = threadIdx.x; xf[t] = bf2f(x[t]); xf[t + 256] = bf2f(x[t + 256]); }

__global__ void __launch_bounds__(256) k_attn(const float* q, const u16* K, const u16* V, float* po, float* pml) {
  __shared__ float sc[KPS], red[4], acc[4][4][128];
  int t = threadIdx.x, s = blockIdx.x, h = blockIdx.y, w = t >> 6, l = t & 63, r = l >> 4, c = l & 15;
  float qf[8];
  for (int j = 0; j < 8; ++j) qf[j] = q[h * DH + 8 * c + j] * 0.08838834764831845f;
  const u16* kb = K + ((size_t)h * SK + s * KPS) * DH;
  const u16* vb = V + ((size_t)h * SK + s * KPS) * DH;
  for (int i = 0; i < KPS / 16; ++i) {
    int key = i * 16 + w * 4 + r;
    u32x4 v = *(const u32x4*)(kb + (size_t)key * DH + 8 * c);
    float d = 0;
    for (int j = 0; j < 4; ++j) d += qf[2 * j] * lo(v[j]) + qf[2 * j + 1] * hi(v[j]);
    for (int o = 8; o > 0; o >>= 1) d += __shfl_xor(d, o);
    if (c == 0) sc[key] = d;
  }
  __syncthreads();
  float m = fmaxf(sc[t], sc[t + 256]);
  m = wmax(m);
  if (l == 0) red[w] = m;
  __syncthreads();
  m = fmaxf(fmaxf(red[0], red[1]), fmaxf(red[2], red[3]));
  __syncthreads();
  float p0 = __expf(sc[t] - m), p1 = __expf(sc[t + 256] - m);
  sc[t] = p0; sc[t + 256] = p1;
  float sum = wsum(p0 + p1);
  if (l == 0) red[w] = sum;
  __syncthreads();
  sum = red[0] + red[1] + red[2] + red[3];
  float a[8] = {0, 0, 0, 0, 0, 0, 0, 0};
  for (int i = 0; i < KPS / 16; ++i) {
    int key = i * 16 + w * 4 + r;
    u32x4 v = *(const u32x4*)(vb + (size_t)key * DH + 8 * c);
    float p = sc[key];
    for (int j = 0; j < 4; ++j) { a[2 * j] += p * lo(v[j]); a[2 * j + 1] += p * hi(v[j]); }
  }
  for (int j = 0; j < 8; ++j) acc[w][r][8 * c + j] = a[j];
  __syncthreads();
  if (t < 128) {
    float o = 0;
    for (int i = 0; i < 16; ++i) o += acc[i >> 2][i & 3][t];
    po[(h * NSPLIT + s) * DH + t] = o;
  }
  if (t == 0) { pml[(h * NSPLIT + s) * 2] = m; pml[(h * NSPLIT + s) * 2 + 1] = sum; }
}

__global__ void __launch_bounds__(256) k_wo(const float* po, const float* pml, const u16* wo, float* part) {
  __shared__ float o[128];
  int t = threadIdx.x, h = blockIdx.x;
  if (t < 128) {
    float M = -INFINITY;
    for (int s = 0; s < NSPLIT; ++s) M = fmaxf(M, pml[(h * NSPLIT + s) * 2]);
    float L = 0, a = 0;
    for (int s = 0; s < NSPLIT; ++s) { float wgt = __expf(pml[(h * NSPLIT + s) * 2] - M); L += wgt * pml[(h * NSPLIT + s) * 2 + 1]; a += wgt * po[(h * NSPLIT + s) * DH + t]; }
    o[t] = a / L;
  }
  __syncthreads();
  float a0 = 0, a1 = 0;
  for (int r = 0; r < 128; ++r) {
    unsigned v = *(const unsigned*)(wo + (size_t)(h * DH + r) * D + 2 * t);
    a0 += o[r] * lo(v); a1 += o[r] * hi(v);
  }
  part[h * D + 2 * t] = a0; part[h * D + 2 * t + 1] = a1;
}

__global__ void __launch_bounds__(256) k_mlp(const float* x, const float* part, const u16* w1, const u16* b1, const u16* w2, float* x1out, float* part2) {
  __shared__ float xs[512], xn[512], red[4], hp[8][32], hh[32];
  int t = threadIdx.x, g = blockIdx.x;
  float a = x[t], b = x[t + 256];
  for (int h = 0; h < H; ++h) { a += part[h * D + t]; b += part[h * D + t + 256]; }
  xs[t] = a; xs[t + 256] = b;
  if (g == 0) { x1out[t] = a; x1out[t + 256] = b; }
  __syncthreads();
  ln512(xs, xn, red);
  int c = t & 31, rr = t >> 5;
  float s = 0;
  for (int r = rr * 64; r < rr * 64 + 64; ++r) s += xn[r] * bf2f(w1[(size_t)r * F + g * 32 + c]);
  hp[rr][c] = s;
  __syncthreads();
  if (t < 32) {
    float v = bf2f(b1[g * 32 + t]);
    for (int i = 0; i < 8; ++i) v += hp[i][t];
    float u = 0.7978845608028654f * (v + 0.044715f * v * v * v);
    hh[t] = 0.5f * v * (1.f + tanhf(u));
  }
  __syncthreads();
  float a0 = 0, a1 = 0;
  for (int r = 0; r < 32; ++r) {
    unsigned v = *(const unsigned*)(w2 + (size_t)(g * 32 + r) * D + 2 * t);
    a0 += hh[r] * lo(v); a1 += hh[r] * hi(v);
  }
  part2[g * D + 2 * t] = a0; part2[g * D + 2 * t + 1] = a1;
}

__global__ void __launch_bounds__(256) k_fin(const float* x1, const float* part2, const u16* b2, float* x2, u16* out) {
  int t = threadIdx.x;
  for (int k = 0; k < 2; ++k) {
    int i = t + 256 * k;
    float a = x1[i] + bf2f(b2[i]);
    for (int g = 0; g < 43; ++g) a += part2[g * D + i];
    x2[i] = a;
    if (out) out[i] = f2bf(a);
  }
}

extern "C" void kernel_launch(void* const* d_in, const int* in_sizes, int n_in, void* d_out, int out_size, void* d_ws, size_t ws_size, hipStream_t stream) {
  const u16* x = (const u16*)d_in[0]; const u16* K = (const u16*)d_in[1]; const u16* V = (const u16*)d_in[2];
  const u16* wq = (const u16*)d_in[3]; const u16* wo = (const u16*)d_in[4];
  const u16* w1 = (const u16*)d_in[6]; const u16* b1 = (const u16*)d_in[7]; const u16* w2 = (const u16*)d_in[8]; const u16* b2 = (const u16*)d_in[9];
  float* ws = (float*)d_ws;
  float* xa = ws; float* xb = ws + 512; float* q = ws + 1024; float* po = q + 4096; float* pml = po + 256 * 128; float* part = pml + 512; float* part2 = part + 32 * 512;
  k_init<<<1, 256, 0, stream>>>(x, xa);
  for (int L = 0; L < 2; ++L) {
    k_q<<<32, 256, 0, stream>>>(xa, wq, q);
    k_attn<<<dim3(NSPLIT, H), 256, 0, stream>>>(q, K, V, po, pml);
    k_wo<<<32, 256, 0, stream>>>(po, pml, wo, part);
    k_mlp<<<43, 256, 0, stream>>>(xa, part, w1, b1, w2, xb, part2);
    k_fin<<<1, 256, 0, stream>>>(xb, part2, b2, xa, L == 1 ? (u16*)d_out : nullptr);
  }
}
```

```cpp
#include <hip/hip_runtime.h>
#include <stdint.h>

typedef unsigned short u16;
typedef unsigned long long u64;
typedef unsigned u32x4 __attribute__((ext_vector_type(4)));
typedef unsigned u32x2 __attribute__((ext_vector_type(2)));
typedef float f32x4 __attribute__((ext_vector_type(4)));
typedef float f32x2 __attribute__((ext_vector_type(2)));
typedef short bf16x8 __attribute__((ext_vector_type(8)));
typedef __bf16 bf16x2 __attribute__((ext_vector_type(2)));
typedef __amdgpu_buffer_rsrc_t rsrc_t;

#define KV_AUX 2
#define W_AUX 2
#define SLP_A 10
#define SLP_B 10
#define SLP_C 10
#define SLP_CN 20
#define SLP_D 10

constexpr int DM = 512, NH = 32, SK = 4096, DH = 128, FF = 1376;
constexpr int NCU = 256, NTHR = 512, NU = 86;
constexpr unsigned SENT = 0xFFFFFFFFu;
constexpr unsigned SPIN_MAX = 1u << 17;
constexpr int SC1 = 16;
constexpr float QS = 0.08838834764831845f * 1.4426950408889634f;

constexpr int WS_FLAGS = 4096;
constexpr int WS_D1 = 8192;
constexpr int A_REC = 384, A_SZ = NCU * A_REC;
constexpr int WS_A = WS_D1 + NCU * 1024;
constexpr int B_SZ = 256 * 128;
constexpr int WS_B = WS_A + 2 * A_SZ;
constexpr int C_SZ = NU * 2048;
constexpr int WS_C = WS_B + 2 * B_SZ;
constexpr int D2_REC = 896;
constexpr int WS_D2 = WS_C + 2 * C_SZ;
constexpr int WS_END = WS_D2 + NCU * D2_REC;
constexpr int WS_STAMP = 4 << 20;

struct Params {
  const u16 *x, *K, *V, *wq, *wo, *w1, *b1, *w2, *b2;
  u16* out;
  unsigned char* ws;
};

__device__ __forceinline__ float lo(unsigned w) { return __builtin_bit_cast(float, w << 16); }
__device__ __forceinline__ float hi(unsigned w) { return __builtin_bit_cast(float, w & 0xffff0000u); }
__device__ __forceinline__ float asf(unsigned w) { return __builtin_bit_cast(float, w); }
__device__ __forceinline__ unsigned asu(float f) { return __builtin_bit_cast(unsigned, f); }
__device__ __forceinline__ unsigned pk2(float a, float b) { const bf16x2 v = {(__bf16)a, (__bf16)b}; return __builtin_bit_cast(unsigned, v); }
__device__ __forceinline__ unsigned sanu(unsigned u) { return u == SENT ? 0xFFFFFFFEu : u; }
__device__ __forceinline__ unsigned san(float f) { return sanu(asu(f)); }
template <int CTRL> __device__ __forceinline__ unsigned dppu(unsigned x) { return (unsigned)__builtin_amdgcn_mov_dpp((int)x, CTRL, 0xf, 0xf, true); }
template <int CTRL> __device__ __forceinline__ float dpp(float x) { return asf(dppu<CTRL>(asu(x))); }
constexpr int XOR1 = 0xB1, XOR2 = 0x4E, HMIR = 0x141, MIR = 0x140, ROR8 = 0x128, BC = 0x150, SHL = 0x100;
__device__ __forceinline__ float tr32(float a, float b) {
  auto s = __builtin_amdgcn_permlane32_swap(asu(a), asu(b), false, false);
  return asf(s[0]) + asf(s[1]);
}
__device__ __forceinline__ float tr16(float a, float b) {
  auto s = __builtin_amdgcn_permlane16_swap(asu(a), asu(b), false, false);
  return asf(s[0]) + asf(s[1]);
}
template <int CTRL> __device__ __forceinline__ float trd(float a, float b, bool sel) {
  const float keep = sel ? b : a, send = sel ? a : b;
  return keep + dpp<CTRL>(send);
}
__device__ __forceinline__ float sum16(float x) { x += dpp<XOR1>(x); x += dpp<XOR2>(x); x += dpp<HMIR>(x); x += dpp<MIR>(x); return x; }
__device__ __forceinline__ float sum64(float x) { x = sum16(x); x = tr16(x, x); x = tr32(x, x); return x; }
__device__ __forceinline__ float max16(float x) { x = fmaxf(x, dpp<XOR1>(x)); x = fmaxf(x, dpp<XOR2>(x)); x = fmaxf(x, dpp<HMIR>(x)); x = fmaxf(x, dpp<MIR>(x)); return x; }
__device__ __forceinline__ float ex2(float x) { return __builtin_amdgcn_exp2f(x); }
__device__ __forceinline__ f32x4 mfma(u32x4 a, u32x4 b, f32x4 c) {
  return __builtin_amdgcn_mfma_f32_16x16x32_bf16(__builtin_bit_cast(bf16x8, a), __builtin_bit_cast(bf16x8, b), c, 0, 0, 0);
}
__device__ __forceinline__ float sum8(f32x4 a, f32x4 b) { const f32x4 s = a + b; return (s[0] + s[1]) + (s[2] + s[3]); }

template <int AUX> __device__ __forceinline__ u32x4 ld128(rsrc_t r, int voff, int soff = 0) { return __builtin_amdgcn_raw_buffer_load_b128(r, voff, soff, AUX); }
template <int AUX> __device__ __forceinline__ u32x2 ld64(rsrc_t r, int voff, int soff = 0) { return __builtin_amdgcn_raw_buffer_load_b64(r, voff, soff, AUX); }
template <int AUX> __device__ __forceinline__ unsigned ld32(rsrc_t r, int voff, int soff = 0) { return __builtin_amdgcn_raw_buffer_load_b32(r, voff, soff, AUX); }
template <int AUX> __device__ __forceinline__ unsigned ld16(rsrc_t r, int voff, int soff = 0) { return __builtin_amdgcn_raw_buffer_load_b16(r, voff, soff, AUX); }
__device__ __forceinline__ void st128(rsrc_t r, int voff, u32x4 v) { __builtin_amdgcn_raw_buffer_store_b128(v, r, voff, 0, SC1); }
__device__ __forceinline__ rsrc_t mk(const void* p, unsigned bytes) { return __builtin_amdgcn_make_buffer_rsrc((void*)p, 0, bytes, 0x00020000); }
__device__ __forceinline__ unsigned um(unsigned a, unsigned b) { return a > b ? a : b; }
__device__ __forceinline__ unsigned mx4(u32x4 v) { return um(um(v.x, v.y), um(v.z, v.w)); }
__device__ __forceinline__ unsigned mx2(u32x2 v) { return um(v.x, v.y); }

#define FENCE() do { asm volatile("" ::: "memory"); __builtin_amdgcn_sched_barrier(0); } while (0)
#define BAR() do { asm volatile("s_waitcnt lgkmcnt(0)" ::: "memory"); __builtin_amdgcn_s_barrier(); asm volatile("" ::: "memory"); } while (0)
#define LWAIT() asm volatile("s_waitcnt lgkmcnt(0)" ::: "memory")
#define TS(i) do { } while (0)
#define TSDUMP() do { } while (0)

struct __attribute__((aligned(16))) Lds {
  unsigned char big[8][16384];
  u32x4 wqs[16][64];
  float obuf[8][128];
  unsigned qb[64];
  float CS[128];
  float ml[8][2];
  float ybuf[16][8];
  float sbuf[16][8];
  float s12[2][8];
  float cs1p[16][8];
  float cbuf[64][8];
  float x2L[64];
  float xs[64];
  float xfull[512];
};

struct Ctx { int t, b, w, l, g, c, xg, idx, h, s, cu, u; rsrc_t rws; };

__device__ __forceinline__ void make_frags(Lds& L, const Ctx& X, const u32x4 (&raw)[2], u32x4 (&fr)[2], int base) {
  unsigned char* st = &L.big[X.w][base];
#pragma unroll
  for (int i = 0; i < 2; ++i) *(u32x4*)(st + (32 * i + (X.l >> 1)) * 32 + (X.l & 1) * 16) = raw[i];
  LWAIT();
#pragma unroll
  for (int ci = 0; ci < 2; ++ci)
#pragma unroll
    for (int j2 = 0; j2 < 4; ++j2) {
      const unsigned a = *(const u16*)(st + (32 * ci + 8 * X.g + 2 * j2) * 32 + X.c * 2), bb = *(const u16*)(st + (32 * ci + 8 * X.g + 2 * j2 + 1) * 32 + X.c * 2);
      fr[ci][j2] = a | (bb << 16);
    }
}

template <int LAYER>
__device__ __forceinline__ void attention(Lds& L, const Ctx& X, const Params& p, const u32x4 (&kf)[16], const u32x4 (&vr)[16], const unsigned (&msk)[4], u32x4 fl, u64 epoch) {
  const int t = X.t, b = X.b, w = X.w, l = X.l;
  (void)b;
  u32x4 qf[4];
#pragma unroll
  for (int i = 0; i < 4; ++i) qf[i] = *(const u32x4*)&L.qb[16 * i + 4 * X.g];
  f32x4 sc[4];
#pragma unroll
  for (int T = 0; T < 4; ++T) sc[T] = f32x4{0.f, 0.f, 0.f, 0.f};
#pragma unroll
  for (int i = 0; i < 4; ++i)
#pragma unroll
    for (int T = 0; T < 4; ++T) sc[T] = mfma(qf[i], kf[4 * T + i], sc[T]);
  const float m = max16(fmaxf(fmaxf(sc[0][0], sc[1][0]), fmaxf(sc[2][0], sc[3][0])));
  const float sel = X.g == 0 ? sc[0][0] : X.g == 1 ? sc[1][0] : X.g == 2 ? sc[2][0] : sc[3][0];
  const float pe = ex2(sel - m);
  const unsigned pd = pk2(pe, pe);
  if (LAYER == 0) TS(5);
  f32x4 o0 = {0.f, 0.f, 0.f, 0.f}, o1 = {0.f, 0.f, 0.f, 0.f};
#define PVS(n) { const unsigned pv = dppu<BC + n>(pd); const u32x4 a = {pv & msk[0], pv & msk[1], pv & msk[2], pv & msk[3]}; if (n & 1) o1 = mfma(a, vr[n], o1); else o0 = mfma(a, vr[n], o0); }
  PVS(0) PVS(1) PVS(2) PVS(3) PVS(4) PVS(5) PVS(6) PVS(7) PVS(8) PVS(9) PVS(10) PVS(11) PVS(12) PVS(13) PVS(14) PVS(15)
#undef PVS
  const float lsum = sum64(lo(pd));
  o0 += o1;
  if (l < 32) *(f32x4*)&L.obuf[w][8 * X.c + 4 * X.g] = o0;
  if (l == 0) { L.ml[w][0] = m; L.ml[w][1] = lsum; }
  if (LAYER == 0) {
    const unsigned e0 = (unsigned)epoch, e1 = (unsigned)(epoch >> 32);
    if (w >= 6 && !__all(((fl.x ^ e0) | (fl.y ^ e1) | (fl.z ^ e0) | (fl.w ^ e1)) == 0u)) {
      for (unsigned sp_ = 0; sp_ < SPIN_MAX; ++sp_) {
        __builtin_amdgcn_s_sleep(16);
        asm volatile("" ::: "memory"); fl = ld128<SC1>(X.rws, WS_FLAGS + (t & 127) * 16);
        if (__all(((fl.x ^ e0) | (fl.y ^ e1) | (fl.z ^ e0) | (fl.w ^ e1)) == 0u)) break;
      }
    }
  }
  BAR();
  if (w == 0) {
    const f32x4 a0 = *(const f32x4*)&L.ml[0][0], a1 = *(const f32x4*)&L.ml[2][0], a2 = *(const f32x4*)&L.ml[4][0], a3 = *(const f32x4*)&L.ml[6][0];
    const float mw[8] = {a0[0], a0[2], a1[0], a1[2], a2[0], a2[2], a3[0], a3[2]}, lw[8] = {a0[1], a0[3], a1[1], a1[3], a2[1], a2[3], a3[1], a3[3]};
    const float M = fmaxf(fmaxf(fmaxf(mw[0], mw[1]), fmaxf(mw[2], mw[3])), fmaxf(fmaxf(mw[4], mw[5]), fmaxf(mw[6], mw[7])));
    f32x2 o = {0.f, 0.f}; float Ls = 0.f;
#pragma unroll
    for (int u = 0; u < 8; ++u) { const float f = ex2(mw[u] - M); o += f * *(const f32x2*)&L.obuf[u][2 * l]; Ls += f * lw[u]; }
    const unsigned pk = sanu(pk2(o[0], o[1]));
    u32x4 v = {pk, dppu<0x55>(pk), dppu<0xAA>(pk), dppu<0xFF>(pk)};
    int off = l * 4;
    if (l == 1) { v = u32x4{san(M), san(Ls), 0u, 0u}; off = 256; }
    if ((l & 3) == 0 || l == 1) st128(X.rws, WS_A + LAYER * A_SZ + X.cu * A_REC + off, v);
  }
  BAR();
}

template <int LAYER, typename HOOK>
__device__ __forceinline__ void wo_stage(Lds& L, const Ctx& X, const Params& p, const u32x4 (&wof)[2], HOOK hook) {
  const int t = X.t, b = X.b, w = X.w, l = X.l;
  (void)b; (void)t;
  const int rec = WS_A + LAYER * A_SZ + (X.xg * 32 + (w >> 1) * 8 + (l & 7)) * A_REC;
  const int o0 = rec + ((w & 1) * 64 + 8 * X.g) * 2;
  u32x4 a0, a1; u32x2 mlv;
  __builtin_amdgcn_s_sleep(SLP_A);
  FENCE();
  a0 = ld128<SC1>(X.rws, o0); a1 = ld128<SC1>(X.rws, o0 + 64); mlv = ld64<SC1>(X.rws, rec + 256);
  FENCE();
  hook();
  FENCE();
  if (!__all(um(um(mx4(a0), mx4(a1)), mx2(mlv)) != SENT)) {
    for (unsigned sp_ = 0; sp_ < SPIN_MAX; ++sp_) {
      asm volatile("" ::: "memory");
      a0 = ld128<SC1>(X.rws, o0); a1 = ld128<SC1>(X.rws, o0 + 64); mlv = ld64<SC1>(X.rws, rec + 256);
      if (__all(um(um(mx4(a0), mx4(a1)), mx2(mlv)) != SENT)) break;
    }
  }
  TS(LAYER * 10 + 8);
  f32x4 d = mfma(wof[0], a0, f32x4{0.f, 0.f, 0.f, 0.f});
  d = mfma(wof[1], a1, d);
  const float m = asf(mlv.x), ls = asf(mlv.y);
  float M = fmaxf(m, dpp<XOR1>(m)); M = fmaxf(M, dpp<XOR2>(M)); M = fmaxf(M, dpp<HMIR>(M));
  const float e = ex2(m - M);
  float Lt = e * ls; Lt += dpp<XOR1>(Lt); Lt += dpp<XOR2>(Lt); Lt += dpp<HMIR>(Lt);
  const float f = e * __builtin_amdgcn_rcpf(Lt);
  d *= f;
  float y2[2];
#pragma unroll
  for (int i = 0; i < 2; ++i) y2[i] = trd<HMIR>(d[i], d[i + 2], (l & 4) != 0);
  float y1 = trd<XOR2>(y2[0], y2[1], (l & 2) != 0);
  y1 += dpp<XOR1>(y1);
  if ((l & 9) == 0) L.ybuf[4 * X.g + ((l >> 1) & 3)][w] = y1;
  BAR();
  if (w == 0) {
    const float s = sum8(*(const f32x4*)&L.ybuf[X.c][0], *(const f32x4*)&L.ybuf[X.c][4]);
    const unsigned pk = sanu(pk2(s, dpp<XOR1>(s)));
    const u32x4 v = {pk, dppu<SHL + 2>(pk), dppu<SHL + 4>(pk), dppu<SHL + 6>(pk)};
    if (l == 0 || l == 8) st128(X.rws, WS_B + LAYER * B_SZ + X.cu * 128 + l * 2, v);
  }
  BAR();
}

template <int LAYER, typename HOOK>
__device__ __forceinline__ void mlp_stage(Lds& L, const Ctx& X, const Params& p, const u32x4 (&xf)[2], const u32x4 (&w1f)[2], const float (&w2f)[16], float& cs1, const float& b1v, float xres, HOOK hook) {
  const int t = X.t, b = X.b, w = X.w, l = X.l;
  (void)b;
  const int bb = WS_B + LAYER * B_SZ;
  const int o0 = bb + ((l & 7) * 32 + 4 * w + (X.g >> 1)) * 128 + (X.g & 1) * 16, o2 = bb + ((l >> 3) * 32 + 4 * X.s + ((l & 7) >> 1)) * 128 + (l & 1) * 16;
  u32x4 a0, a1, a2;
  __builtin_amdgcn_s_sleep(SLP_B);
  FENCE();
  a0 = ld128<SC1>(X.rws, o0); a1 = ld128<SC1>(X.rws, o0 + 256); a2 = ld128<SC1>(X.rws, o2);
  FENCE();
  hook();
  FENCE();
  if (!__all(um(um(mx4(a0), mx4(a1)), mx4(a2)) != SENT)) {
    for (unsigned sp_ = 0; sp_ < SPIN_MAX; ++sp_) {
      asm volatile("" ::: "memory");
      a0 = ld128<SC1>(X.rws, o0); a1 = ld128<SC1>(X.rws, o0 + 256); a2 = ld128<SC1>(X.rws, o2);
      if (__all(um(um(mx4(a0), mx4(a1)), mx4(a2)) != SENT)) break;
    }
  }
  TS(LAYER * 10 + 10);
  if (X.u >= 0) {
    const bool src = X.c < 8;
#pragma unroll
    for (int j = 0; j < 4; ++j) { a0[j] = src ? a0[j] : xf[0][j]; a1[j] = src ? a1[j] : xf[1][j]; }
    const u32x4 ones = {0x3F803F80u, 0x3F803F80u, 0x3F803F80u, 0x3F803F80u};
    const f32x4 z = {0.f, 0.f, 0.f, 0.f};
    f32x4 S = mfma(w1f[0], a0, z), s1 = mfma(ones, a0, z), G = mfma(a0, a0, z);
    S = mfma(w1f[1], a1, S); s1 = mfma(ones, a1, s1); G = mfma(a1, a1, G);
    float S2[2];
#pragma unroll
    for (int j = 0; j < 2; ++j) S2[j] = trd<ROR8>(S[j], S[j + 2], (l & 8) != 0);
    float S1 = trd<HMIR>(S2[0], S2[1], (l & 4) != 0);
    S1 += dpp<XOR2>(S1); S1 += dpp<XOR1>(S1);
    const float s1t = sum16(s1[0]);
    const float s2t = sum64((G[0] + G[1]) + (G[2] + G[3]));
    if ((l & 3) == 0) L.sbuf[4 * X.g + ((l >> 3) & 1) * 2 + ((l >> 2) & 1)][w] = S1;
    if (l == 0) { L.s12[0][w] = s1t; L.s12[1][w] = s2t; }
    BAR();
    const float St = sum8(*(const f32x4*)&L.sbuf[X.c][0], *(const f32x4*)&L.sbuf[X.c][4]);
    if (LAYER == 0) cs1 = sum8(*(const f32x4*)&L.cs1p[X.c][0], *(const f32x4*)&L.cs1p[X.c][4]);
    const float mean = sum8(*(const f32x4*)&L.s12[0][0], *(const f32x4*)&L.s12[0][4]) * (1.f / 512);
    const float ms = sum8(*(const f32x4*)&L.s12[1][0], *(const f32x4*)&L.s12[1][4]) * (1.f / 512);
    const float rstd = __builtin_amdgcn_rsqf(fmaxf(ms - mean * mean, 0.f) + 1e-5f);
    const float hp = rstd * (St - mean * cs1) + b1v;
    const float uu = hp * (0.7978845608028654f + 0.7978845608028654f * 0.044715f * hp * hp);
    const float ee = ex2(uu * (2.f * 1.4426950408889634f));
    const float hv = hp - hp * __builtin_amdgcn_rcpf(ee + 1.f);
    float acc0 = 0.f, acc1 = 0.f;
#define DSTEP(F) acc0 = fmaf(dpp<BC + F>(hv), w2f[F], acc0); acc1 = fmaf(dpp<BC + F + 1>(hv), w2f[F + 1], acc1);
    DSTEP(0) DSTEP(2) DSTEP(4) DSTEP(6) DSTEP(8) DSTEP(10) DSTEP(12) DSTEP(14)
#undef DSTEP
    const unsigned av = san(acc0 + acc1);
    const u32x4 v = {av, dppu<0x55>(av), dppu<0xAA>(av), dppu<0xFF>(av)};
    if ((l & 3) == 0) st128(X.rws, WS_C + LAYER * C_SZ + X.u * 2048 + t * 4, v);
    TS(LAYER * 10 + 17);
  }
  if (w == 7) {
    float o[8];
#pragma unroll
    for (int j = 0; j < 4; ++j) { o[2 * j] = lo(a2[j]); o[2 * j + 1] = hi(a2[j]); }
    float o4[4], oo2[2];
#pragma unroll
    for (int j = 0; j < 4; ++j) o4[j] = tr32(o[j], o[j + 4]);
#pragma unroll
    for (int j = 0; j < 2; ++j) oo2[j] = tr16(o4[j], o4[j + 2]);
    const float v = trd<ROR8>(oo2[0], oo2[1], (l & 8) != 0);
    L.xs[8 * (l & 7) + (l >> 3)] = v + xres;
  }
  if (X.u >= 0) __builtin_amdgcn_s_sleep(SLP_C); else __builtin_amdgcn_s_sleep(SLP_CN);
}

template <int LAYER>
__device__ __forceinline__ void c_gather(Lds& L, const Ctx& X, const Params& p) {
  const int t = X.t, b = X.b, w = X.w;
  (void)b; (void)t;
  u32x4 a[3];
  for (unsigned sp_ = 0;; ++sp_) {
    asm volatile("" ::: "memory");
    unsigned mxv = 0;
#pragma unroll
    for (int i = 0; i < 3; ++i) {
      const int src = w * 4 + X.g + 32 * i;
      a[i] = ld128<SC1>(X.rws, src < NU ? WS_C + LAYER * C_SZ + src * 2048 + (64 * X.s + 4 * X.c) * 4 : WS_END);
      mxv = um(mxv, mx4(a[i]));
    }
    if (__all(mxv != SENT) || sp_ > SPIN_MAX) break;
  }
  TS(LAYER * 10 + 12);
  float v[4];
#pragma unroll
  for (int j = 0; j < 4; ++j) v[j] = asf(a[0][j]) + asf(a[1][j]) + asf(a[2][j]);
  const float v0 = tr32(v[0], v[2]), v1 = tr32(v[1], v[3]);
  L.cbuf[4 * X.c + X.g][w] = tr16(v0, v1);
  BAR();
}

__global__ void __launch_bounds__(NTHR, 2) fused_kernel(Params p) {
  __shared__ Lds L;
  Ctx X;
  const int t = threadIdx.x, b = blockIdx.x;
  const int w = __builtin_amdgcn_readfirstlane(t >> 6), l = t & 63, g = l >> 4, c = l & 15;
  const int xg = b & 7, idx = b >> 3, h = xg * 4 + (idx >> 3), s = idx & 7, cu = xg * 32 + idx;
  int u = idx < 12 ? 4 * (xg + 8 * (idx >> 2)) + (idx & 3) : -1;
  if (u >= NU) u = -1;
  TS(0);
  const rsrc_t rws = mk(p.ws, WS_END);
  X.t = t; X.b = b; X.w = w; X.l = l; X.g = g; X.c = c; X.xg = xg; X.idx = idx; X.h = h; X.s = s; X.cu = cu; X.u = u; X.rws = rws;
  const rsrc_t rK = mk(p.K + (size_t)cu * 65536, 131072), rV = mk(p.V + (size_t)cu * 65536, 131072);
  const rsrc_t rx = mk(p.x, 1024), rwq = mk(p.wq, DM * NH * DH * 2), rwo = mk(p.wo, DM * NH * DH * 2);
  const unsigned mz = u >= 0 ? 1u : 0u;
  const rsrc_t rw1 = mk(p.w1, mz * DM * FF * 2), rw2 = mk(p.w2, mz * DM * FF * 2), rb1 = mk(p.b1, mz * FF * 2), rb2 = mk(p.b2, DM * 2);
  const rsrc_t rd1 = mk(p.ws + WS_D1, w == 0 ? NCU * 1024 : 0);
  unsigned msk[4];
#pragma unroll
  for (int i = 0; i < 4; ++i) msk[i] = (c >> 1) == i ? ((c & 1) ? 0xFFFF0000u : 0x0000FFFFu) : 0u;
  const u32x2 ep = ld64<0>(rws, 0);
  const int xcol = 8 * (l & 7) + (l >> 3);
  const unsigned xres_r = ld16<0>(rx, (64 * s + xcol) * 2), b2_r = ld16<0>(rb2, (64 * s + xcol) * 2);
  u32x4 xf[2];
#pragma unroll
  for (int ci = 0; ci < 2; ++ci) xf[ci] = ld128<0>(rx, c == 8 ? (64 * w + 32 * ci + 8 * g) * 2 : 1024);
  if (w == 0) {
    const u32x4 xv = ld128<0>(rx, l * 16);
    const unsigned xq = ld16<0>(rx, (64 * s + 4 * c + g) * 2);
    u32x4 wqr[16];
#pragma unroll
    for (int n = 0; n < 16; ++n) wqr[n] = ld128<W_AUX>(rwq, (64 * s + g) * 8192 + h * 256 + c * 16, n * 4 * 8192);
    FENCE();
    __builtin_amdgcn_s_barrier();
    FENCE();
    float xs[8];
#pragma unroll
    for (int j = 0; j < 4; ++j) { xs[2 * j] = lo(xv[j]); xs[2 * j + 1] = hi(xv[j]); }
    const float mean = sum64(((xs[0] + xs[1]) + (xs[2] + xs[3])) + ((xs[4] + xs[5]) + (xs[6] + xs[7]))) * (1.f / 512);
    float d2 = 0.f;
#pragma unroll
    for (int j = 0; j < 8; ++j) { const float d = xs[j] - mean; d2 += d * d; }
    const float rstd = __builtin_amdgcn_rsqf(sum64(d2) * (1.f / 512) + 1e-5f);
    const float xn = (lo(xq) - mean) * rstd;
    const unsigned pd = pk2(xn, xn);
    const u32x4 onesA = {0x3F803F80u & msk[0], 0x3F803F80u & msk[1], 0x3F803F80u & msk[2], 0x3F803F80u & msk[3]};
    f32x4 aq = {0.f, 0.f, 0.f, 0.f}, ac = {0.f, 0.f, 0.f, 0.f};
#define QST(n) { const unsigned pv = dppu<BC + n>(pd); const u32x4 a = {pv & msk[0], pv & msk[1], pv & msk[2], pv & msk[3]}; aq = mfma(a, wqr[n], aq); ac = mfma(onesA, wqr[n], ac); L.wqs[n][l] = wqr[n]; }
    QST(0) QST(1) QST(2) QST(3) QST(4) QST(5) QST(6) QST(7) QST(8) QST(9) QST(10) QST(11) QST(12) QST(13) QST(14) QST(15)
#undef QST
    const unsigned tag = ep.x + 1u;
    if (l < 32) {
      const int off = WS_D1 + cu * 1024 + (8 * c + 4 * g) * 8;
      st128(rws, off, u32x4{pk2(aq[0], ac[0]), tag, pk2(aq[1], ac[1]), tag});
      st128(rws, off + 16, u32x4{pk2(aq[2], ac[2]), tag, pk2(aq[3], ac[3]), tag});
    }
    TS(1);
  }
  FENCE();
  if (w == 1) {
#pragma unroll
    for (int k = 0; k < 6; ++k) {
      const int q = l + 64 * k;
      int off = WS_END;
      if (q < 17) off = WS_A + cu * A_REC + q * 16;
      else if (q < 34) off = WS_A + A_SZ + cu * A_REC + (q - 17) * 16;
      else if (q < 36) off = WS_B + cu * 128 + (q - 34) * 16;
      else if (q < 38) off = WS_B + B_SZ + cu * 128 + (q - 36) * 16;
      else if (q >= 64 && q < 113) off = WS_D2 + cu * D2_REC + (q - 64) * 16;
      else if (q >= 128 && u >= 0) off = WS_C + ((q - 128) >> 7) * C_SZ + u * 2048 + ((q - 128) & 127) * 16;
      st128(rws, off, u32x4{SENT, SENT, SENT, SENT});
    }
    FENCE();
    __builtin_amdgcn_s_barrier();
    asm volatile("s_waitcnt vmcnt(0)" ::: "memory");
    const u64 e1 = (((u64)ep.y << 32) | ep.x) + 1;
    if (l == 0) __builtin_amdgcn_raw_buffer_store_b64(u32x2{(unsigned)e1, (unsigned)(e1 >> 32)}, rws, WS_FLAGS + cu * 8, 0, SC1);
    TS(4);
  }
  if (w >= 2) __builtin_amdgcn_s_barrier();
  FENCE();
  u32x4 kf[16], vr[16];
#pragma unroll
  for (int i = 0; i < 16; ++i) kf[i] = ld128<KV_AUX>(rK, w * 16384 + l * 16, i * 1024);
  FENCE();
  u32x4 d1[8];
#pragma unroll
  for (int i = 0; i < 8; ++i) d1[i] = ld128<SC1>(rd1, h * 8192 + l * 16, i * 1024);
  FENCE();
#pragma unroll
  for (int i = 0; i < 16; ++i)  vr[i] = ld128<KV_AUX>(rV, w * 16384 + g * 4096 + c * 16, i * 256);
  FENCE();
  const u64 epoch = (((u64)ep.y << 32) | ep.x) + 1;
  {
    unsigned char* st = L.big[w];
#pragma unroll
    for (int i = 0; i < 16; ++i) { const int key = 4 * i + g; *(u32x4*)(st + key * 256 + ((c ^ (key & 15)) * 16)) = kf[i]; }
    LWAIT();
#pragma unroll
    for (int T = 0; T < 4; ++T)
#pragma unroll
      for (int i = 0; i < 4; ++i) kf[4 * T + i] = *(const u32x4*)(st + (16 * T + c) * 256 + (((4 * i + g) ^ c) * 16));
  }
  TS(2);
  if (w == 0) {
    const unsigned tag = (unsigned)epoch;
    unsigned bad = 0;
#pragma unroll
    for (int i = 0; i < 8; ++i) bad |= (d1[i].y ^ tag) | (d1[i].w ^ tag);
    if (!__all(bad == 0u)) {
      for (unsigned sp_ = 0; sp_ < SPIN_MAX; ++sp_) {
        asm volatile("" ::: "memory");
        bad = 0;
#pragma unroll
        for (int i = 0; i < 8; ++i) { d1[i] = ld128<SC1>(rd1, h * 8192 + l * 16, i * 1024); bad |= (d1[i].y ^ tag) | (d1[i].w ^ tag); }
        if (__all(bad == 0u)) break;
      }
    }
    float qa = 0.f, qc = 0.f, ca = 0.f, cb = 0.f;
#pragma unroll
    for (int i = 0; i < 8; ++i) { qa += lo(d1[i].x); ca += hi(d1[i].x); qc += lo(d1[i].z); cb += hi(d1[i].z); }
    L.qb[l] = pk2(qa * QS, qc * QS);
    *(f32x2*)&L.CS[2 * l] = f32x2{ca, cb};
  }
  BAR();
  TS(3);
  u32x4 wor[2];
#pragma unroll
  for (int i = 0; i < 2; ++i) wor[i] = ld128<W_AUX>(rwo, (xg * 512 + 64 * w + 32 * i + (l >> 1)) * 1024 + idx * 32 + (l & 1) * 16);
  const rsrc_t rfl = mk(p.ws + WS_FLAGS, w >= 6 ? 2048 : 0);
  const u32x4 fl = ld128<SC1>(rfl, (t & 127) * 16);
  FENCE();

  attention<0>(L, X, p, kf, vr, msk, fl, epoch);
  TS(6);
  u32x4 wof[2];
  u32x4 w1r[2]; unsigned w2r[16]; unsigned b1r = 0;
  const int uc = u >= 0 ? u : 0;
  wo_stage<0>(L, X, p, wof, [&]() {
#pragma unroll
    for (int i = 0; i < 2; ++i) w1r[i] = ld128<W_AUX>(rw1, (64 * w + 32 * i + (l >> 1)) * (FF * 2) + uc * 32 + (l & 1) * 16);
#pragma unroll
    for (int f = 0; f < 16; ++f) w2r[f] = ld32<W_AUX>(rw2, (16 * uc + f) * 1024 + (t >> 1) * 4);
    b1r = ld16<0>(rb1, (16 * uc + c) * 2);
    FENCE();
    make_frags(L, X, wor, wof, 0);
  });
  TS(9);
  u32x4 w1f[2]; float w2f[16]; float cs1 = 0.f, b1v = 0.f;
  const float b2v = lo(b2_r);
  mlp_stage<0>(L, X, p, xf, w1f, w2f, cs1, b1v, lo(xres_r) + b2v, [&]() {
    make_frags(L, X, w1r, w1f, 2048);
#pragma unroll
    for (int f = 0; f < 16; ++f) w2f[f] = (t & 1) ? hi(w2r[f]) : lo(w2r[f]);
    b1v = lo(b1r);
    const u32x4 ones = {0x3F803F80u, 0x3F803F80u, 0x3F803F80u, 0x3F803F80u};
    f32x4 S = mfma(w1f[0], ones, f32x4{0.f, 0.f, 0.f, 0.f});
    S = mfma(w1f[1], ones, S);
    if (c == 0) {
#pragma unroll
      for (int i = 0; i < 4; ++i) L.cs1p[4 * g + i][w] = S[i];
    }
  });
  TS(11);
  c_gather<0>(L, X, p);
  {
    float wqf[16];
#pragma unroll
    for (int i = 0; i < 2; ++i) {
      const u32x4 q4 = L.wqs[2 * w + i][l];
#pragma unroll
      for (int j = 0; j < 4; ++j) { wqf[8 * i + 2 * j] = lo(q4[j]); wqf[8 * i + 2 * j + 1] = hi(q4[j]); }
    }
    const int r0 = 8 * w + g;
    const float xa = sum8(*(const f32x4*)&L.cbuf[r0][0], *(const f32x4*)&L.cbuf[r0][4]) + L.xs[r0];
    const float xb = sum8(*(const f32x4*)&L.cbuf[r0 + 4][0], *(const f32x4*)&L.cbuf[r0 + 4][4]) + L.xs[r0 + 4];
    float q8[8];
#pragma unroll
    for (int j = 0; j < 8; ++j) q8[j] = xa * wqf[j] + xb * wqf[8 + j];
    float q4[4];
#pragma unroll
    for (int j = 0; j < 4; ++j) q4[j] = tr32(q8[j], q8[j + 4]);
    const f32x2 q2 = {tr16(q4[0], q4[2]), tr16(q4[1], q4[3])};
    *(f32x2*)&L.obuf[w][8 * c + 2 * g] = q2;
    if (c == 0) { L.x2L[r0] = xa; L.x2L[r0 + 4] = xb; }
    BAR();
    if (w == 0) {
      f32x4 q = {0.f, 0.f, 0.f, 0.f};
#pragma unroll
      for (int i = 0; i < 8; ++i) q += *(const f32x4*)&L.obuf[i][4 * (l & 31)];
      if (l < 32) st128(rws, WS_D2 + cu * D2_REC + l * 16, u32x4{san(q[0]), san(q[1]), san(q[2]), san(q[3])});
    } else if (w == 1) {
      const float xv2 = L.x2L[l];
      const float mn = sum64(xv2) * (1.f / 64);
      const float dd = xv2 - mn;
      const float m2 = sum64(dd * dd);
      const unsigned xu = san(xv2);
      u32x4 v = {xu, dppu<0x55>(xu), dppu<0xAA>(xu), dppu<0xFF>(xu)};
      int off = 512 + l * 4;
      if (l == 1) { v = u32x4{san(mn), san(m2), 0u, 0u}; off = 768; }
      if ((l & 3) == 0 || l == 1) st128(rws, WS_D2 + cu * D2_REC + off, v);
    }
    BAR();
  }
  TS(13);
  float xres2;
  {
    __builtin_amdgcn_s_sleep(SLP_D);
    const int rec = WS_D2 + (h * 8 + (l >> 3)) * D2_REC;
    u32x2 q2, st2; u32x4 x4;
    for (unsigned sp_ = 0;; ++sp_) {
      asm volatile("" ::: "memory");
      q2 = ld64<SC1>(rws, rec + (16 * w + 2 * (l & 7)) * 4);
      st2 = ld64<SC1>(rws, rec + 768);
      x4 = ld128<SC1>(rws, WS_D2 + (h * 8 + w) * D2_REC + 512 + c * 16);
      if (__all(um(um(mx2(q2), mx2(st2)), mx4(x4)) != SENT) || sp_ > SPIN_MAX) break;
    }
    TS(14);
    float qa = asf(q2.x), qb = asf(q2.y);
    qa += dpp<ROR8>(qa); qb += dpp<ROR8>(qb);
    qa = tr16(qa, qa); qb = tr16(qb, qb);
    qa = tr32(qa, qa); qb = tr32(qb, qb);
    const float ms = asf(st2.x);
    float mean = ms + dpp<ROR8>(ms); mean = tr16(mean, mean); mean = tr32(mean, mean); mean *= 0.125f;
    const float dm = ms - mean;
    float M2 = asf(st2.y) + 64.f * dm * dm; M2 += dpp<ROR8>(M2); M2 = tr16(M2, M2); M2 = tr32(M2, M2);
    const float rstd = __builtin_amdgcn_rsqf(M2 * (1.f / 512) + 1e-5f) * QS;
    const f32x2 cs = *(const f32x2*)&L.CS[16 * w + 2 * (l & 7)];
    if (l < 8) L.qb[8 * w + l] = pk2((qa - mean * cs[0]) * rstd, (qb - mean * cs[1]) * rstd);
    if (g == 0) *(u32x4*)&L.xfull[64 * w + 4 * c] = x4;
    xres2 = L.x2L[xcol] + b2v;
  }
  BAR();
  TS(15);
  attention<1>(L, X, p, kf, vr, msk, fl, epoch);
  TS(16);
  wo_stage<1>(L, X, p, wof, [&]() {
#pragma unroll
  for (int ci = 0; ci < 2; ++ci) {
    const f32x4 p0 = *(const f32x4*)&L.xfull[64 * w + 32 * ci + 8 * g], p1 = *(const f32x4*)&L.xfull[64 * w + 32 * ci + 8 * g + 4];
    const float xv8[8] = {p0[0], p0[1], p0[2], p0[3], p1[0], p1[1], p1[2], p1[3]};
#pragma unroll
    for (int j = 0; j < 4; ++j) {
      const unsigned hh = pk2(xv8[2 * j], xv8[2 * j + 1]);
      const unsigned ll = pk2(xv8[2 * j] - lo(hh), xv8[2 * j + 1] - hi(hh));
      xf[ci][j] = c == 8 ? hh : c == 9 ? ll : 0u;
    }
  }
  });
  TS(19);
  const bool fin = (xg == 0 && idx >= 24);
  if (u < 0 && !fin) { TSDUMP(); return; }
  mlp_stage<1>(L, X, p, xf, w1f, w2f, cs1, b1v, xres2, []() {});
  TS(21);
  if (!fin) { TSDUMP(); return; }
  c_gather<1>(L, X, p);
  if (w == 0) {
    const float o = sum8(*(const f32x4*)&L.cbuf[l][0], *(const f32x4*)&L.cbuf[l][4]) + L.xs[l];
    p.out[64 * s + l] = (u16)(pk2(o, o) & 0xffffu);
    if (s == 0 && l == 0) *(u64*)p.ws = epoch;
  }
  TS(23);
  TSDUMP();
}

extern "C" void kernel_launch(void* const* d_in, const int* in_sizes, int n_in, void* d_out, int out_size, void* d_ws, size_t ws_size, hipStream_t stream) {
  Params p;
  p.x = (const u16*)d_in[0]; p.K = (const u16*)d_in[1]; p.V = (const u16*)d_in[2]; p.wq = (const u16*)d_in[3]; p.wo = (const u16*)d_in[4];
  p.w1 = (const u16*)d_in[6]; p.b1 = (const u16*)d_in[7]; p.w2 = (const u16*)d_in[8]; p.b2 = (const u16*)d_in[9];
  p.out = (u16*)d_out; p.ws = (unsigned char*)d_ws;
  hipLaunchKernelGGL(fused_kernel, dim3(NCU), dim3(NTHR), 0, stream, p);
}
```

```cpp
#include <hip/hip_runtime.h>
#include <stdint.h>

typedef unsigned short u16;
typedef unsigned long long u64;
typedef unsigned u32x4 __attribute__((ext_vector_type(4)));
typedef unsigned u32x2 __attribute__((ext_vector_type(2)));
typedef float f32x4 __attribute__((ext_vector_type(4)));
typedef float f32x2 __attribute__((ext_vector_type(2)));
typedef short bf16x8 __attribute__((ext_vector_type(8)));
typedef __bf16 bf16x2 __attribute__((ext_vector_type(2)));
typedef __amdgpu_buffer_rsrc_t rsrc_t;

#define KV_AUX 2
#define W_AUX 2
#define SLP_A 4
#define SLP_B 4
#define SLP_C 4
#define SLP_CN 12
#define SLP_D 4

constexpr int DM = 512, NH = 32, SK = 4096, DH = 128, FF = 1376;
constexpr int NCU = 256, NTHR = 512, NU = 86;
constexpr unsigned SENT = 0xFFFFFFFFu;
constexpr unsigned SPIN_MAX = 1u << 17;
constexpr int SC1 = 16;
constexpr float QS = 0.08838834764831845f * 1.4426950408889634f;

constexpr int WS_FLAGS = 4096;
constexpr int WS_D1 = 8192;
constexpr int A_REC = 384, A_SZ = NCU * A_REC;
constexpr int WS_A = WS_D1 + NCU * 1024;
constexpr int B_SZ = 256 * 128;
constexpr int WS_B = WS_A + 2 * A_SZ;
constexpr int C_SZ = NU * 2048;
constexpr int WS_C = WS_B + 2 * B_SZ;
constexpr int D2_REC = 896;
constexpr int WS_D2 = WS_C + 2 * C_SZ;
constexpr int WS_END = WS_D2 + NCU * D2_REC;
constexpr int WS_STAMP = 4 << 20;

struct Params {
  const u16 *x, *K, *V, *wq, *wo, *w1, *b1, *w2, *b2;
  u16* out;
  unsigned char* ws;
};

__device__ __forceinline__ float lo(unsigned w) { return __builtin_bit_cast(float, w << 16); }
__device__ __forceinline__ float hi(unsigned w) { return __builtin_bit_cast(float, w & 0xffff0000u); }
__device__ __forceinline__ float asf(unsigned w) { return __builtin_bit_cast(float, w); }
__device__ __forceinline__ unsigned asu(float f) { return __builtin_bit_cast(unsigned, f); }
__device__ __forceinline__ unsigned pk2(float a, float b) { const bf16x2 v = {(__bf16)a, (__bf16)b}; return __builtin_bit_cast(unsigned, v); }
__device__ __forceinline__ unsigned sanu(unsigned u) { return u == SENT ? 0xFFFFFFFEu : u; }
__device__ __forceinline__ unsigned san(float f) { return sanu(asu(f)); }
template <int CTRL> __device__ __forceinline__ unsigned dppu(unsigned x) { return (unsigned)__builtin_amdgcn_mov_dpp((int)x, CTRL, 0xf, 0xf, true); }
template <int CTRL> __device__ __forceinline__ float dpp(float x) { return asf(dppu<CTRL>(asu(x))); }
constexpr int XOR1 = 0xB1, XOR2 = 0x4E, HMIR = 0x141, MIR = 0x140, ROR8 = 0x128, BC = 0x150, SHL = 0x100;
__device__ __forceinline__ float tr32(float a, float b) {
  auto s = __builtin_amdgcn_permlane32_swap(asu(a), asu(b), false, false);
  return asf(s[0]) + asf(s[1]);
}
__device__ __forceinline__ float tr16(float a, float b) {
  auto s = __builtin_amdgcn_permlane16_swap(asu(a), asu(b), false, false);
  return asf(s[0]) + asf(s[1]);
}
template <int CTRL> __device__ __forceinline__ float trd(float a, float b, bool sel) {
  const float keep = sel ? b : a, send = sel ? a : b;
  return keep + dpp<CTRL>(send);
}
__device__ __forceinline__ float sum16(float x) { x += dpp<XOR1>(x); x += dpp<XOR2>(x); x += dpp<HMIR>(x); x += dpp<MIR>(x); return x; }
__device__ __forceinline__ float sum64(float x) { x = sum16(x); x = tr16(x, x); x = tr32(x, x); return x; }
__device__ __forceinline__ float max16(float x) { x = fmaxf(x, dpp<XOR1>(x)); x = fmaxf(x, dpp<XOR2>(x)); x = fmaxf(x, dpp<HMIR>(x)); x = fmaxf(x, dpp<MIR>(x)); return x; }
__device__ __forceinline__ float ex2(float x) { return __builtin_amdgcn_exp2f(x); }
__device__ __forceinline__ f32x4 mfma(u32x4 a, u32x4 b, f32x4 c) {
  return __builtin_amdgcn_mfma_f32_16x16x32_bf16(__builtin_bit_cast(bf16x8, a), __builtin_bit_cast(bf16x8, b), c, 0, 0, 0);
}
__device__ __forceinline__ float sum8(f32x4 a, f32x4 b) { const f32x4 s = a + b; return (s[0] + s[1]) + (s[2] + s[3]); }

template <int AUX> __device__ __forceinline__ u32x4 ld128(rsrc_t r, int voff, int soff = 0) { return __builtin_amdgcn_raw_buffer_load_b128(r, voff, soff, AUX); }
template <int AUX> __device__ __forceinline__ u32x2 ld64(rsrc_t r, int voff, int soff = 0) { return __builtin_amdgcn_raw_buffer_load_b64(r, voff, soff, AUX); }
template <int AUX> __device__ __forceinline__ unsigned ld32(rsrc_t r, int voff, int soff = 0) { return __builtin_amdgcn_raw_buffer_load_b32(r, voff, soff, AUX); }
template <int AUX> __device__ __forceinline__ unsigned ld16(rsrc_t r, int voff, int soff = 0) { return __builtin_amdgcn_raw_buffer_load_b16(r, voff, soff, AUX); }
__device__ __forceinline__ void st128(rsrc_t r, int voff, u32x4 v) { __builtin_amdgcn_raw_buffer_store_b128(v, r, voff, 0, SC1); }
__device__ __forceinline__ rsrc_t mk(const void* p, unsigned bytes) { return __builtin_amdgcn_make_buffer_rsrc((void*)p, 0, bytes, 0x00020000); }
__device__ __forceinline__ unsigned um(unsigned a, unsigned b) { return a > b ? a : b; }
__device__ __forceinline__ unsigned mx4(u32x4 v) { return um(um(v.x, v.y), um(v.z, v.w)); }
__device__ __forceinline__ unsigned mx2(u32x2 v) { return um(v.x, v.y); }

#define FENCE() do { asm volatile("" ::: "memory"); __builtin_amdgcn_sched_barrier(0); } while (0)
#define BAR() do { asm volatile("s_waitcnt lgkmcnt(0)" ::: "memory"); __builtin_amdgcn_s_barrier(); asm volatile("" ::: "memory"); } while (0)
#define LWAIT() asm volatile("s_waitcnt lgkmcnt(0)" ::: "memory")
#define TS(i) do { } while (0)
#define TSDUMP() do { } while (0)

struct __attribute__((aligned(16))) Lds {
  unsigned char big[8][16384];
  u32x4 wqs[16][64];
  float obuf[8][128];
  unsigned qb[64];
  float CS[128];
  float ml[8][2];
  float ybuf[16][8];
  float sbuf[16][8];
  float s12[2][8];
  float cs1p[16][8];
  float cbuf[64][8];
  float x2L[64];
  float xs[64];
  float xfull[512];
};

struct Ctx { int t, b, w, l, g, c, xg, idx, h, s, cu, u; rsrc_t rws; };

__device__ __forceinline__ void make_frags(Lds& L, const Ctx& X, const u32x4 (&raw)[2], u32x4 (&fr)[2], int base) {
  unsigned char* st = &L.big[X.w][base];
#pragma unroll
  for (int i = 0; i < 2; ++i) *(u32x4*)(st + (32 * i + (X.l >> 1)) * 32 + (X.l & 1) * 16) = raw[i];
  LWAIT();
#pragma unroll
  for (int ci = 0; ci < 2; ++ci)
#pragma unroll
    for (int j2 = 0; j2 < 4; ++j2) {
      const unsigned a = *(const u16*)(st + (32 * ci + 8 * X.g + 2 * j2) * 32 + X.c * 2), bb = *(const u16*)(st + (32 * ci + 8 * X.g + 2 * j2 + 1) * 32 + X.c * 2);
      fr[ci][j2] = a | (bb << 16);
    }
}

template <int LAYER>
__device__ __forceinline__ void attention(Lds& L, const Ctx& X, const Params& p, const u32x4 (&kf)[16], const u32x4 (&vr)[16], const unsigned (&msk)[4], u32x4 fl, u64 epoch) {
  const int t = X.t, b = X.b, w = X.w, l = X.l;
  (void)b;
  u32x4 qf[4];
#pragma unroll
  for (int i = 0; i < 4; ++i) qf[i] = *(const u32x4*)&L.qb[16 * i + 4 * X.g];
  f32x4 sc[4];
#pragma unroll
  for (int T = 0; T < 4; ++T) sc[T] = f32x4{0.f, 0.f, 0.f, 0.f};
#pragma unroll
  for (int i = 0; i < 4; ++i)
#pragma unroll
    for (int T = 0; T < 4; ++T) sc[T] = mfma(qf[i], kf[4 * T + i], sc[T]);
  const float m = max16(fmaxf(fmaxf(sc[0][0], sc[1][0]), fmaxf(sc[2][0], sc[3][0])));
  const float sel = X.g == 0 ? sc[0][0] : X.g == 1 ? sc[1][0] : X.g == 2 ? sc[2][0] : sc[3][0];
  const float pe = ex2(sel - m);
  const unsigned pd = pk2(pe, pe);
  if (LAYER == 0) TS(5);
  f32x4 o0 = {0.f, 0.f, 0.f, 0.f}, o1 = {0.f, 0.f, 0.f, 0.f};
#define PVS(n) { const unsigned pv = dppu<BC + n>(pd); const u32x4 a = {pv & msk[0], pv & msk[1], pv & msk[2], pv & msk[3]}; if (n & 1) o1 = mfma(a, vr[n], o1); else o0 = mfma(a, vr[n], o0); }
  PVS(0) PVS(1) PVS(2) PVS(3) PVS(4) PVS(5) PVS(6) PVS(7) PVS(8) PVS(9) PVS(10) PVS(11) PVS(12) PVS(13) PVS(14) PVS(15)
#undef PVS
  const float lsum = sum64(lo(pd));
  o0 += o1;
  if (l < 32) *(f32x4*)&L.obuf[w][8 * X.c + 4 * X.g] = o0;
  if (l == 0) { L.ml[w][0] = m; L.ml[w][1] = lsum; }
  if (LAYER == 0) {
    const unsigned e0 = (unsigned)epoch, e1 = (unsigned)(epoch >> 32);
    if (w >= 6 && !__all(((fl.x ^ e0) | (fl.y ^ e1) | (fl.z ^ e0) | (fl.w ^ e1)) == 0u)) {
      for (unsigned sp_ = 0; sp_ < SPIN_MAX; ++sp_) {
        __builtin_amdgcn_s_sleep(16);
        asm volatile("" ::: "memory"); fl = ld128<SC1>(X.rws, WS_FLAGS + (t & 127) * 16);
        if (__all(((fl.x ^ e0) | (fl.y ^ e1) | (fl.z ^ e0) | (fl.w ^ e1)) == 0u)) break;
      }
    }
  }
  BAR();
  if (w == 0) {
    const f32x4 a0 = *(const f32x4*)&L.ml[0][0], a1 = *(const f32x4*)&L.ml[2][0], a2 = *(const f32x4*)&L.ml[4][0], a3 = *(const f32x4*)&L.ml[6][0];
    const float mw[8] = {a0[0], a0[2], a1[0], a1[2], a2[0], a2[2], a3[0], a3[2]}, lw[8] = {a0[1], a0[3], a1[1], a1[3], a2[1], a2[3], a3[1], a3[3]};
    const float M = fmaxf(fmaxf(fmaxf(mw[0], mw[1]), fmaxf(mw[2], mw[3])), fmaxf(fmaxf(mw[4], mw[5]), fmaxf(mw[6], mw[7])));
    f32x2 o = {0.f, 0.f}; float Ls = 0.f;
#pragma unroll
    for (int u = 0; u < 8; ++u) { const float f = ex2(mw[u] - M); o += f * *(const f32x2*)&L.obuf[u][2 * l]; Ls += f * lw[u]; }
    const unsigned pk = sanu(pk2(o[0], o[1]));
    u32x4 v = {pk, dppu<0x55>(pk), dppu<0xAA>(pk), dppu<0xFF>(pk)};
    int off = l * 4;
    if (l == 1) { v = u32x4{san(M), san(Ls), 0u, 0u}; off = 256; }
    if ((l & 3) == 0 || l == 1) st128(X.rws, WS_A + LAYER * A_SZ + X.cu * A_REC + off, v);
  }
  BAR();
}

template <int LAYER, typename HOOK>
__device__ __forceinline__ void wo_stage(Lds& L, const Ctx& X, const Params& p, const u32x4 (&wof)[2], HOOK hook) {
  const int t = X.t, b = X.b, w = X.w, l = X.l;
  (void)b; (void)t;
  const int rec = WS_A + LAYER * A_SZ + (X.xg * 32 + (w >> 1) * 8 + (l & 7)) * A_REC;
  const int o0 = rec + ((w & 1) * 64 + 8 * X.g) * 2;
  u32x4 a0, a1; u32x2 mlv;
  __builtin_amdgcn_s_sleep(SLP_A);
  FENCE();
  a0 = ld128<SC1>(X.rws, o0); a1 = ld128<SC1>(X.rws, o0 + 64); mlv = ld64<SC1>(X.rws, rec + 256);
  FENCE();
  hook();
  FENCE();
  if (!__all(um(um(mx4(a0), mx4(a1)), mx2(mlv)) != SENT)) {
    for (unsigned sp_ = 0; sp_ < SPIN_MAX; ++sp_) {
      asm volatile("" ::: "memory");
      a0 = ld128<SC1>(X.rws, o0); a1 = ld128<SC1>(X.rws, o0 + 64); mlv = ld64<SC1>(X.rws, rec + 256);
      if (__all(um(um(mx4(a0), mx4(a1)), mx2(mlv)) != SENT)) break;
    }
  }
  TS(LAYER * 10 + 8);
  f32x4 d = mfma(wof[0], a0, f32x4{0.f, 0.f, 0.f, 0.f});
  d = mfma(wof[1], a1, d);
  const float m = asf(mlv.x), ls = asf(mlv.y);
  float M = fmaxf(m, dpp<XOR1>(m)); M = fmaxf(M, dpp<XOR2>(M)); M = fmaxf(M, dpp<HMIR>(M));
  const float e = ex2(m - M);
  float Lt = e * ls; Lt += dpp<XOR1>(Lt); Lt += dpp<XOR2>(Lt); Lt += dpp<HMIR>(Lt);
  const float f = e * __builtin_amdgcn_rcpf(Lt);
  d *= f;
  float y2[2];
#pragma unroll
  for (int i = 0; i < 2; ++i) y2[i] = trd<HMIR>(d[i], d[i + 2], (l & 4) != 0);
  float y1 = trd<XOR2>(y2[0], y2[1], (l & 2) != 0);
  y1 += dpp<XOR1>(y1);
  if ((l & 9) == 0) L.ybuf[4 * X.g + ((l >> 1) & 3)][w] = y1;
  BAR();
  if (w == 0) {
    const float s = sum8(*(const f32x4*)&L.ybuf[X.c][0], *(const f32x4*)&L.ybuf[X.c][4]);
    const unsigned pk = sanu(pk2(s, dpp<XOR1>(s)));
    const u32x4 v = {pk, dppu<SHL + 2>(pk), dppu<SHL + 4>(pk), dppu<SHL + 6>(pk)};
    if (l == 0 || l == 8) st128(X.rws, WS_B + LAYER * B_SZ + X.cu * 128 + l * 2, v);
  }
  BAR();
}

template <int LAYER, typename HOOK>
__device__ __forceinline__ void mlp_stage(Lds& L, const Ctx& X, const Params& p, const u32x4 (&xf)[2], const u32x4 (&w1f)[2], const float (&w2f)[16], float& cs1, const float& b1v, float xres, HOOK hook) {
  const int t = X.t, b = X.b, w = X.w, l = X.l;
  (void)b;
  const int bb = WS_B + LAYER * B_SZ;
  const int o0 = bb + ((l & 7) * 32 + 4 * w + (X.g >> 1)) * 128 + (X.g & 1) * 16, o2 = bb + ((l >> 3) * 32 + 4 * X.s + ((l & 7) >> 1)) * 128 + (l & 1) * 16;
  u32x4 a0, a1, a2;
  __builtin_amdgcn_s_sleep(SLP_B);
  FENCE();
  a0 = ld128<SC1>(X.rws, o0); a1 = ld128<SC1>(X.rws, o0 + 256); a2 = ld128<SC1>(X.rws, o2);
  FENCE();
  hook();
  FENCE();
  if (!__all(um(um(mx4(a0), mx4(a1)), mx4(a2)) != SENT)) {
    for (unsigned sp_ = 0; sp_ < SPIN_MAX; ++sp_) {
      asm volatile("" ::: "memory");
      a0 = ld128<SC1>(X.rws, o0); a1 = ld128<SC1>(X.rws, o0 + 256); a2 = ld128<SC1>(X.rws, o2);
      if (__all(um(um(mx4(a0), mx4(a1)), mx4(a2)) != SENT)) break;
    }
  }
  TS(LAYER * 10 + 10);
  if (X.u >= 0) {
    const bool src = X.c < 8;
#pragma unroll
    for (int j = 0; j < 4; ++j) { a0[j] = src ? a0[j] : xf[0][j]; a1[j] = src ? a1[j] : xf[1][j]; }
    const u32x4 ones = {0x3F803F80u, 0x3F803F80u, 0x3F803F80u, 0x3F803F80u};
    const f32x4 z = {0.f, 0.f, 0.f, 0.f};
    f32x4 S = mfma(w1f[0], a0, z), s1 = mfma(ones, a0, z), G = mfma(a0, a0, z);
    S = mfma(w1f[1], a1, S); s1 = mfma(ones, a1, s1); G = mfma(a1, a1, G);
    float S2[2];
#pragma unroll
    for (int j = 0; j < 2; ++j) S2[j] = trd<ROR8>(S[j], S[j + 2], (l & 8) != 0);
    float S1 = trd<HMIR>(S2[0], S2[1], (l & 4) != 0);
    S1 += dpp<XOR2>(S1); S1 += dpp<XOR1>(S1);
    const float s1t = sum16(s1[0]);
    const float s2t = sum64((G[0] + G[1]) + (G[2] + G[3]));
    if ((l & 3) == 0) L.sbuf[4 * X.g + ((l >> 3) & 1) * 2 + ((l >> 2) & 1)][w] = S1;
    if (l == 0) { L.s12[0][w] = s1t; L.s12[1][w] = s2t; }
    BAR();
    const float St = sum8(*(const f32x4*)&L.sbuf[X.c][0], *(const f32x4*)&L.sbuf[X.c][4]);
    if (LAYER == 0) cs1 = sum8(*(const f32x4*)&L.cs1p[X.c][0], *(const f32x4*)&L.cs1p[X.c][4]);
    const float mean = sum8(*(const f32x4*)&L.s12[0][0], *(const f32x4*)&L.s12[0][4]) * (1.f / 512);
    const float ms = sum8(*(const f32x4*)&L.s12[1][0], *(const f32x4*)&L.s12[1][4]) * (1.f / 512);
    const float rstd = __builtin_amdgcn_rsqf(fmaxf(ms - mean * mean, 0.f) + 1e-5f);
    const float hp = rstd * (St - mean * cs1) + b1v;
    const float uu = hp * (0.7978845608028654f + 0.7978845608028654f * 0.044715f * hp * hp);
    const float ee = ex2(uu * (2.f * 1.4426950408889634f));
    const float hv = hp - hp * __builtin_amdgcn_rcpf(ee + 1.f);
    float acc0 = 0.f, acc1 = 0.f;
#define DSTEP(F) acc0 = fmaf(dpp<BC + F>(hv), w2f[F], acc0); acc1 = fmaf(dpp<BC + F + 1>(hv), w2f[F + 1], acc1);
    DSTEP(0) DSTEP(2) DSTEP(4) DSTEP(6) DSTEP(8) DSTEP(10) DSTEP(12) DSTEP(14)
#undef DSTEP
    const unsigned av = san(acc0 + acc1);
    const u32x4 v = {av, dppu<0x55>(av), dppu<0xAA>(av), dppu<0xFF>(av)};
    if ((l & 3) == 0) st128(X.rws, WS_C + LAYER * C_SZ + X.u * 2048 + t * 4, v);
    TS(LAYER * 10 + 17);
  }
  if (w == 7) {
    float o[8];
#pragma unroll
    for (int j = 0; j < 4; ++j) { o[2 * j] = lo(a2[j]); o[2 * j + 1] = hi(a2[j]); }
    float o4[4], oo2[2];
#pragma unroll
    for (int j = 0; j < 4; ++j) o4[j] = tr32(o[j], o[j + 4]);
#pragma unroll
    for (int j = 0; j < 2; ++j) oo2[j] = tr16(o4[j], o4[j + 2]);
    const float v = trd<ROR8>(oo2[0], oo2[1], (l & 8) != 0);
    L.xs[8 * (l & 7) + (l >> 3)] = v + xres;
  }
  if (X.u >= 0) __builtin_amdgcn_s_sleep(SLP_C); else __builtin_amdgcn_s_sleep(SLP_CN);
}

template <int LAYER>
__device__ __forceinline__ void c_gather(Lds& L, const Ctx& X, const Params& p) {
  const int t = X.t, b = X.b, w = X.w;
  (void)b; (void)t;
  u32x4 a[3];
  for (unsigned sp_ = 0;; ++sp_) {
    asm volatile("" ::: "memory");
    unsigned mxv = 0;
#pragma unroll
    for (int i = 0; i < 3; ++i) {
      const int src = w * 4 + X.g + 32 * i;
      a[i] = ld128<SC1>(X.rws, src < NU ? WS_C + LAYER * C_SZ + src * 2048 + (64 * X.s + 4 * X.c) * 4 : WS_END);
      mxv = um(mxv, mx4(a[i]));
    }
    if (__all(mxv != SENT) || sp_ > SPIN_MAX) break;
  }
  TS(LAYER * 10 + 12);
  float v[4];
#pragma unroll
  for (int j = 0; j < 4; ++j) v[j] = asf(a[0][j]) + asf(a[1][j]) + asf(a[2][j]);
  const float v0 = tr32(v[0], v[2]), v1 = tr32(v[1], v[3]);
  L.cbuf[4 * X.c + X.g][w] = tr16(v0, v1);
  BAR();
}

__global__ void __launch_bounds__(NTHR, 2) fused_kernel(Params p) {
  __shared__ Lds L;
  Ctx X;
  const int t = threadIdx.x, b = blockIdx.x;
  const int w = __builtin_amdgcn_readfirstlane(t >> 6), l = t & 63, g = l >> 4, c = l & 15;
  const int xg = b & 7, idx = b >> 3, h = xg * 4 + (idx >> 3), s = idx & 7, cu = xg * 32 + idx;
  int u = idx < 12 ? 4 * (xg + 8 * (idx >> 2)) + (idx & 3) : -1;
  if (u >= NU) u = -1;
  TS(0);
  const rsrc_t rws = mk(p.ws, WS_END);
  X.t = t; X.b = b; X.w = w; X.l = l; X.g = g; X.c = c; X.xg = xg; X.idx = idx; X.h = h; X.s = s; X.cu = cu; X.u = u; X.rws = rws;
  const rsrc_t rK = mk(p.K + (size_t)cu * 65536, 131072), rV = mk(p.V + (size_t)cu * 65536, 131072);
  const rsrc_t rx = mk(p.x, 1024), rwq = mk(p.wq, DM * NH * DH * 2), rwo = mk(p.wo, DM * NH * DH * 2);
  const unsigned mz = u >= 0 ? 1u : 0u;
  const rsrc_t rw1 = mk(p.w1, mz * DM * FF * 2), rw2 = mk(p.w2, mz * DM * FF * 2), rb1 = mk(p.b1, mz * FF * 2), rb2 = mk(p.b2, DM * 2);
  const rsrc_t rd1 = mk(p.ws + WS_D1, w == 0 ? NCU * 1024 : 0);
  unsigned msk[4];
#pragma unroll
  for (int i = 0; i < 4; ++i) msk[i] = (c >> 1) == i ? ((c & 1) ? 0xFFFF0000u : 0x0000FFFFu) : 0u;
  const u32x2 ep = ld64<0>(rws, 0);
  const int xcol = 8 * (l & 7) + (l >> 3);
  const unsigned xres_r = ld16<0>(rx, (64 * s + xcol) * 2), b2_r = ld16<0>(rb2, (64 * s + xcol) * 2);
  u32x4 xf[2];
#pragma unroll
  for (int ci = 0; ci < 2; ++ci) xf[ci] = ld128<0>(rx, c == 8 ? (64 * w + 32 * ci + 8 * g) * 2 : 1024);
  if (w == 0) {
    const u32x4 xv = ld128<0>(rx, l * 16);
    const unsigned xq = ld16<0>(rx, (64 * s + 4 * c + g) * 2);
    u32x4 wqr[16];
#pragma unroll
    for (int n = 0; n < 16; ++n) wqr[n] = ld128<W_AUX>(rwq, (64 * s + g) * 8192 + h * 256 + c * 16, n * 4 * 8192);
    FENCE();
    __builtin_amdgcn_s_barrier();
    FENCE();
    float xs[8];
#pragma unroll
    for (int j = 0; j < 4; ++j) { xs[2 * j] = lo(xv[j]); xs[2 * j + 1] = hi(xv[j]); }
    const float mean = sum64(((xs[0] + xs[1]) + (xs[2] + xs[3])) + ((xs[4] + xs[5]) + (xs[6] + xs[7]))) * (1.f / 512);
    float d2 = 0.f;
#pragma unroll
    for (int j = 0; j < 8; ++j) { const float d = xs[j] - mean; d2 += d * d; }
    const float rstd = __builtin_amdgcn_rsqf(sum64(d2) * (1.f / 512) + 1e-5f);
    const float xn = (lo(xq) - mean) * rstd;
    const unsigned pd = pk2(xn, xn);
    const u32x4 onesA = {0x3F803F80u & msk[0], 0x3F803F80u & msk[1], 0x3F803F80u & msk[2], 0x3F803F80u & msk[3]};
    f32x4 aq = {0.f, 0.f, 0.f, 0.f}, ac = {0.f, 0.f, 0.f, 0.f};
#define QST(n) { const unsigned pv = dppu<BC + n>(pd); const u32x4 a = {pv & msk[0], pv & msk[1], pv & msk[2], pv & msk[3]}; aq = mfma(a, wqr[n], aq); ac = mfma(onesA, wqr[n], ac); L.wqs[n][l] = wqr[n]; }
    QST(0) QST(1) QST(2) QST(3) QST(4) QST(5) QST(6) QST(7) QST(8) QST(9) QST(10) QST(11) QST(12) QST(13) QST(14) QST(15)
#undef QST
    const unsigned tag = ep.x + 1u;
    if (l < 32) {
      const int off = WS_D1 + cu * 1024 + (8 * c + 4 * g) * 8;
      st128(rws, off, u32x4{pk2(aq[0], ac[0]), tag, pk2(aq[1], ac[1]), tag});
      st128(rws, off + 16, u32x4{pk2(aq[2], ac[2]), tag, pk2(aq[3], ac[3]), tag});
    }
    TS(1);
  }
  FENCE();
  if (w == 1) {
#pragma unroll
    for (int k = 0; k < 6; ++k) {
      const int q = l + 64 * k;
      int off = WS_END;
      if (q < 17) off = WS_A + cu * A_REC + q * 16;
      else if (q < 34) off = WS_A + A_SZ + cu * A_REC + (q - 17) * 16;
      else if (q < 36) off = WS_B + cu * 128 + (q - 34) * 16;
      else if (q < 38) off = WS_B + B_SZ + cu * 128 + (q - 36) * 16;
      else if (q >= 64 && q < 113) off = WS_D2 + cu * D2_REC + (q - 64) * 16;
      else if (q >= 128 && u >= 0) off = WS_C + ((q - 128) >> 7) * C_SZ + u * 2048 + ((q - 128) & 127) * 16;
      st128(rws, off, u32x4{SENT, SENT, SENT, SENT});
    }
    FENCE();
    __builtin_amdgcn_s_barrier();
    asm volatile("s_waitcnt vmcnt(0)" ::: "memory");
    const u64 e1 = (((u64)ep.y << 32) | ep.x) + 1;
    if (l == 0) __builtin_amdgcn_raw_buffer_store_b64(u32x2{(unsigned)e1, (unsigned)(e1 >> 32)}, rws, WS_FLAGS + cu * 8, 0, SC1);
    TS(4);
  }
  if (w >= 2) __builtin_amdgcn_s_barrier();
  FENCE();
  u32x4 kf[16], vr[16];
#pragma unroll
  for (int i = 0; i < 16; ++i) kf[i] = ld128<KV_AUX>(rK, w * 16384 + l * 16, i * 1024);
  FENCE();
  u32x4 d1[8];
#pragma unroll
  for (int i = 0; i < 8; ++i) d1[i] = ld128<SC1>(rd1, h * 8192 + l * 16, i * 1024);
  FENCE();
#pragma unroll
  for (int i = 0; i < 16; ++i)  vr[i] = ld128<KV_AUX>(rV, w * 16384 + g * 4096 + c * 16, i * 256);
  FENCE();
  const u64 epoch = (((u64)ep.y << 32) | ep.x) + 1;
  {
    unsigned char* st = L.big[w];
#pragma unroll
    for (int i = 0; i < 16; ++i) { const int key = 4 * i + g; *(u32x4*)(st + key * 256 + ((c ^ (key & 15)) * 16)) = kf[i]; }
    LWAIT();
#pragma unroll
    for (int T = 0; T < 4; ++T)
#pragma unroll
      for (int i = 0; i < 4; ++i) kf[4 * T + i] = *(const u32x4*)(st + (16 * T + c) * 256 + (((4 * i + g) ^ c) * 16));
  }
  TS(2);
  if (w == 0) {
    const unsigned tag = (unsigned)epoch;
    unsigned bad = 0;
#pragma unroll
    for (int i = 0; i < 8; ++i) bad |= (d1[i].y ^ tag) | (d1[i].w ^ tag);
    if (!__all(bad == 0u)) {
      for (unsigned sp_ = 0; sp_ < SPIN_MAX; ++sp_) {
        asm volatile("" ::: "memory");
        bad = 0;
#pragma unroll
        for (int i = 0; i < 8; ++i) { d1[i] = ld128<SC1>(rd1, h * 8192 + l * 16, i * 1024); bad |= (d1[i].y ^ tag) | (d1[i].w ^ tag); }
        if (__all(bad == 0u)) break;
      }
    }
    float qa = 0.f, qc = 0.f, ca = 0.f, cb = 0.f;
#pragma unroll
    for (int i = 0; i < 8; ++i) { qa += lo(d1[i].x); ca += hi(d1[i].x); qc += lo(d1[i].z); cb += hi(d1[i].z); }
    L.qb[l] = pk2(qa * QS, qc * QS);
    *(f32x2*)&L.CS[2 * l] = f32x2{ca, cb};
  }
  BAR();
  TS(3);
  u32x4 wor[2];
#pragma unroll
  for (int i = 0; i < 2; ++i) wor[i] = ld128<W_AUX>(rwo, (xg * 512 + 64 * w + 32 * i + (l >> 1)) * 1024 + idx * 32 + (l & 1) * 16);
  const rsrc_t rfl = mk(p.ws + WS_FLAGS, w >= 6 ? 2048 : 0);
  const u32x4 fl = ld128<SC1>(rfl, (t & 127) * 16);
  FENCE();

  attention<0>(L, X, p, kf, vr, msk, fl, epoch);
  TS(6);
  u32x4 wof[2];
  u32x4 w1r[2]; unsigned w2r[16]; unsigned b1r = 0;
  const int uc = u >= 0 ? u : 0;
  wo_stage<0>(L, X, p, wof, [&]() {
#pragma unroll
    for (int i = 0; i < 2; ++i) w1r[i] = ld128<W_AUX>(rw1, (64 * w + 32 * i + (l >> 1)) * (FF * 2) + uc * 32 + (l & 1) * 16);
#pragma unroll
    for (int f = 0; f < 16; ++f) w2r[f] = ld32<W_AUX>(rw2, (16 * uc + f) * 1024 + (t >> 1) * 4);
    b1r = ld16<0>(rb1, (16 * uc + c) * 2);
    FENCE();
    make_frags(L, X, wor, wof, 0);
  });
  TS(9);
  u32x4 w1f[2]; float w2f[16]; float cs1 = 0.f, b1v = 0.f;
  const float b2v = lo(b2_r);
  mlp_stage<0>(L, X, p, xf, w1f, w2f, cs1, b1v, lo(xres_r) + b2v, [&]() {
    make_frags(L, X, w1r, w1f, 2048);
#pragma unroll
    for (int f = 0; f < 16; ++f) w2f[f] = (t & 1) ? hi(w2r[f]) : lo(w2r[f]);
    b1v = lo(b1r);
    const u32x4 ones = {0x3F803F80u, 0x3F803F80u, 0x3F803F80u, 0x3F803F80u};
    f32x4 S = mfma(w1f[0], ones, f32x4{0.f, 0.f, 0.f, 0.f});
    S = mfma(w1f[1], ones, S);
    if (c == 0) {
#pragma unroll
      for (int i = 0; i < 4; ++i) L.cs1p[4 * g + i][w] = S[i];
    }
  });
  TS(11);
  c_gather<0>(L, X, p);
  {
    float wqf[16];
#pragma unroll
    for (int i = 0; i < 2; ++i) {
      const u32x4 q4 = L.wqs[2 * w + i][l];
#pragma unroll
      for (int j = 0; j < 4; ++j) { wqf[8 * i + 2 * j] = lo(q4[j]); wqf[8 * i + 2 * j + 1] = hi(q4[j]); }
    }
    const int r0 = 8 * w + g;
    const float xa = sum8(*(const f32x4*)&L.cbuf[r0][0], *(const f32x4*)&L.cbuf[r0][4]) + L.xs[r0];
    const float xb = sum8(*(const f32x4*)&L.cbuf[r0 + 4][0], *(const f32x4*)&L.cbuf[r0 + 4][4]) + L.xs[r0 + 4];
    float q8[8];
#pragma unroll
    for (int j = 0; j < 8; ++j) q8[j] = xa * wqf[j] + xb * wqf[8 + j];
    float q4[4];
#pragma unroll
    for (int j = 0; j < 4; ++j) q4[j] = tr32(q8[j], q8[j + 4]);
    const f32x2 q2 = {tr16(q4[0], q4[2]), tr16(q4[1], q4[3])};
    *(f32x2*)&L.obuf[w][8 * c + 2 * g] = q2;
    if (c == 0) { L.x2L[r0] = xa; L.x2L[r0 + 4] = xb; }
    BAR();
    if (w == 0) {
      f32x4 q = {0.f, 0.f, 0.f, 0.f};
#pragma unroll
      for (int i = 0; i < 8; ++i) q += *(const f32x4*)&L.obuf[i][4 * (l & 31)];
      if (l < 32) st128(rws, WS_D2 + cu * D2_REC + l * 16, u32x4{san(q[0]), san(q[1]), san(q[2]), san(q[3])});
    } else if (w == 1) {
      const float xv2 = L.x2L[l];
      const float mn = sum64(xv2) * (1.f / 64);
      const float dd = xv2 - mn;
      const float m2 = sum64(dd * dd);
      const unsigned xu = san(xv2);
      u32x4 v = {xu, dppu<0x55>(xu), dppu<0xAA>(xu), dppu<0xFF>(xu)};
      int off = 512 + l * 4;
      if (l == 1) { v = u32x4{san(mn), san(m2), 0u, 0u}; off = 768; }
      if ((l & 3) == 0 || l == 1) st128(rws, WS_D2 + cu * D2_REC + off, v);
    }
    BAR();
  }
  TS(13);
  float xres2;
  {
    __builtin_amdgcn_s_sleep(SLP_D);
    const int rec = WS_D2 + (h * 8 + (l >> 3)) * D2_REC;
    u32x2 q2, st2; u32x4 x4;
    for (unsigned sp_ = 0;; ++sp_) {
      asm volatile("" ::: "memory");
      q2 = ld64<SC1>(rws, rec + (16 * w + 2 * (l & 7)) * 4);
      st2 = ld64<SC1>(rws, rec + 768);
      x4 = ld128<SC1>(rws, WS_D2 + (h * 8 + w) * D2_REC + 512 + c * 16);
      if (__all(um(um(mx2(q2), mx2(st2)), mx4(x4)) != SENT) || sp_ > SPIN_MAX) break;
    }
    TS(14);
    float qa = asf(q2.x), qb = asf(q2.y);
    qa += dpp<ROR8>(qa); qb += dpp<ROR8>(qb);
    qa = tr16(qa, qa); qb = tr16(qb, qb);
    qa = tr32(qa, qa); qb = tr32(qb, qb);
    const float ms = asf(st2.x);
    float mean = ms + dpp<ROR8>(ms); mean = tr16(mean, mean); mean = tr32(mean, mean); mean *= 0.125f;
    const float dm = ms - mean;
    float M2 = asf(st2.y) + 64.f * dm * dm; M2 += dpp<ROR8>(M2); M2 = tr16(M2, M2); M2 = tr32(M2, M2);
    const float rstd = __builtin_amdgcn_rsqf(M2 * (1.f / 512) + 1e-5f) * QS;
    const f32x2 cs = *(const f32x2*)&L.CS[16 * w + 2 * (l & 7)];
    if (l < 8) L.qb[8 * w + l] = pk2((qa - mean * cs[0]) * rstd, (qb - mean * cs[1]) * rstd);
    if (g == 0) *(u32x4*)&L.xfull[64 * w + 4 * c] = x4;
    xres2 = L.x2L[xcol] + b2v;
  }
  BAR();
  TS(15);
  attention<1>(L, X, p, kf, vr, msk, fl, epoch);
  TS(16);
  wo_stage<1>(L, X, p, wof, [&]() {
#pragma unroll
  for (int ci = 0; ci < 2; ++ci) {
    const f32x4 p0 = *(const f32x4*)&L.xfull[64 * w + 32 * ci + 8 * g], p1 = *(const f32x4*)&L.xfull[64 * w + 32 * ci + 8 * g + 4];
    const float xv8[8] = {p0[0], p0[1], p0[2], p0[3], p1[0], p1[1], p1[2], p1[3]};
#pragma unroll
    for (int j = 0; j < 4; ++j) {
      const unsigned hh = pk2(xv8[2 * j], xv8[2 * j + 1]);
      const unsigned ll = pk2(xv8[2 * j] - lo(hh), xv8[2 * j + 1] - hi(hh));
      xf[ci][j] = c == 8 ? hh : c == 9 ? ll : 0u;
    }
  }
  });
  TS(19);
  const bool fin = (xg == 0 && idx >= 24);
  if (u < 0 && !fin) { TSDUMP(); return; }
  mlp_stage<1>(L, X, p, xf, w1f, w2f, cs1, b1v, xres2, []() {});
  TS(21);
  if (!fin) { TSDUMP(); return; }
  c_gather<1>(L, X, p);
  if (w == 0) {
    const float o = sum8(*(const f32x4*)&L.cbuf[l][0], *(const f32x4*)&L.cbuf[l][4]) + L.xs[l];
    p.out[64 * s + l] = (u16)(pk2(o, o) & 0xffffu);
    if (s == 0 && l == 0) *(u64*)p.ws = epoch;
  }
  TS(23);
  TSDUMP();
}

extern "C" void kernel_launch(void* const* d_in, const int* in_sizes, int n_in, void* d_out, int out_size, void* d_ws, size_t ws_size, hipStream_t stream) {
  Params p;
  p.x = (const u16*)d_in[0]; p.K = (const u16*)d_in[1]; p.V = (const u16*)d_in[2]; p.wq = (const u16*)d_in[3]; p.wo = (const u16*)d_in[4];
  p.w1 = (const u16*)d_in[6]; p.b1 = (const u16*)d_in[7]; p.w2 = (const u16*)d_in[8]; p.b2 = (const u16*)d_in[9];
  p.out = (u16*)d_out; p.ws = (unsigned char*)d_ws;
  hipLaunchKernelGGL(fused_kernel, dim3(NCU), dim3(NTHR), 0, stream, p);
}
```

```cpp
#include <hip/hip_runtime.h>
#include <stdint.h>

typedef unsigned short u16;
typedef unsigned long long u64;
typedef unsigned u32x4 __attribute__((ext_vector_type(4)));
typedef unsigned u32x2 __attribute__((ext_vector_type(2)));
typedef float f32x4 __attribute__((ext_vector_type(4)));
typedef float f32x2 __attribute__((ext_vector_type(2)));
typedef short bf16x8 __attribute__((ext_vector_type(8)));
typedef __bf16 bf16x2 __attribute__((ext_vector_type(2)));
typedef __amdgpu_buffer_rsrc_t rsrc_t;

#define KV_AUX 2
#define W_AUX 2
#define SLP_A 4
#define SLP_B 4
#define SLP_C 4
#define SLP_CN 12
#define SLP_D 4

constexpr int DM = 512, NH = 32, SK = 4096, DH = 128, FF = 1376;
constexpr int NCU = 256, NTHR = 512, NU = 86;
constexpr unsigned SENT = 0xFFFFFFFFu;
constexpr unsigned SPIN_MAX = 1u << 17;
constexpr int SC1 = 16;
constexpr float QS = 0.08838834764831845f * 1.4426950408889634f;

constexpr int WS_FLAGS = 4096;
constexpr int WS_D1 = 8192;
constexpr int A_REC = 384, A_SZ = NCU * A_REC;
constexpr int WS_A = WS_D1 + NCU * 1024;
constexpr int B_SZ = 256 * 128;
constexpr int WS_B = WS_A + 2 * A_SZ;
constexpr int C_SZ = NU * 2048;
constexpr int WS_C = WS_B + 2 * B_SZ;
constexpr int D2_REC = 896;
constexpr int WS_D2 = WS_C + 2 * C_SZ;
constexpr int WS_END = WS_D2 + NCU * D2_REC;
constexpr int WS_STAMP = 4 << 20;

struct Params {
  const u16 *x, *K, *V, *wq, *wo, *w1, *b1, *w2, *b2;
  u16* out;
  unsigned char* ws;
};

__device__ __forceinline__ float lo(unsigned w) { return __builtin_bit_cast(float, w << 16); }
__device__ __forceinline__ float hi(unsigned w) { return __builtin_bit_cast(float, w & 0xffff0000u); }
__device__ __forceinline__ float asf(unsigned w) { return __builtin_bit_cast(float, w); }
__device__ __forceinline__ unsigned asu(float f) { return __builtin_bit_cast(unsigned, f); }
__device__ __forceinline__ unsigned pk2(float a, float b) { const bf16x2 v = {(__bf16)a, (__bf16)b}; return __builtin_bit_cast(unsigned, v); }
__device__ __forceinline__ unsigned sanu(unsigned u) { return u == SENT ? 0xFFFFFFFEu : u; }
__device__ __forceinline__ unsigned san(float f) { return sanu(asu(f)); }
template <int CTRL> __device__ __forceinline__ unsigned dppu(unsigned x) { return (unsigned)__builtin_amdgcn_mov_dpp((int)x, CTRL, 0xf, 0xf, true); }
template <int CTRL> __device__ __forceinline__ float dpp(float x) { return asf(dppu<CTRL>(asu(x))); }
constexpr int XOR1 = 0xB1, XOR2 = 0x4E, HMIR = 0x141, MIR = 0x140, ROR8 = 0x128, BC = 0x150, SHL = 0x100;
__device__ __forceinline__ float tr32(float a, float b) {
  auto s = __builtin_amdgcn_permlane32_swap(asu(a), asu(b), false, false);
  return asf(s[0]) + asf(s[1]);
}
__device__ __forceinline__ float tr16(float a, float b) {
  auto s = __builtin_amdgcn_permlane16_swap(asu(a), asu(b), false, false);
  return asf(s[0]) + asf(s[1]);
}
template <int CTRL> __device__ __forceinline__ float trd(float a, float b, bool sel) {
  const float keep = sel ? b : a, send = sel ? a : b;
  return keep + dpp<CTRL>(send);
}
__device__ __forceinline__ float sum16(float x) { x += dpp<XOR1>(x); x += dpp<XOR2>(x); x += dpp<HMIR>(x); x += dpp<MIR>(x); return x; }
__device__ __forceinline__ float sum64(float x) { x = sum16(x); x = tr16(x, x); x = tr32(x, x); return x; }
__device__ __forceinline__ float max16(float x) { x = fmaxf(x, dpp<XOR1>(x)); x = fmaxf(x, dpp<XOR2>(x)); x = fmaxf(x, dpp<HMIR>(x)); x = fmaxf(x, dpp<MIR>(x)); return x; }
__device__ __forceinline__ float ex2(float x) { return __builtin_amdgcn_exp2f(x); }
__device__ __forceinline__ f32x4 mfma(u32x4 a, u32x4 b, f32x4 c) {
  return __builtin_amdgcn_mfma_f32_16x16x32_bf16(__builtin_bit_cast(bf16x8, a), __builtin_bit_cast(bf16x8, b), c, 0, 0, 0);
}
__device__ __forceinline__ float sum8(f32x4 a, f32x4 b) { const f32x4 s = a + b; return (s[0] + s[1]) + (s[2] + s[3]); }

template <int AUX> __device__ __forceinline__ u32x4 ld128(rsrc_t r, int voff, int soff = 0) { return __builtin_amdgcn_raw_buffer_load_b128(r, voff, soff, AUX); }
template <int AUX> __device__ __forceinline__ u32x2 ld64(rsrc_t r, int voff, int soff = 0) { return __builtin_amdgcn_raw_buffer_load_b64(r, voff, soff, AUX); }
template <int AUX> __device__ __forceinline__ unsigned ld32(rsrc_t r, int voff, int soff = 0) { return __builtin_amdgcn_raw_buffer_load_b32(r, voff, soff, AUX); }
template <int AUX> __device__ __forceinline__ unsigned ld16(rsrc_t r, int voff, int soff = 0) { return __builtin_amdgcn_raw_buffer_load_b16(r, voff, soff, AUX); }
__device__ __forceinline__ void st128(rsrc_t r, int voff, u32x4 v) { __builtin_amdgcn_raw_buffer_store_b128(v, r, voff, 0, SC1); }
__device__ __forceinline__ rsrc_t mk(const void* p, unsigned bytes) { return __builtin_amdgcn_make_buffer_rsrc((void*)p, 0, bytes, 0x00020000); }
__device__ __forceinline__ unsigned um(unsigned a, unsigned b) { return a > b ? a : b; }
__device__ __forceinline__ unsigned mx4(u32x4 v) { return um(um(v.x, v.y), um(v.z, v.w)); }
__device__ __forceinline__ unsigned mx2(u32x2 v) { return um(v.x, v.y); }

#define FENCE() do { asm volatile("" ::: "memory"); __builtin_amdgcn_sched_barrier(0); } while (0)
#define BAR() do { asm volatile("s_waitcnt lgkmcnt(0)" ::: "memory"); __builtin_amdgcn_s_barrier(); asm volatile("" ::: "memory"); } while (0)
#define LWAIT() asm volatile("s_waitcnt lgkmcnt(0)" ::: "memory")
#define TS(i) do { } while (0)
#define TSDUMP() do { } while (0)

struct __attribute__((aligned(16))) Lds {
  unsigned char big[8][16384];
  u32x4 wqs[16][64];
  float obuf[8][128];
  unsigned qb[64];
  float CS[128];
  float ml[8][2];
  float ybuf[16][8];
  float sbuf[16][8];
  float s12[2][8];
  float cs1p[16][8];
  float cbuf[64][8];
  float x2L[64];
  float xs[64];
  float xfull[512];
};

struct Ctx { int t, b, w, l, g, c, xg, idx, h, s, cu, u; rsrc_t rws; };

__device__ __forceinline__ void make_frags(Lds& L, const Ctx& X, const u32x4 (&raw)[2], u32x4 (&fr)[2], int base) {
  unsigned char* st = &L.big[X.w][base];
#pragma unroll
  for (int i = 0; i < 2; ++i) *(u32x4*)(st + (32 * i + (X.l >> 1)) * 32 + (X.l & 1) * 16) = raw[i];
  LWAIT();
#pragma unroll
  for (int ci = 0; ci < 2; ++ci)
#pragma unroll
    for (int j2 = 0; j2 < 4; ++j2) {
      const unsigned a = *(const u16*)(st + (32 * ci + 8 * X.g + 2 * j2) * 32 + X.c * 2), bb = *(const u16*)(st + (32 * ci + 8 * X.g + 2 * j2 + 1) * 32 + X.c * 2);
      fr[ci][j2] = a | (bb << 16);
    }
}

template <int LAYER>
__device__ __forceinline__ void attention(Lds& L, const Ctx& X, const Params& p, const u32x4 (&kf)[16], const u32x4 (&vr)[16], const unsigned (&msk)[4], u32x4 fl, u64 epoch) {
  const int t = X.t, b = X.b, w = X.w, l = X.l;
  (void)b;
  u32x4 qf[4];
#pragma unroll
  for (int i = 0; i < 4; ++i) qf[i] = *(const u32x4*)&L.qb[16 * i + 4 * X.g];
  f32x4 sc[4];
#pragma unroll
  for (int T = 0; T < 4; ++T) sc[T] = f32x4{0.f, 0.f, 0.f, 0.f};
#pragma unroll
  for (int i = 0; i < 4; ++i)
#pragma unroll
    for (int T = 0; T < 4; ++T) sc[T] = mfma(qf[i], kf[4 * T + i], sc[T]);
  const float m = max16(fmaxf(fmaxf(sc[0][0], sc[1][0]), fmaxf(sc[2][0], sc[3][0])));
  const float sel = X.g == 0 ? sc[0][0] : X.g == 1 ? sc[1][0] : X.g == 2 ? sc[2][0] : sc[3][0];
  const float pe = ex2(sel - m);
  const unsigned pd = pk2(pe, pe);
  if (LAYER == 0) TS(5);
  f32x4 o0 = {0.f, 0.f, 0.f, 0.f}, o1 = {0.f, 0.f, 0.f, 0.f};
#define PVS(n) { const unsigned pv = dppu<BC + n>(pd); const u32x4 a = {pv & msk[0], pv & msk[1], pv & msk[2], pv & msk[3]}; if (n & 1) o1 = mfma(a, vr[n], o1); else o0 = mfma(a, vr[n], o0); }
  PVS(0) PVS(1) PVS(2) PVS(3) PVS(4) PVS(5) PVS(6) PVS(7) PVS(8) PVS(9) PVS(10) PVS(11) PVS(12) PVS(13) PVS(14) PVS(15)
#undef PVS
  const float lsum = sum64(lo(pd));
  o0 += o1;
  if (l < 32) *(f32x4*)&L.obuf[w][8 * X.c + 4 * X.g] = o0;
  if (l == 0) { L.ml[w][0] = m; L.ml[w][1] = lsum; }
  if (LAYER == 0) {
    const unsigned e0 = (unsigned)epoch, e1 = (unsigned)(epoch >> 32);
    if (w >= 6 && !__all(((fl.x ^ e0) | (fl.y ^ e1) | (fl.z ^ e0) | (fl.w ^ e1)) == 0u)) {
      for (unsigned sp_ = 0; sp_ < SPIN_MAX; ++sp_) {
        __builtin_amdgcn_s_sleep(16);
        asm volatile("" ::: "memory"); fl = ld128<SC1>(X.rws, WS_FLAGS + (t & 127) * 16);
        if (__all(((fl.x ^ e0) | (fl.y ^ e1) | (fl.z ^ e0) | (fl.w ^ e1)) == 0u)) break;
      }
    }
  }
  BAR();
  if (w == 0) {
    const f32x4 a0 = *(const f32x4*)&L.ml[0][0], a1 = *(const f32x4*)&L.ml[2][0], a2 = *(const f32x4*)&L.ml[4][0], a3 = *(const f32x4*)&L.ml[6][0];
    const float mw[8] = {a0[0], a0[2], a1[0], a1[2], a2[0], a2[2], a3[0], a3[2]}, lw[8] = {a0[1], a0[3], a1[1], a1[3], a2[1], a2[3], a3[1], a3[3]};
    const float M = fmaxf(fmaxf(fmaxf(mw[0], mw[1]), fmaxf(mw[2], mw[3])), fmaxf(fmaxf(mw[4], mw[5]), fmaxf(mw[6], mw[7])));
    f32x2 o = {0.f, 0.f}; float Ls = 0.f;
#pragma unroll
    for (int u = 0; u < 8; ++u) { const float f = ex2(mw[u] - M); o += f * *(const f32x2*)&L.obuf[u][2 * l]; Ls += f * lw[u]; }
    const unsigned pk = sanu(pk2(o[0], o[1]));
    u32x4 v = {pk, dppu<0x55>(pk), dppu<0xAA>(pk), dppu<0xFF>(pk)};
    int off = l * 4;
    if (l == 1) { v = u32x4{san(M), san(Ls), 0u, 0u}; off = 256; }
    if ((l & 3) == 0 || l == 1) st128(X.rws, WS_A + LAYER * A_SZ + X.cu * A_REC + off, v);
  }
  BAR();
}

template <int LAYER, typename HOOK>
__device__ __forceinline__ void wo_stage(Lds& L, const Ctx& X, const Params& p, const u32x4 (&wof)[2], HOOK hook) {
  const int t = X.t, b = X.b, w = X.w, l = X.l;
  (void)b; (void)t;
  const int rec = WS_A + LAYER * A_SZ + (X.xg * 32 + (w >> 1) * 8 + (l & 7)) * A_REC;
  const int o0 = rec + ((w & 1) * 64 + 8 * X.g) * 2;
  u32x4 a0, a1; u32x2 mlv;
  __builtin_amdgcn_s_sleep(SLP_A);
  FENCE();
  a0 = ld128<SC1>(X.rws, o0); a1 = ld128<SC1>(X.rws, o0 + 64); mlv = ld64<SC1>(X.rws, rec + 256);
  FENCE();
  hook();
  FENCE();
  if (!__all(um(um(mx4(a0), mx4(a1)), mx2(mlv)) != SENT)) {
    for (unsigned sp_ = 0; sp_ < SPIN_MAX; ++sp_) {
      asm volatile("" ::: "memory");
      a0 = ld128<SC1>(X.rws, o0); a1 = ld128<SC1>(X.rws, o0 + 64); mlv = ld64<SC1>(X.rws, rec + 256);
      if (__all(um(um(mx4(a0), mx4(a1)), mx2(mlv)) != SENT)) break;
    }
  }
  TS(LAYER * 10 + 8);
  f32x4 d = mfma(wof[0], a0, f32x4{0.f, 0.f, 0.f, 0.f});
  d = mfma(wof[1], a1, d);
  const float m = asf(mlv.x), ls = asf(mlv.y);
  float M = fmaxf(m, dpp<XOR1>(m)); M = fmaxf(M, dpp<XOR2>(M)); M = fmaxf(M, dpp<HMIR>(M));
  const float e = ex2(m - M);
  float Lt = e * ls; Lt += dpp<XOR1>(Lt); Lt += dpp<XOR2>(Lt); Lt += dpp<HMIR>(Lt);
  const float f = e * __builtin_amdgcn_rcpf(Lt);
  d *= f;
  float y2[2];
#pragma unroll
  for (int i = 0; i < 2; ++i) y2[i] = trd<HMIR>(d[i], d[i + 2], (l & 4) != 0);
  float y1 = trd<XOR2>(y2[0], y2[1], (l & 2) != 0);
  y1 += dpp<XOR1>(y1);
  if ((l & 9) == 0) L.ybuf[4 * X.g + ((l >> 1) & 3)][w] = y1;
  BAR();
  if (w == 0) {
    const float s = sum8(*(const f32x4*)&L.ybuf[X.c][0], *(const f32x4*)&L.ybuf[X.c][4]);
    const unsigned pk = sanu(pk2(s, dpp<XOR1>(s)));
    const u32x4 v = {pk, dppu<SHL + 2>(pk), dppu<SHL + 4>(pk), dppu<SHL + 6>(pk)};
    if (l == 0 || l == 8) st128(X.rws, WS_B + LAYER * B_SZ + X.cu * 128 + l * 2, v);
  }
  BAR();
}

template <int LAYER, typename HOOK>
__device__ __forceinline__ void mlp_stage(Lds& L, const Ctx& X, const Params& p, const u32x4 (&xf)[2], const u32x4 (&w1f)[2], const float (&w2f)[16], float& cs1, const float& b1v, float xres, HOOK hook) {
  const int t = X.t, b = X.b, w = X.w, l = X.l;
  (void)b;
  const int bb = WS_B + LAYER * B_SZ;
  const int o0 = bb + ((l & 7) * 32 + 4 * w + (X.g >> 1)) * 128 + (X.g & 1) * 16, o2 = bb + ((l >> 3) * 32 + 4 * X.s + ((l & 7) >> 1)) * 128 + (l & 1) * 16;
  u32x4 a0, a1, a2;
  __builtin_amdgcn_s_sleep(SLP_B);
  FENCE();
  a0 = ld128<SC1>(X.rws, o0); a1 = ld128<SC1>(X.rws, o0 + 256); a2 = ld128<SC1>(X.rws, o2);
  FENCE();
  hook();
  FENCE();
  if (!__all(um(um(mx4(a0), mx4(a1)), mx4(a2)) != SENT)) {
    for (unsigned sp_ = 0; sp_ < SPIN_MAX; ++sp_) {
      asm volatile("" ::: "memory");
      a0 = ld128<SC1>(X.rws, o0); a1 = ld128<SC1>(X.rws, o0 + 256); a2 = ld128<SC1>(X.rws, o2);
      if (__all(um(um(mx4(a0), mx4(a1)), mx4(a2)) != SENT)) break;
    }
  }
  TS(LAYER * 10 + 10);
  if (X.u >= 0) {
    const bool src = X.c < 8;
#pragma unroll
    for (int j = 0; j < 4; ++j) { a0[j] = src ? a0[j] : xf[0][j]; a1[j] = src ? a1[j] : xf[1][j]; }
    const u32x4 ones = {0x3F803F80u, 0x3F803F80u, 0x3F803F80u, 0x3F803F80u};
    const f32x4 z = {0.f, 0.f, 0.f, 0.f};
    f32x4 S = mfma(w1f[0], a0, z), s1 = mfma(ones, a0, z), G = mfma(a0, a0, z);
    S = mfma(w1f[1], a1, S); s1 = mfma(ones, a1, s1); G = mfma(a1, a1, G);
    float S2[2];
#pragma unroll
    for (int j = 0; j < 2; ++j) S2[j] = trd<ROR8>(S[j], S[j + 2], (l & 8) != 0);
    float S1 = trd<HMIR>(S2[0], S2[1], (l & 4) != 0);
    S1 += dpp<XOR2>(S1); S1 += dpp<XOR1>(S1);
    const float s1t = sum16(s1[0]);
    const float s2t = sum64((G[0] + G[1]) + (G[2] + G[3]));
    if ((l & 3) == 0) L.sbuf[4 * X.g + ((l >> 3) & 1) * 2 + ((l >> 2) & 1)][w] = S1;
    if (l == 0) { L.s12[0][w] = s1t; L.s12[1][w] = s2t; }
    BAR();
    const float St = sum8(*(const f32x4*)&L.sbuf[X.c][0], *(const f32x4*)&L.sbuf[X.c][4]);
    if (LAYER == 0) cs1 = sum8(*(const f32x4*)&L.cs1p[X.c][0], *(const f32x4*)&L.cs1p[X.c][4]);
    const float mean = sum8(*(const f32x4*)&L.s12[0][0], *(const f32x4*)&L.s12[0][4]) * (1.f / 512);
    const float ms = sum8(*(const f32x4*)&L.s12[1][0], *(const f32x4*)&L.s12[1][4]) * (1.f / 512);
    const float rstd = __builtin_amdgcn_rsqf(fmaxf(ms - mean * mean, 0.f) + 1e-5f);
    const float hp = rstd * (St - mean * cs1) + b1v;
    const float uu = hp * (0.7978845608028654f + 0.7978845608028654f * 0.044715f * hp * hp);
    const float ee = ex2(uu * (2.f * 1.4426950408889634f));
    const float hv = hp - hp * __builtin_amdgcn_rcpf(ee + 1.f);
    float acc0 = 0.f, acc1 = 0.f;
#define DSTEP(F) acc0 = fmaf(dpp<BC + F>(hv), w2f[F], acc0); acc1 = fmaf(dpp<BC + F + 1>(hv), w2f[F + 1], acc1);
    DSTEP(0) DSTEP(2) DSTEP(4) DSTEP(6) DSTEP(8) DSTEP(10) DSTEP(12) DSTEP(14)
#undef DSTEP
    const unsigned av = san(acc0 + acc1);
    const u32x4 v = {av, dppu<0x55>(av), dppu<0xAA>(av), dppu<0xFF>(av)};
    if ((l & 3) == 0) st128(X.rws, WS_C + LAYER * C_SZ + X.u * 2048 + t * 4, v);
    TS(LAYER * 10 + 17);
  }
  if (w == 7) {
    float o[8];
#pragma unroll
    for (int j = 0; j < 4; ++j) { o[2 * j] = lo(a2[j]); o[2 * j + 1] = hi(a2[j]); }
    float o4[4], oo2[2];
#pragma unroll
    for (int j = 0; j < 4; ++j) o4[j] = tr32(o[j], o[j + 4]);
#pragma unroll
    for (int j = 0; j < 2; ++j) oo2[j] = tr16(o4[j], o4[j + 2]);
    const float v = trd<ROR8>(oo2[0], oo2[1], (l & 8) != 0);
    L.xs[8 * (l & 7) + (l >> 3)] = v + xres;
  }
  if (X.u >= 0) __builtin_amdgcn_s_sleep(SLP_C); else __builtin_amdgcn_s_sleep(SLP_CN);
}

template <int LAYER>
__device__ __forceinline__ void c_gather(Lds& L, const Ctx& X, const Params& p) {
  const int t = X.t, b = X.b, w = X.w;
  (void)b; (void)t;
  u32x4 a[3];
  for (unsigned sp_ = 0;; ++sp_) {
    asm volatile("" ::: "memory");
    unsigned mxv = 0;
#pragma unroll
    for (int i = 0; i < 3; ++i) {
      const int src = w * 4 + X.g + 32 * i;
      a[i] = ld128<SC1>(X.rws, src < NU ? WS_C + LAYER * C_SZ + src * 2048 + (64 * X.s + 4 * X.c) * 4 : WS_END);
      mxv = um(mxv, mx4(a[i]));
    }
    if (__all(mxv != SENT) || sp_ > SPIN_MAX) break;
  }
  TS(LAYER * 10 + 12);
  float v[4];
#pragma unroll
  for (int j = 0; j < 4; ++j) v[j] = asf(a[0][j]) + asf(a[1][j]) + asf(a[2][j]);
  const float v0 = tr32(v[0], v[2]), v1 = tr32(v[1], v[3]);
  L.cbuf[4 * X.c + X.g][w] = tr16(v0, v1);
  BAR();
}

__global__ void __launch_bounds__(NTHR, 2) fused_kernel(Params p) {
  unsigned long long tstart;
  asm volatile("s_memrealtime %0\n\ts_waitcnt lgkmcnt(0)" : "=s"(tstart) :: "memory");
  __builtin_amdgcn_sched_barrier(0);
  __shared__ Lds L;
  Ctx X;
  const int t = threadIdx.x, b = blockIdx.x;
  const int w = __builtin_amdgcn_readfirstlane(t >> 6), l = t & 63, g = l >> 4, c = l & 15;
  const int xg = b & 7, idx = b >> 3, h = xg * 4 + (idx >> 3), s = idx & 7, cu = xg * 32 + idx;
  int u = idx < 12 ? 4 * (xg + 8 * (idx >> 2)) + (idx & 3) : -1;
  if (u >= NU) u = -1;
  const rsrc_t rws = mk(p.ws, WS_END);
  X.t = t; X.b = b; X.w = w; X.l = l; X.g = g; X.c = c; X.xg = xg; X.idx = idx; X.h = h; X.s = s; X.cu = cu; X.u = u; X.rws = rws;
  const rsrc_t rK = mk(p.K + (size_t)cu * 65536, 131072), rV = mk(p.V + (size_t)cu * 65536, 131072);
  const rsrc_t rx = mk(p.x, 1024), rwq = mk(p.wq, DM * NH * DH * 2), rwo = mk(p.wo, DM * NH * DH * 2);
  const unsigned mz = u >= 0 ? 1u : 0u;
  const rsrc_t rw1 = mk(p.w1, mz * DM * FF * 2), rw2 = mk(p.w2, mz * DM * FF * 2), rb1 = mk(p.b1, mz * FF * 2), rb2 = mk(p.b2, DM * 2);
  const rsrc_t rd1 = mk(p.ws + WS_D1, w == 0 ? NCU * 1024 : 0);
  unsigned msk[4];
#pragma unroll
  for (int i = 0; i < 4; ++i) msk[i] = (c >> 1) == i ? ((c & 1) ? 0xFFFF0000u : 0x0000FFFFu) : 0u;
  const u32x2 ep = ld64<0>(rws, 0);
  const int xcol = 8 * (l & 7) + (l >> 3);
  const unsigned xres_r = ld16<0>(rx, (64 * s + xcol) * 2), b2_r = ld16<0>(rb2, (64 * s + xcol) * 2);
  u32x4 xf[2];
#pragma unroll
  for (int ci = 0; ci < 2; ++ci) xf[ci] = ld128<0>(rx, c == 8 ? (64 * w + 32 * ci + 8 * g) * 2 : 1024);
  if (w == 0) {
    const u32x4 xv = ld128<0>(rx, l * 16);
    const unsigned xq = ld16<0>(rx, (64 * s + 4 * c + g) * 2);
    u32x4 wqr[16];
#pragma unroll
    for (int n = 0; n < 16; ++n) wqr[n] = ld128<W_AUX>(rwq, (64 * s + g) * 8192 + h * 256 + c * 16, n * 4 * 8192);
    FENCE();
    __builtin_amdgcn_s_barrier();
    FENCE();
    float xs[8];
#pragma unroll
    for (int j = 0; j < 4; ++j) { xs[2 * j] = lo(xv[j]); xs[2 * j + 1] = hi(xv[j]); }
    const float mean = sum64(((xs[0] + xs[1]) + (xs[2] + xs[3])) + ((xs[4] + xs[5]) + (xs[6] + xs[7]))) * (1.f / 512);
    float d2 = 0.f;
#pragma unroll
    for (int j = 0; j < 8; ++j) { const float d = xs[j] - mean; d2 += d * d; }
    const float rstd = __builtin_amdgcn_rsqf(sum64(d2) * (1.f / 512) + 1e-5f);
    const float xn = (lo(xq) - mean) * rstd;
    const unsigned pd = pk2(xn, xn);
    const u32x4 onesA = {0x3F803F80u & msk[0], 0x3F803F80u & msk[1], 0x3F803F80u & msk[2], 0x3F803F80u & msk[3]};
    f32x4 aq = {0.f, 0.f, 0.f, 0.f}, ac = {0.f, 0.f, 0.f, 0.f};
#define QST(n) { const unsigned pv = dppu<BC + n>(pd); const u32x4 a = {pv & msk[0], pv & msk[1], pv & msk[2], pv & msk[3]}; aq = mfma(a, wqr[n], aq); ac = mfma(onesA, wqr[n], ac); L.wqs[n][l] = wqr[n]; }
    QST(0) QST(1) QST(2) QST(3) QST(4) QST(5) QST(6) QST(7) QST(8) QST(9) QST(10) QST(11) QST(12) QST(13) QST(14) QST(15)
#undef QST
    const unsigned tag = ep.x + 1u;
    if (l < 32) {
      const int off = WS_D1 + cu * 1024 + (8 * c + 4 * g) * 8;
      st128(rws, off, u32x4{pk2(aq[0], ac[0]), tag, pk2(aq[1], ac[1]), tag});
      st128(rws, off + 16, u32x4{pk2(aq[2], ac[2]), tag, pk2(aq[3], ac[3]), tag});
    }
    TS(1);
  }
  FENCE();
  if (w == 1) {
#pragma unroll
    for (int k = 0; k < 6; ++k) {
      const int q = l + 64 * k;
      int off = WS_END;
      if (q < 17) off = WS_A + cu * A_REC + q * 16;
      else if (q < 34) off = WS_A + A_SZ + cu * A_REC + (q - 17) * 16;
      else if (q < 36) off = WS_B + cu * 128 + (q - 34) * 16;
      else if (q < 38) off = WS_B + B_SZ + cu * 128 + (q - 36) * 16;
      else if (q >= 64 && q < 113) off = WS_D2 + cu * D2_REC + (q - 64) * 16;
      else if (q >= 128 && u >= 0) off = WS_C + ((q - 128) >> 7) * C_SZ + u * 2048 + ((q - 128) & 127) * 16;
      st128(rws, off, u32x4{SENT, SENT, SENT, SENT});
    }
    FENCE();
    __builtin_amdgcn_s_barrier();
    asm volatile("s_waitcnt vmcnt(0)" ::: "memory");
    const u64 e1 = (((u64)ep.y << 32) | ep.x) + 1;
    if (l == 0) __builtin_amdgcn_raw_buffer_store_b64(u32x2{(unsigned)e1, (unsigned)(e1 >> 32)}, rws, WS_FLAGS + cu * 8, 0, SC1);
    TS(4);
  }
  if (w >= 2) __builtin_amdgcn_s_barrier();
  FENCE();
  u32x4 kf[16], vr[16];
#pragma unroll
  for (int i = 0; i < 16; ++i) kf[i] = ld128<KV_AUX>(rK, w * 16384 + l * 16, i * 1024);
  FENCE();
  u32x4 d1[8];
#pragma unroll
  for (int i = 0; i < 8; ++i) d1[i] = ld128<SC1>(rd1, h * 8192 + l * 16, i * 1024);
  FENCE();
#pragma unroll
  for (int i = 0; i < 16; ++i)  vr[i] = ld128<KV_AUX>(rV, w * 16384 + g * 4096 + c * 16, i * 256);
  FENCE();
  const u64 epoch = (((u64)ep.y << 32) | ep.x) + 1;
  {
    unsigned char* st = L.big[w];
#pragma unroll
    for (int i = 0; i < 16; ++i) { const int key = 4 * i + g; *(u32x4*)(st + key * 256 + ((c ^ (key & 15)) * 16)) = kf[i]; }
    LWAIT();
#pragma unroll
    for (int T = 0; T < 4; ++T)
#pragma unroll
      for (int i = 0; i < 4; ++i) kf[4 * T + i] = *(const u32x4*)(st + (16 * T + c) * 256 + (((4 * i + g) ^ c) * 16));
  }
  TS(2);
  if (w == 0) {
    const unsigned tag = (unsigned)epoch;
    unsigned bad = 0;
#pragma unroll
    for (int i = 0; i < 8; ++i) bad |= (d1[i].y ^ tag) | (d1[i].w ^ tag);
    if (!__all(bad == 0u)) {
      for (unsigned sp_ = 0; sp_ < SPIN_MAX; ++sp_) {
        asm volatile("" ::: "memory");
        bad = 0;
#pragma unroll
        for (int i = 0; i < 8; ++i) { d1[i] = ld128<SC1>(rd1, h * 8192 + l * 16, i * 1024); bad |= (d1[i].y ^ tag) | (d1[i].w ^ tag); }
        if (__all(bad == 0u)) break;
      }
    }
    float qa = 0.f, qc = 0.f, ca = 0.f, cb = 0.f;
#pragma unroll
    for (int i = 0; i < 8; ++i) { qa += lo(d1[i].x); ca += hi(d1[i].x); qc += lo(d1[i].z); cb += hi(d1[i].z); }
    L.qb[l] = pk2(qa * QS, qc * QS);
    *(f32x2*)&L.CS[2 * l] = f32x2{ca, cb};
  }
  BAR();
  TS(3);
  u32x4 wor[2];
#pragma unroll
  for (int i = 0; i < 2; ++i) wor[i] = ld128<W_AUX>(rwo, (xg * 512 + 64 * w + 32 * i + (l >> 1)) * 1024 + idx * 32 + (l & 1) * 16);
  const rsrc_t rfl = mk(p.ws + WS_FLAGS, w >= 6 ? 2048 : 0);
  const u32x4 fl = ld128<SC1>(rfl, (t & 127) * 16);
  FENCE();

  attention<0>(L, X, p, kf, vr, msk, fl, epoch);
  TS(6);
  u32x4 wof[2];
  u32x4 w1r[2]; unsigned w2r[16]; unsigned b1r = 0;
  const int uc = u >= 0 ? u : 0;
  wo_stage<0>(L, X, p, wof, [&]() {
#pragma unroll
    for (int i = 0; i < 2; ++i) w1r[i] = ld128<W_AUX>(rw1, (64 * w + 32 * i + (l >> 1)) * (FF * 2) + uc * 32 + (l & 1) * 16);
#pragma unroll
    for (int f = 0; f < 16; ++f) w2r[f] = ld32<W_AUX>(rw2, (16 * uc + f) * 1024 + (t >> 1) * 4);
    b1r = ld16<0>(rb1, (16 * uc + c) * 2);
    FENCE();
    make_frags(L, X, wor, wof, 0);
  });
  TS(9);
  u32x4 w1f[2]; float w2f[16]; float cs1 = 0.f, b1v = 0.f;
  const float b2v = lo(b2_r);
  mlp_stage<0>(L, X, p, xf, w1f, w2f, cs1, b1v, lo(xres_r) + b2v, [&]() {
    make_frags(L, X, w1r, w1f, 2048);
#pragma unroll
    for (int f = 0; f < 16; ++f) w2f[f] = (t & 1) ? hi(w2r[f]) : lo(w2r[f]);
    b1v = lo(b1r);
    const u32x4 ones = {0x3F803F80u, 0x3F803F80u, 0x3F803F80u, 0x3F803F80u};
    f32x4 S = mfma(w1f[0], ones, f32x4{0.f, 0.f, 0.f, 0.f});
    S = mfma(w1f[1], ones, S);
    if (c == 0) {
#pragma unroll
      for (int i = 0; i < 4; ++i) L.cs1p[4 * g + i][w] = S[i];
    }
  });
  TS(11);
  c_gather<0>(L, X, p);
  {
    float wqf[16];
#pragma unroll
    for (int i = 0; i < 2; ++i) {
      const u32x4 q4 = L.wqs[2 * w + i][l];
#pragma unroll
      for (int j = 0; j < 4; ++j) { wqf[8 * i + 2 * j] = lo(q4[j]); wqf[8 * i + 2 * j + 1] = hi(q4[j]); }
    }
    const int r0 = 8 * w + g;
    const float xa = sum8(*(const f32x4*)&L.cbuf[r0][0], *(const f32x4*)&L.cbuf[r0][4]) + L.xs[r0];
    const float xb = sum8(*(const f32x4*)&L.cbuf[r0 + 4][0], *(const f32x4*)&L.cbuf[r0 + 4][4]) + L.xs[r0 + 4];
    float q8[8];
#pragma unroll
    for (int j = 0; j < 8; ++j) q8[j] = xa * wqf[j] + xb * wqf[8 + j];
    float q4[4];
#pragma unroll
    for (int j = 0; j < 4; ++j) q4[j] = tr32(q8[j], q8[j + 4]);
    const f32x2 q2 = {tr16(q4[0], q4[2]), tr16(q4[1], q4[3])};
    *(f32x2*)&L.obuf[w][8 * c + 2 * g] = q2;
    if (c == 0) { L.x2L[r0] = xa; L.x2L[r0 + 4] = xb; }
    BAR();
    if (w == 0) {
      f32x4 q = {0.f, 0.f, 0.f, 0.f};
#pragma unroll
      for (int i = 0; i < 8; ++i) q += *(const f32x4*)&L.obuf[i][4 * (l & 31)];
      if (l < 32) st128(rws, WS_D2 + cu * D2_REC + l * 16, u32x4{san(q[0]), san(q[1]), san(q[2]), san(q[3])});
    } else if (w == 1) {
      const float xv2 = L.x2L[l];
      const float mn = sum64(xv2) * (1.f / 64);
      const float dd = xv2 - mn;
      const float m2 = sum64(dd * dd);
      const unsigned xu = san(xv2);
      u32x4 v = {xu, dppu<0x55>(xu), dppu<0xAA>(xu), dppu<0xFF>(xu)};
      int off = 512 + l * 4;
      if (l == 1) { v = u32x4{san(mn), san(m2), 0u, 0u}; off = 768; }
      if ((l & 3) == 0 || l == 1) st128(rws, WS_D2 + cu * D2_REC + off, v);
    }
    BAR();
  }
  TS(13);
  float xres2;
  {
    __builtin_amdgcn_s_sleep(SLP_D);
    const int rec = WS_D2 + (h * 8 + (l >> 3)) * D2_REC;
    u32x2 q2, st2; u32x4 x4;
    for (unsigned sp_ = 0;; ++sp_) {
      asm volatile("" ::: "memory");
      q2 = ld64<SC1>(rws, rec + (16 * w + 2 * (l & 7)) * 4);
      st2 = ld64<SC1>(rws, rec + 768);
      x4 = ld128<SC1>(rws, WS_D2 + (h * 8 + w) * D2_REC + 512 + c * 16);
      if (__all(um(um(mx2(q2), mx2(st2)), mx4(x4)) != SENT) || sp_ > SPIN_MAX) break;
    }
    TS(14);
    float qa = asf(q2.x), qb = asf(q2.y);
    qa += dpp<ROR8>(qa); qb += dpp<ROR8>(qb);
    qa = tr16(qa, qa); qb = tr16(qb, qb);
    qa = tr32(qa, qa); qb = tr32(qb, qb);
    const float ms = asf(st2.x);
    float mean = ms + dpp<ROR8>(ms); mean = tr16(mean, mean); mean = tr32(mean, mean); mean *= 0.125f;
    const float dm = ms - mean;
    float M2 = asf(st2.y) + 64.f * dm * dm; M2 += dpp<ROR8>(M2); M2 = tr16(M2, M2); M2 = tr32(M2, M2);
    const float rstd = __builtin_amdgcn_rsqf(M2 * (1.f / 512) + 1e-5f) * QS;
    const f32x2 cs = *(const f32x2*)&L.CS[16 * w + 2 * (l & 7)];
    if (l < 8) L.qb[8 * w + l] = pk2((qa - mean * cs[0]) * rstd, (qb - mean * cs[1]) * rstd);
    if (g == 0) *(u32x4*)&L.xfull[64 * w + 4 * c] = x4;
    xres2 = L.x2L[xcol] + b2v;
  }
  BAR();
  TS(15);
  attention<1>(L, X, p, kf, vr, msk, fl, epoch);
  TS(16);
  wo_stage<1>(L, X, p, wof, [&]() {
#pragma unroll
  for (int ci = 0; ci < 2; ++ci) {
    const f32x4 p0 = *(const f32x4*)&L.xfull[64 * w + 32 * ci + 8 * g], p1 = *(const f32x4*)&L.xfull[64 * w + 32 * ci + 8 * g + 4];
    const float xv8[8] = {p0[0], p0[1], p0[2], p0[3], p1[0], p1[1], p1[2], p1[3]};
#pragma unroll
    for (int j = 0; j < 4; ++j) {
      const unsigned hh = pk2(xv8[2 * j], xv8[2 * j + 1]);
      const unsigned ll = pk2(xv8[2 * j] - lo(hh), xv8[2 * j + 1] - hi(hh));
      xf[ci][j] = c == 8 ? hh : c == 9 ? ll : 0u;
    }
  }
  });
  TS(19);
  const bool fin = (xg == 0 && idx >= 24);
  if (u < 0 && !fin) { TSDUMP(); return; }
  mlp_stage<1>(L, X, p, xf, w1f, w2f, cs1, b1v, xres2, []() {});
  TS(21);
  if (b == 0) { while (__builtin_amdgcn_s_memrealtime() - tstart < 6000ull) __builtin_amdgcn_s_sleep(1); }
  if (!fin) { TSDUMP(); return; }
  c_gather<1>(L, X, p);
  if (w == 0) {
    const float o = sum8(*(const f32x4*)&L.cbuf[l][0], *(const f32x4*)&L.cbuf[l][4]) + L.xs[l];
    p.out[64 * s + l] = (u16)(pk2(o, o) & 0xffffu);
    if (s == 0 && l == 0) *(u64*)p.ws = epoch;
  }
  TS(23);
  TSDUMP();
}

extern "C" void kernel_launch(void* const* d_in, const int* in_sizes, int n_in, void* d_out, int out_size, void* d_ws, size_t ws_size, hipStream_t stream) {
  Params p;
  p.x = (const u16*)d_in[0]; p.K = (const u16*)d_in[1]; p.V = (const u16*)d_in[2]; p.wq = (const u16*)d_in[3]; p.wo = (const u16*)d_in[4];
  p.w1 = (const u16*)d_in[6]; p.b1 = (const u16*)d_in[7]; p.w2 = (const u16*)d_in[8]; p.b2 = (const u16*)d_in[9];
  p.out = (u16*)d_out; p.ws = (unsigned char*)d_ws;
  hipLaunchKernelGGL(fused_kernel, dim3(NCU), dim3(NTHR), 0, stream, p);
}
```

```cpp
#include <hip/hip_runtime.h>
#include <stdint.h>

typedef unsigned short u16;
typedef unsigned long long u64;
typedef unsigned u32x4 __attribute__((ext_vector_type(4)));
typedef unsigned u32x2 __attribute__((ext_vector_type(2)));
typedef float f32x4 __attribute__((ext_vector_type(4)));
typedef float f32x2 __attribute__((ext_vector_type(2)));
typedef short bf16x8 __attribute__((ext_vector_type(8)));
typedef __bf16 bf16x2 __attribute__((ext_vector_type(2)));
typedef __amdgpu_buffer_rsrc_t rsrc_t;

#define KV_AUX 0
#define W_AUX 2
#define SLP_A 4
#define SLP_B 4
#define SLP_C 4
#define SLP_CN 10
#define SLP_D 4

constexpr int DM = 512, NH = 32, SK = 4096, DH = 128, FF = 1376;
constexpr int NCU = 256, NTHR = 512, NU = 86;
constexpr unsigned SENT = 0xFFFFFFFFu;
constexpr unsigned SPIN_MAX = 1u << 17;
constexpr int SC1 = 16;
constexpr float QS = 0.08838834764831845f * 1.4426950408889634f;
constexpr unsigned ONE2 = 0x3F803F80u;
constexpr int OOB = 0x40000000;

constexpr int WS_FLAGS = 4096;
constexpr int WS_D1 = 8192;
constexpr int A_REC = 384, A_SZ = NCU * A_REC;
constexpr int WS_A = WS_D1 + NCU * 1024;
constexpr int B_SZ = NCU * 128;
constexpr int WS_B = WS_A + 2 * A_SZ;
constexpr int C_SZ = NU * 2048;
constexpr int WS_C = WS_B + 2 * B_SZ;
constexpr int D2_REC = 1408;
constexpr int WS_D2 = WS_C + 2 * C_SZ;
constexpr int WS_END = WS_D2 + NCU * D2_REC;
constexpr int WS_STAMP = 4 << 20;

struct Params {
  const u16 *x, *K, *V, *wq, *wo, *w1, *b1, *w2, *b2;
  u16* out;
  unsigned char* ws;
};

__device__ __forceinline__ float lo(unsigned w) { return __builtin_bit_cast(float, w << 16); }
__device__ __forceinline__ float hi(unsigned w) { return __builtin_bit_cast(float, w & 0xffff0000u); }
__device__ __forceinline__ float asf(unsigned w) { return __builtin_bit_cast(float, w); }
__device__ __forceinline__ unsigned asu(float f) { return __builtin_bit_cast(unsigned, f); }
__device__ __forceinline__ unsigned pk2(float a, float b) { const bf16x2 v = {(__bf16)a, (__bf16)b}; return __builtin_bit_cast(unsigned, v); }
__device__ __forceinline__ unsigned sanu(unsigned u) { return u < 0xFFFFFFFEu ? u : 0xFFFFFFFEu; }
__device__ __forceinline__ unsigned san(float f) { return sanu(asu(f)); }
template <int CTRL> __device__ __forceinline__ unsigned dppu(unsigned x) { return (unsigned)__builtin_amdgcn_mov_dpp((int)x, CTRL, 0xf, 0xf, true); }
template <int CTRL> __device__ __forceinline__ float dpp(float x) { return asf(dppu<CTRL>(asu(x))); }
constexpr int XOR1 = 0xB1, XOR2 = 0x4E, HMIR = 0x141, MIR = 0x140, ROR8 = 0x128, BC = 0x150, SHL = 0x100;
__device__ __forceinline__ float tr32(float a, float b) {
  auto s = __builtin_amdgcn_permlane32_swap(asu(a), asu(b), false, false);
  return asf(s[0]) + asf(s[1]);
}
__device__ __forceinline__ float tr16(float a, float b) {
  auto s = __builtin_amdgcn_permlane16_swap(asu(a), asu(b), false, false);
  return asf(s[0]) + asf(s[1]);
}
template <int CTRL> __device__ __forceinline__ float trd(float a, float b, bool sel) {
  const float keep = sel ? b : a, send = sel ? a : b;
  return keep + dpp<CTRL>(send);
}
__device__ __forceinline__ float sum8l(float x) { x += dpp<XOR1>(x); x += dpp<XOR2>(x); x += dpp<HMIR>(x); return x; }
__device__ __forceinline__ float sum16(float x) { x = sum8l(x); x += dpp<MIR>(x); return x; }
__device__ __forceinline__ float sum64(float x) { x = sum16(x); x = tr16(x, x); x = tr32(x, x); return x; }
__device__ __forceinline__ float fmx(float a, float b) { float r; asm("v_max_f32 %0, %1, %2" : "=v"(r) : "v"(a), "v"(b)); return r; }
__device__ __forceinline__ float fmx3(float a, float b, float c) { float r; asm("v_max3_f32 %0, %1, %2, %3" : "=v"(r) : "v"(a), "v"(b), "v"(c)); return r; }
#define MD(ctl) "s_nop 1\n\tv_max_f32_dpp %0, %0, %0 " ctl " row_mask:0xf bank_mask:0xf\n\t"
__device__ __forceinline__ float max8l(float x) { asm(MD("quad_perm:[1,0,3,2]") MD("quad_perm:[2,3,0,1]") MD("row_half_mirror") : "+v"(x)); return x; }
__device__ __forceinline__ float max16(float x) { asm(MD("quad_perm:[1,0,3,2]") MD("quad_perm:[2,3,0,1]") MD("row_half_mirror") MD("row_mirror") : "+v"(x)); return x; }
__device__ __forceinline__ float mx16r(float a) { auto s = __builtin_amdgcn_permlane16_swap(asu(a), asu(a), false, false); return fmx(asf(s[0]), asf(s[1])); }
__device__ __forceinline__ float mx32r(float a) { auto s = __builtin_amdgcn_permlane32_swap(asu(a), asu(a), false, false); return fmx(asf(s[0]), asf(s[1])); }
__device__ __forceinline__ float dot2(unsigned a, unsigned b, float c) {
  return __builtin_amdgcn_fdot2_f32_bf16(__builtin_bit_cast(bf16x2, a), __builtin_bit_cast(bf16x2, b), c, false);
}
__device__ __forceinline__ float ex2(float x) { return __builtin_amdgcn_exp2f(x); }
__device__ __forceinline__ f32x4 mfma(u32x4 a, u32x4 b, f32x4 c) {
  return __builtin_amdgcn_mfma_f32_16x16x32_bf16(__builtin_bit_cast(bf16x8, a), __builtin_bit_cast(bf16x8, b), c, 0, 0, 0);
}
__device__ __forceinline__ float sum4(f32x4 s) { return (s[0] + s[1]) + (s[2] + s[3]); }
__device__ __forceinline__ float sum8(f32x4 a, f32x4 b) { return sum4(a + b); }
__device__ __forceinline__ float sel4(int g, float a, float b, float c, float d) { const float x = (g & 1) ? b : a, y = (g & 1) ? d : c; return (g & 2) ? y : x; }

template <int AUX> __device__ __forceinline__ u32x4 ld128(rsrc_t r, int voff, int soff = 0) { return __builtin_amdgcn_raw_buffer_load_b128(r, voff, soff, AUX); }
template <int AUX> __device__ __forceinline__ u32x2 ld64(rsrc_t r, int voff, int soff = 0) { return __builtin_amdgcn_raw_buffer_load_b64(r, voff, soff, AUX); }
template <int AUX> __device__ __forceinline__ unsigned ld32(rsrc_t r, int voff, int soff = 0) { return __builtin_amdgcn_raw_buffer_load_b32(r, voff, soff, AUX); }
template <int AUX> __device__ __forceinline__ unsigned ld16(rsrc_t r, int voff, int soff = 0) { return __builtin_amdgcn_raw_buffer_load_b16(r, voff, soff, AUX); }
__device__ __forceinline__ void st128(rsrc_t r, int voff, int soff, u32x4 v) { __builtin_amdgcn_raw_buffer_store_b128(v, r, voff, soff, SC1); }
__device__ __forceinline__ rsrc_t mk(const void* p, unsigned bytes) { return __builtin_amdgcn_make_buffer_rsrc((void*)p, 0, bytes, 0x00020000); }
__device__ __forceinline__ unsigned um(unsigned a, unsigned b) { return a > b ? a : b; }
__device__ __forceinline__ unsigned mx4(u32x4 v) { return um(um(v.x, v.y), um(v.z, v.w)); }
__device__ __forceinline__ unsigned mx2(u32x2 v) { return um(v.x, v.y); }
template <typename T> __device__ __forceinline__ void pin(T& x) { asm volatile("" : "+v"(x)); }

#define LD4(dst, off, imm) "buffer_load_dwordx4 %[" dst "], %[" off "], %[rs], %[so] offen offset:" imm " sc1\n\t"
#define LD2(dst, off, imm) "buffer_load_dwordx2 %[" dst "], %[" off "], %[rs], %[so] offen offset:" imm " sc1\n\t"
__device__ __forceinline__ void gather_442(u32x4& a, u32x4& b, u32x2& c, int oa, int oc, rsrc_t rs, int so, u64 m) {
  asm volatile("s_mov_b64 exec, %[m]\n\t" LD4("a", "oa", "0") LD4("b", "oa", "64") LD2("c", "oc", "0") "s_mov_b64 exec, -1"
               : [a] "+v"(a), [b] "+v"(b), [c] "+v"(c) : [oa] "v"(oa), [oc] "v"(oc), [rs] "s"(rs), [so] "s"(so), [m] "s"(m) : "memory");
}
__device__ __forceinline__ void gather_44(u32x4& a, u32x4& b, int oa, rsrc_t rs, int so, u64 m) {
  asm volatile("s_mov_b64 exec, %[m]\n\t" LD4("a", "oa", "0") LD4("b", "oa", "256") "s_mov_b64 exec, -1"
               : [a] "+v"(a), [b] "+v"(b) : [oa] "v"(oa), [rs] "s"(rs), [so] "s"(so), [m] "s"(m) : "memory");
}
__device__ __forceinline__ void gather_4(u32x4& a, int oa, rsrc_t rs, int so, u64 m) {
  asm volatile("s_mov_b64 exec, %[m]\n\t" LD4("a", "oa", "0") "s_mov_b64 exec, -1"
               : [a] "+v"(a) : [oa] "v"(oa), [rs] "s"(rs), [so] "s"(so), [m] "s"(m) : "memory");
}
__device__ __forceinline__ void gather_444(u32x4& a, u32x4& b, u32x4& c, int oa, int ob, int oc, rsrc_t rs, int so, u64 m) {
  asm volatile("s_mov_b64 exec, %[m]\n\t" LD4("a", "oa", "0") LD4("b", "ob", "0") LD4("c", "oc", "0") "s_mov_b64 exec, -1"
               : [a] "+v"(a), [b] "+v"(b), [c] "+v"(c) : [oa] "v"(oa), [ob] "v"(ob), [oc] "v"(oc), [rs] "s"(rs), [so] "s"(so), [m] "s"(m) : "memory");
}
__device__ __forceinline__ u64 umask(bool on, u64 m) { const unsigned f = __builtin_amdgcn_readfirstlane(on ? ~0u : 0u); return m & (((u64)f << 32) | f); }
#define VMWAIT(N, ...) asm volatile("s_waitcnt vmcnt(" #N ")" : __VA_ARGS__ :: "memory")
#define UNLIKELY(x) __builtin_expect(!!(x), 0)

#define FENCE() do { asm volatile("" ::: "memory"); __builtin_amdgcn_sched_barrier(0); } while (0)
#define BAR() do { asm volatile("s_waitcnt lgkmcnt(0)" ::: "memory"); __builtin_amdgcn_s_barrier(); asm volatile("" ::: "memory"); } while (0)
#define LWAIT() asm volatile("s_waitcnt lgkmcnt(0)" ::: "memory")
#define TS(i) do { } while (0)
#define TSDUMP() do { } while (0)

struct __attribute__((aligned(16))) Lds {
  union {
    float obuf[8][128];
    struct { float cbuf[64][8]; u16 xhl[8][2][64]; };
  };
  float ml[8][2];
  unsigned qb[64];
  unsigned csb[64];
  float ybuf[16][8];
  float sbuf[16][8];
  float s12[2][8];
  float cs1p[16][8];
  float xs[64];
  float xfull[512];
  unsigned qrec[8][72];
  float st2[8][4];
  unsigned qflag;
  unsigned pad_[3];
  u32x4 wqs[16][64];
  unsigned char big[8][16384];
};
#define LP(T, field, off) (*(T*)((unsigned char*)&L.field + (off)))

struct Ctx {
  int t, b, w, l, g, c, xg, idx, h, s, cu, u;
  rsrc_t rws;
  int aA, aAml, aApub, aB0, aB2, aBpub, aC0, aC1, aC2, aCpub, aDg, aDq, aDx;
  int o_ob, o_l8, o_ml, o_yw, o_c32, o_sw, o_cw, o_l32, o_l4, o_xw, o_xr, o_qr, o_st, o_dd, o_xs;
};
constexpr u64 M_C8 = 0x00FF00FF00FF00FFull;

__device__ __forceinline__ void make_frags(Lds& L, const Ctx& X, const u32x4 (&raw)[2], u32x4 (&fr)[2], int base) {
  unsigned char* st = &L.big[X.w][base];
#pragma unroll
  for (int i = 0; i < 2; ++i) *(u32x4*)(st + (32 * i + (X.l >> 1)) * 32 + (X.l & 1) * 16) = raw[i];
  LWAIT();
#pragma unroll
  for (int ci = 0; ci < 2; ++ci)
#pragma unroll
    for (int j2 = 0; j2 < 4; ++j2) {
      const unsigned a = *(const u16*)(st + (32 * ci + 8 * X.g + 2 * j2) * 32 + X.c * 2), bb = *(const u16*)(st + (32 * ci + 8 * X.g + 2 * j2 + 1) * 32 + X.c * 2);
      fr[ci][j2] = a | (bb << 16);
    }
  pin(fr[0]); pin(fr[1]);
}

template <int LAYER>
__device__ __forceinline__ void pv_publish(Lds& L, const Ctx& X, const Params& p, float tsel, float tmax, float scale, const u32x4 (&vr)[16], const unsigned (&msk)[4], u32x4 fl, u64 epoch) {
  const int t = X.t, b = X.b, w = X.w, l = X.l;
  (void)b; (void)t;
  const float pe = ex2((tsel - tmax) * scale);
  const unsigned pd = pk2(pe, pe);
  if (LAYER == 0) TS(5);
  f32x4 o0 = {0.f, 0.f, 0.f, 0.f}, o1 = {0.f, 0.f, 0.f, 0.f};
#define PVS(n) { const unsigned pv = dppu<BC + n>(pd); const u32x4 a = {pv & msk[0], pv & msk[1], pv & msk[2], pv & msk[3]}; if (n & 1) o1 = mfma(a, vr[n], o1); else o0 = mfma(a, vr[n], o0); }
  PVS(0) PVS(1) PVS(2) PVS(3) PVS(4) PVS(5) PVS(6) PVS(7) PVS(8) PVS(9) PVS(10) PVS(11) PVS(12) PVS(13) PVS(14) PVS(15)
#undef PVS
  const float lsum = sum64(lo(pd));
  o0 += o1;
  if (l < 32) LP(f32x4, obuf, X.o_ob) = o0;
  if (l == 0) *(f32x2*)&L.ml[w][0] = f32x2{tmax * scale, lsum};
  if (LAYER == 0) {
    const unsigned e0 = (unsigned)epoch, e1 = (unsigned)(epoch >> 32);
    if (UNLIKELY(w >= 6 && !__all(((fl.x ^ e0) | (fl.y ^ e1) | (fl.z ^ e0) | (fl.w ^ e1)) == 0u))) {
      for (unsigned sp_ = 0; sp_ < SPIN_MAX; ++sp_) {
        __builtin_amdgcn_s_sleep(16);
        asm volatile("" ::: "memory"); fl = ld128<SC1>(X.rws, WS_FLAGS + (t & 127) * 16);
        if (__all(((fl.x ^ e0) | (fl.y ^ e1) | (fl.z ^ e0) | (fl.w ^ e1)) == 0u)) break;
      }
    }
  }
  BAR();
  if (w == 0) {
    const f32x2 mlu = LP(const f32x2, ml, X.o_ml);
    f32x2 ob[8];
#pragma unroll
    for (int u = 0; u < 8; ++u) ob[u] = LP(const f32x2, obuf[u], X.o_l8);
    const float M = max8l(mlu[0]);
    const float f = ex2(mlu[0] - M);
    const float Ls = sum8l(f * mlu[1]);
    f32x2 o = dpp<BC + 0>(f) * ob[0];
    o += dpp<BC + 1>(f) * ob[1]; o += dpp<BC + 2>(f) * ob[2]; o += dpp<BC + 3>(f) * ob[3];
    o += dpp<BC + 4>(f) * ob[4]; o += dpp<BC + 5>(f) * ob[5]; o += dpp<BC + 6>(f) * ob[6]; o += dpp<BC + 7>(f) * ob[7];
    const unsigned pk = sanu(pk2(o[0], o[1]));
    u32x4 v = {pk, dppu<0x55>(pk), dppu<0xAA>(pk), dppu<0xFF>(pk)};
    if (l == 1) v = u32x4{san(M), san(Ls), 0u, 0u};
    st128(X.rws, X.aApub, LAYER * A_SZ, v);
  }
}

template <int LAYER, int NYOUNG, typename HOOK>
__device__ __forceinline__ void wo_stage(Lds& L, const Ctx& X, const Params& p, const u32x4 (&wof)[2], HOOK hook) {
  const int t = X.t, b = X.b, w = X.w, l = X.l;
  (void)b; (void)t;
  const unsigned ini = X.c < 8 ? SENT : 0u;
  u32x4 a0 = {ini, ini, ini, ini}, a1 = a0; u32x2 mlv = {ini, ini};
  pin(a0); pin(a1); pin(mlv);
  BAR();
  __builtin_amdgcn_s_sleep(SLP_A);
  gather_442(a0, a1, mlv, X.aA, X.aAml, X.rws, LAYER * A_SZ, M_C8);
  FENCE();
  hook();
  FENCE();
  if (NYOUNG == 0) VMWAIT(0, "+v"(a0), "+v"(a1), "+v"(mlv)); else VMWAIT(23, "+v"(a0), "+v"(a1), "+v"(mlv));
  {
    bool bad = um(um(mx4(a0), mx4(a1)), mx2(mlv)) == SENT;
    if (UNLIKELY(__any(bad))) {
      for (unsigned sp_ = 0; sp_ < SPIN_MAX; ++sp_) {
        gather_442(a0, a1, mlv, X.aA, X.aAml, X.rws, LAYER * A_SZ, __ballot(bad));
        VMWAIT(0, "+v"(a0), "+v"(a1), "+v"(mlv));
        bad = um(um(mx4(a0), mx4(a1)), mx2(mlv)) == SENT;
        if (!__any(bad)) break;
      }
    }
  }
  TS(LAYER * 10 + 8);
  f32x4 d = mfma(wof[0], a0, f32x4{0.f, 0.f, 0.f, 0.f});
  d = mfma(wof[1], a1, d);
  const float m = asf(mlv.x), ls = asf(mlv.y);
  const float M = max8l(m);
  const float e = ex2(m - M);
  const float f = X.c < 8 ? e * __builtin_amdgcn_rcpf(sum8l(e * ls)) : 0.f;
  d *= f;
  float y2[2];
#pragma unroll
  for (int i = 0; i < 2; ++i) y2[i] = trd<HMIR>(d[i], d[i + 2], (l & 4) != 0);
  float y1 = trd<XOR2>(y2[0], y2[1], (l & 2) != 0);
  y1 += dpp<XOR1>(y1);
  if ((l & 9) == 0) LP(float, ybuf, X.o_yw) = y1;
  BAR();
  if (w == 0) {
    const float s = sum8(LP(const f32x4, ybuf, X.o_c32), LP(const f32x4, ybuf[0][4], X.o_c32));
    const unsigned pk = sanu(pk2(s, dpp<XOR1>(s)));
    const u32x4 v = {pk, dppu<SHL + 2>(pk), dppu<SHL + 4>(pk), dppu<SHL + 6>(pk)};
    st128(X.rws, X.aBpub, LAYER * B_SZ, v);
  }
}

template <int LAYER, typename HOOK>
__device__ __forceinline__ void mlp_stage(Lds& L, const Ctx& X, const Params& p, const u32x4 (&xf)[2], const u32x4 (&w1f)[2], const unsigned (&w2p)[8], float& cs1, const float& b1v, const float& xres, HOOK hook) {
  const int t = X.t, b = X.b, w = X.w, l = X.l;
  (void)b; (void)t;
  u32x4 a0 = {0u, 0u, 0u, 0u}, a1 = a0, a2 = a0;
  pin(a0); pin(a1); pin(a2);
  const bool mlp = X.u >= 0;
  const u64 m01 = umask(mlp, M_C8), m2 = umask(w == 7, ~0ull);
  BAR();
  __builtin_amdgcn_s_sleep(SLP_B);
  gather_44(a0, a1, X.aB0, X.rws, LAYER * B_SZ, m01);
  gather_4(a2, X.aB2, X.rws, LAYER * B_SZ, m2);
  FENCE();
  hook();
  FENCE();
  VMWAIT(0, "+v"(a0), "+v"(a1), "+v"(a2));
  {
    bool bad = um(um(mx4(a0), mx4(a1)), mx4(a2)) == SENT;
    if (UNLIKELY(__any(bad))) {
      for (unsigned sp_ = 0; sp_ < SPIN_MAX; ++sp_) {
        const u64 bm = __ballot(bad);
        gather_44(a0, a1, X.aB0, X.rws, LAYER * B_SZ, bm & m01);
        gather_4(a2, X.aB2, X.rws, LAYER * B_SZ, bm & m2);
        VMWAIT(0, "+v"(a0), "+v"(a1), "+v"(a2));
        bad = um(um(mx4(a0), mx4(a1)), mx4(a2)) == SENT;
        if (!__any(bad)) break;
      }
    }
  }
  TS(LAYER * 10 + 10);
  if (mlp) {
    const bool src = X.c < 8;
#pragma unroll
    for (int j = 0; j < 4; ++j) { a0[j] = src ? a0[j] : xf[0][j]; a1[j] = src ? a1[j] : xf[1][j]; }
    const u32x4 ones = {ONE2, ONE2, ONE2, ONE2};
    const f32x4 z = {0.f, 0.f, 0.f, 0.f};
    f32x4 S = mfma(w1f[0], a0, z), G = mfma(a0, a0, z), s1 = mfma(ones, a0, z);
    S = mfma(w1f[1], a1, S); G = mfma(a1, a1, G); s1 = mfma(ones, a1, s1);
    float S2[2];
#pragma unroll
    for (int j = 0; j < 2; ++j) S2[j] = trd<ROR8>(S[j], S[j + 2], (l & 8) != 0);
    float S1 = trd<HMIR>(S2[0], S2[1], (l & 4) != 0);
    S1 += dpp<XOR2>(S1); S1 += dpp<XOR1>(S1);
    const float s2t = sum64(sum4(G));
    const float s1t = sum16(s1[0]);
    if ((l & 3) == 0) LP(float, sbuf, X.o_sw) = S1;
    if (l == 0) { L.s12[0][w] = s1t; L.s12[1][w] = s2t; }
    BAR();
    const float St = sum8(LP(const f32x4, sbuf, X.o_c32), LP(const f32x4, sbuf[0][4], X.o_c32));
    if (LAYER == 0) { cs1 = sum8(LP(const f32x4, cs1p, X.o_c32), LP(const f32x4, cs1p[0][4], X.o_c32)); pin(cs1); }
    const float mean = sum8(*(const f32x4*)&L.s12[0][0], *(const f32x4*)&L.s12[0][4]) * (1.f / 512);
    const float ms = sum8(*(const f32x4*)&L.s12[1][0], *(const f32x4*)&L.s12[1][4]) * (1.f / 512);
    const float rstd = __builtin_amdgcn_rsqf(fmx(ms - mean * mean, 0.f) + 1e-5f);
    const float hp = rstd * (St - mean * cs1) + b1v;
    const float uu = hp * (0.7978845608028654f + 0.7978845608028654f * 0.044715f * hp * hp);
    const float ee = ex2(uu * (2.f * 1.4426950408889634f));
    const float hv = hp - hp * __builtin_amdgcn_rcpf(ee + 1.f);
    const unsigned hp2 = pk2(hv, dpp<XOR1>(hv));
    float acc0 = 0.f, acc1 = 0.f;
#define DSTEP(F) acc0 = dot2(dppu<BC + 2 * F>(hp2), w2p[F], acc0); acc1 = dot2(dppu<BC + 2 * F + 2>(hp2), w2p[F + 1], acc1);
    DSTEP(0) DSTEP(2) DSTEP(4) DSTEP(6)
#undef DSTEP
    const unsigned av = san(acc0 + acc1);
    const u32x4 v = {av, dppu<0x55>(av), dppu<0xAA>(av), dppu<0xFF>(av)};
    st128(X.rws, X.aCpub, LAYER * C_SZ, v);
    TS(LAYER * 10 + 17);
  }
  if (w == 7) {
    float o[8];
#pragma unroll
    for (int j = 0; j < 4; ++j) { o[2 * j] = lo(a2[j]); o[2 * j + 1] = hi(a2[j]); }
    float o4[4], oo2[2];
#pragma unroll
    for (int j = 0; j < 4; ++j) o4[j] = tr32(o[j], o[j + 4]);
#pragma unroll
    for (int j = 0; j < 2; ++j) oo2[j] = tr16(o4[j], o4[j + 2]);
    const float v = trd<ROR8>(oo2[0], oo2[1], (l & 8) != 0);
    LP(float, xs, X.o_xs) = v + xres;
  }
  if (mlp) __builtin_amdgcn_s_sleep(SLP_C); else __builtin_amdgcn_s_sleep(SLP_CN);
}

template <int LAYER>
__device__ __forceinline__ float c_gather(Lds& L, const Ctx& X, const Params& p) {
  const int t = X.t, b = X.b, w = X.w;
  (void)b; (void)t; (void)w;
  u32x4 a0, a1, a2;
  asm volatile("" : "=v"(a0), "=v"(a1), "=v"(a2));
  gather_444(a0, a1, a2, X.aC0, X.aC1, X.aC2, X.rws, LAYER * C_SZ, ~0ull);
  VMWAIT(0, "+v"(a0), "+v"(a1), "+v"(a2));
  {
    bool bad = um(um(mx4(a0), mx4(a1)), mx4(a2)) == SENT;
    if (UNLIKELY(__any(bad))) {
      for (unsigned sp_ = 0; sp_ < SPIN_MAX; ++sp_) {
        gather_444(a0, a1, a2, X.aC0, X.aC1, X.aC2, X.rws, LAYER * C_SZ, __ballot(bad));
        VMWAIT(0, "+v"(a0), "+v"(a1), "+v"(a2));
        bad = um(um(mx4(a0), mx4(a1)), mx4(a2)) == SENT;
        if (!__any(bad)) break;
      }
    }
  }
  TS(LAYER * 10 + 12);
  float v[4];
#pragma unroll
  for (int j = 0; j < 4; ++j) v[j] = asf(a0[j]) + asf(a1[j]) + asf(a2[j]);
  const float v0 = tr32(v[0], v[2]), v1 = tr32(v[1], v[3]);
  LP(float, cbuf, X.o_cw) = tr16(v0, v1);
  BAR();
  return sum8(LP(const f32x4, cbuf, X.o_l32), LP(const f32x4, cbuf[0][4], X.o_l32)) + LP(const float, xs, X.o_l4);
}

__global__ void __launch_bounds__(NTHR, 2) fused_kernel(Params p) {
  __shared__ Lds L;
  Ctx X;
  const int t = threadIdx.x, b = blockIdx.x;
  const int w = __builtin_amdgcn_readfirstlane(t >> 6), l = t & 63, g = l >> 4, c = l & 15;
  const int xg = b & 7, idx = b >> 3, s = idx & 7, cu = xg * 32 + idx;
  const int hg = cu >> 3;
  const int h = xg + 8 * (idx >> 3);
  int u = idx < 12 ? 4 * (xg + 8 * (idx >> 2)) + (idx & 3) : -1;
  if (u >= NU) u = -1;
  TS(0);
  const rsrc_t rws = mk(p.ws, WS_END);
  X.t = t; X.b = b; X.w = w; X.l = l; X.g = g; X.c = c; X.xg = xg; X.idx = idx; X.h = h; X.s = s; X.cu = cu; X.u = u; X.rws = rws;
  const rsrc_t rK = mk(p.K + (size_t)(h * 8 + s) * 65536, 131072), rV = mk(p.V + (size_t)(h * 8 + s) * 65536, 131072);
  const rsrc_t rx = mk(p.x, 1024), rwq = mk(p.wq, DM * NH * DH * 2), rwo = mk(p.wo, DM * NH * DH * 2);
  const unsigned mz = u >= 0 ? 1u : 0u;
  const rsrc_t rw1 = mk(p.w1, mz * DM * FF * 2), rw2 = mk(p.w2, mz * DM * FF * 2), rb1 = mk(p.b1, mz * FF * 2), rb2 = mk(p.b2, DM * 2);
  const rsrc_t rd1 = mk(p.ws + WS_D1, w == 0 ? NCU * 1024 : 0);
  unsigned msk[4];
#pragma unroll
  for (int i = 0; i < 4; ++i) msk[i] = (c >> 1) == i ? ((c & 1) ? 0xFFFF0000u : 0x0000FFFFu) : 0u;
  const u32x2 ep = ld64<0>(rws, 0);
  if (w == 0) {
    const u32x4 xv = ld128<0>(rx, l * 16);
    const unsigned xq = ld16<0>(rx, (64 * s + 4 * c + g) * 2);
    u32x4 wqr[16];
#pragma unroll
    for (int n = 0; n < 16; ++n) wqr[n] = ld128<W_AUX>(rwq, (64 * s + g) * 8192 + h * 256 + c * 16, n * 4 * 8192);
    FENCE();
    __builtin_amdgcn_s_barrier();
    FENCE();
    float xs[8];
#pragma unroll
    for (int j = 0; j < 4; ++j) { xs[2 * j] = lo(xv[j]); xs[2 * j + 1] = hi(xv[j]); }
    const float mean = sum64(((xs[0] + xs[1]) + (xs[2] + xs[3])) + ((xs[4] + xs[5]) + (xs[6] + xs[7]))) * (1.f / 512);
    float d2 = 0.f;
#pragma unroll
    for (int j = 0; j < 8; ++j) { const float d = xs[j] - mean; d2 += d * d; }
    const float rstd = __builtin_amdgcn_rsqf(sum64(d2) * (1.f / 512) + 1e-5f);
    const float xn = (lo(xq) - mean) * rstd;
    const unsigned pd = pk2(xn, xn);
    const u32x4 onesA = {ONE2 & msk[0], ONE2 & msk[1], ONE2 & msk[2], ONE2 & msk[3]};
    f32x4 aq = {0.f, 0.f, 0.f, 0.f}, ac = {0.f, 0.f, 0.f, 0.f};
#define QST(n) { const unsigned pv = dppu<BC + n>(pd); const u32x4 a = {pv & msk[0], pv & msk[1], pv & msk[2], pv & msk[3]}; aq = mfma(a, wqr[n], aq); ac = mfma(onesA, wqr[n], ac); L.wqs[n][l] = wqr[n]; }
    QST(0) QST(1) QST(2) QST(3) QST(4) QST(5) QST(6) QST(7) QST(8) QST(9) QST(10) QST(11) QST(12) QST(13) QST(14) QST(15)
#undef QST
    const unsigned tag = ep.x + 1u;
    if (l < 32) {
      const int off = WS_D1 + cu * 1024 + (8 * c + 4 * g) * 8;
      st128(rws, off, 0, u32x4{pk2(aq[0], ac[0]), tag, pk2(aq[1], ac[1]), tag});
      st128(rws, off + 16, 0, u32x4{pk2(aq[2], ac[2]), tag, pk2(aq[3], ac[3]), tag});
    }
    TS(1);
  }
  if (w == 1) {
#pragma unroll
    for (int k = 0; k < 6; ++k) {
      const int q = l + 64 * k;
      int off = OOB;
      if (q < 17) off = WS_A + cu * A_REC + q * 16;
      else if (q < 34) off = WS_A + A_SZ + cu * A_REC + (q - 17) * 16;
      else if (q < 36) off = WS_B + cu * 128 + (q - 34) * 16;
      else if (q < 38) off = WS_B + B_SZ + cu * 128 + (q - 36) * 16;
      else if (q >= 64 && q < 80) off = WS_D2 + cu * D2_REC + ((q - 64) >> 1) * 128 + (q & 1) * 16;
      else if (q >= 80 && q < 97) off = WS_D2 + cu * D2_REC + 1024 + (q - 80) * 16;
      else if (q >= 128 && u >= 0) off = WS_C + ((q - 128) >> 7) * C_SZ + u * 2048 + ((q - 128) & 127) * 16;
      st128(rws, off, 0, u32x4{SENT, SENT, SENT, SENT});
    }
    FENCE();
    __builtin_amdgcn_s_barrier();
    asm volatile("s_waitcnt vmcnt(0)" ::: "memory");
    const u64 e1 = (((u64)ep.y << 32) | ep.x) + 1;
    if (l == 0) __builtin_amdgcn_raw_buffer_store_b64(u32x2{(unsigned)e1, (unsigned)(e1 >> 32)}, rws, WS_FLAGS + cu * 8, 0, SC1);
    TS(4);
  }
  if (t == 128) L.qflag = 0u;
  if (w >= 2) { LWAIT(); __builtin_amdgcn_s_barrier(); }
  FENCE();
  u32x4 kf[16], vr[16];
#pragma unroll
  for (int i = 0; i < 16; ++i) kf[i] = ld128<KV_AUX>(rK, w * 16384 + l * 16, i * 1024);
  FENCE();
  u32x4 d1[8];
#pragma unroll
  for (int i = 0; i < 8; ++i) d1[i] = ld128<SC1>(rd1, hg * 8192 + l * 16, i * 1024);
  FENCE();
  LWAIT();
  __builtin_amdgcn_s_barrier();
  FENCE();
#pragma unroll
  for (int i = 0; i < 8; ++i) vr[i] = ld128<KV_AUX>(rV, w * 16384 + g * 4096 + c * 16, i * 256);
  FENCE();
  {
    const int rec = WS_A + (xg * 32 + (w >> 1) * 8 + (l & 7)) * A_REC;
    X.aA = rec + ((w & 1) * 64 + 8 * g) * 2; X.aAml = rec + 256;
    X.aApub = (l & 3) == 0 ? WS_A + cu * A_REC + l * 4 : l == 1 ? WS_A + cu * A_REC + 256 : OOB;
    X.aB0 = WS_B + ((l & 7) * 32 + 4 * w + (g >> 1)) * 128 + (g & 1) * 16;
    X.aB2 = WS_B + ((l >> 3) * 32 + 4 * s + ((l & 7) >> 1)) * 128 + (l & 1) * 16;
    X.aBpub = (l == 0 || l == 8) ? WS_B + cu * 128 + l * 2 : OOB;
    const int src = w * 4 + g, cc = WS_C + src * 2048 + (64 * s + 4 * c) * 4;
    X.aC0 = cc; X.aC1 = cc + 32 * 2048; X.aC2 = src + 64 < NU ? cc + 64 * 2048 : OOB;
    X.aCpub = (l & 3) == 0 ? WS_C + (u >= 0 ? u : 0) * 2048 + t * 4 : OOB;
    X.aDg = WS_D2 + (hg * 8 + w) * D2_REC + (l < 16 ? (l >> 1) * 128 + (l & 1) * 16 : l < 32 ? 1024 + (l - 16) * 16 : 1280);
    X.aDq = (l == 0 || l == 8) ? WS_D2 + cu * D2_REC + w * 128 + l * 2 : OOB;
    X.aDx = w == 0 ? ((l & 3) == 0 ? WS_D2 + cu * D2_REC + 1024 + l * 4 : OOB) : (l == 0 ? WS_D2 + cu * D2_REC + 1280 : OOB);
    pin(X.aA); pin(X.aAml); pin(X.aApub); pin(X.aB0); pin(X.aB2); pin(X.aBpub); pin(X.aC0); pin(X.aC1); pin(X.aC2); pin(X.aCpub); pin(X.aDg); pin(X.aDq); pin(X.aDx);
    X.o_ob = (w * 128 + 8 * c + 4 * g) * 4; X.o_l8 = l * 8; X.o_ml = (l & 7) * 8;
    X.o_yw = ((4 * g + ((l >> 1) & 3)) * 8 + ((w + 2 * g) & 7)) * 4; X.o_c32 = c * 32;
    X.o_sw = ((4 * g + ((l >> 3) & 1) * 2 + ((l >> 2) & 1)) * 8 + ((w + 2 * g) & 7)) * 4;
    X.o_cw = ((4 * c + g) * 8 + ((w + c) & 7)) * 4; X.o_l32 = l * 32; X.o_l4 = l * 4;
    X.o_xw = w * 256 + l * 2; X.o_xr = w * 256 + (c & 1) * 128 + g * 16;
    X.o_qr = (l & 7) * 288 + g * 16; X.o_st = (l & 7) * 16;
    X.o_dd = l < 16 ? (int)__builtin_offsetof(Lds, qrec) + w * 288 + l * 16 : l < 32 ? (int)__builtin_offsetof(Lds, xfull) + w * 256 + (l - 16) * 16 : (int)__builtin_offsetof(Lds, st2) + w * 16;
    X.o_xs = (8 * (l & 7) + (l >> 3)) * 4;
    pin(X.o_ob); pin(X.o_l8); pin(X.o_ml); pin(X.o_yw); pin(X.o_c32); pin(X.o_sw); pin(X.o_cw); pin(X.o_l32); pin(X.o_l4); pin(X.o_xw); pin(X.o_xr); pin(X.o_qr); pin(X.o_st); pin(X.o_dd); pin(X.o_xs);
  }
  const u64 epoch = (((u64)ep.y << 32) | ep.x) + 1;
  u32x4 wqB[2];
  {
    const unsigned char* st = (const unsigned char*)&L.wqs[0][0];
#pragma unroll
    for (int ci = 0; ci < 2; ++ci)
#pragma unroll
      for (int j2 = 0; j2 < 4; ++j2) {
        const int d0 = 32 * ci + 8 * g + 2 * j2;
        const unsigned a = *(const u16*)(st + (d0 >> 2) * 1024 + (d0 & 3) * 256 + (16 * w + c) * 2), bb = *(const u16*)(st + ((d0 + 1) >> 2) * 1024 + ((d0 + 1) & 3) * 256 + (16 * w + c) * 2);
        wqB[ci][j2] = a | (bb << 16);
      }
    pin(wqB[0]); pin(wqB[1]);
  }
  {
    unsigned char* st = L.big[w];
#pragma unroll
    for (int i = 0; i < 16; ++i) { const int key = 4 * i + g; *(u32x4*)(st + key * 256 + ((c ^ (key & 15)) * 16)) = kf[i]; }
    LWAIT();
#pragma unroll
    for (int T = 0; T < 4; ++T)
#pragma unroll
      for (int i = 0; i < 4; ++i) kf[4 * T + i] = *(const u32x4*)(st + (16 * T + c) * 256 + (((4 * i + g) ^ c) * 16));
  }
  TS(2);
  if (w == 0) {
    const unsigned tag = (unsigned)epoch;
    unsigned bad = 0;
#pragma unroll
    for (int i = 0; i < 8; ++i) bad |= (d1[i].y ^ tag) | (d1[i].w ^ tag);
    if (UNLIKELY(!__all(bad == 0u))) {
      for (unsigned sp_ = 0; sp_ < SPIN_MAX; ++sp_) {
        asm volatile("" ::: "memory");
        bad = 0;
#pragma unroll
        for (int i = 0; i < 8; ++i) { d1[i] = ld128<SC1>(rd1, hg * 8192 + l * 16, i * 1024); bad |= (d1[i].y ^ tag) | (d1[i].w ^ tag); }
        if (__all(bad == 0u)) break;
      }
    }
    float qa = 0.f, qc = 0.f, ca = 0.f, cb = 0.f;
#pragma unroll
    for (int i = 0; i < 8; ++i) { qa += lo(d1[i].x); ca += hi(d1[i].x); qc += lo(d1[i].z); cb += hi(d1[i].z); }
    L.qb[l] = pk2(qa, qc);
    L.csb[l] = pk2(ca, cb);
    LWAIT();
    if (l == 0) __hip_atomic_store(&L.qflag, 1u, __ATOMIC_RELAXED, __HIP_MEMORY_SCOPE_WORKGROUP);
  } else {
    for (unsigned sp_ = 0; sp_ < (SPIN_MAX << 4); ++sp_) {
      if (__hip_atomic_load(&L.qflag, __ATOMIC_RELAXED, __HIP_MEMORY_SCOPE_WORKGROUP) != 0u) break;
      __builtin_amdgcn_s_sleep(1);
    }
  }
  asm volatile("" ::: "memory");
  TS(3);

  float kcs;
  u32x4 wor[2];
  {
    f32x4 sc[4], sk[4];
#pragma unroll
    for (int T = 0; T < 4; ++T) { sc[T] = f32x4{0.f, 0.f, 0.f, 0.f}; sk[T] = sc[T]; }
#pragma unroll
    for (int i = 0; i < 4; ++i) {
      const u32x4 qf = *(const u32x4*)&L.qb[16 * i + 4 * g], cf = *(const u32x4*)&L.csb[16 * i + 4 * g];
#pragma unroll
      for (int T = 0; T < 4; ++T) { sc[T] = mfma(qf, kf[4 * T + i], sc[T]); sk[T] = mfma(cf, kf[4 * T + i], sk[T]); }
    }
    kcs = sel4(g, sk[0][0], sk[1][0], sk[2][0], sk[3][0]); pin(kcs);
    const float tmax = max16(fmx(fmx3(sc[0][0], sc[1][0], sc[2][0]), sc[3][0]));
    float tsel = sel4(g, sc[0][0], sc[1][0], sc[2][0], sc[3][0]);
    float tm = tmax;
    pin(tsel); pin(tm);
    FENCE();
#pragma unroll
    for (int i = 8; i < 16; ++i) vr[i] = ld128<KV_AUX>(rV, w * 16384 + g * 4096 + c * 16, i * 256);
#pragma unroll
    for (int i = 0; i < 2; ++i) wor[i] = ld128<W_AUX>(rwo, ((xg + 8 * (w >> 1)) * 128 + 64 * (w & 1) + 32 * i + (l >> 1)) * 1024 + idx * 32 + (l & 1) * 16);
    const rsrc_t rfl = mk(p.ws + WS_FLAGS, w >= 6 ? 2048 : 0);
    const u32x4 fl = ld128<SC1>(rfl, (t & 127) * 16);
    FENCE();
    pv_publish<0>(L, X, p, tsel, tm, QS, vr, msk, fl, epoch);
  }
  TS(6);
  u32x4 wof[2];
  u32x4 w1r[2]; unsigned w2r[16]; unsigned b1r = 0;
  const int uc = u >= 0 ? u : 0;
  const int xcol = 8 * (l & 7) + (l >> 3);
  unsigned xres_r, b2_r;
  u32x4 xf[2];
  wo_stage<0, 1>(L, X, p, wof, [&]() {
#pragma unroll
    for (int i = 0; i < 2; ++i) w1r[i] = ld128<W_AUX>(rw1, (64 * w + 32 * i + (l >> 1)) * (FF * 2) + uc * 32 + (l & 1) * 16);
#pragma unroll
    for (int f = 0; f < 16; ++f) w2r[f] = ld32<W_AUX>(rw2, (16 * uc + f) * 1024 + (t >> 1) * 4);
    b1r = ld16<0>(rb1, (16 * uc + c) * 2);
    xres_r = ld16<0>(rx, (64 * s + xcol) * 2); b2_r = ld16<0>(rb2, (64 * s + xcol) * 2);
#pragma unroll
    for (int ci = 0; ci < 2; ++ci) xf[ci] = ld128<0>(rx, c == 8 ? (64 * w + 32 * ci + 8 * g) * 2 : 1024);
    FENCE();
    make_frags(L, X, wor, wof, 0);
  });
  TS(9);
  u32x4 w1f[2]; unsigned w2p[8]; float cs1 = 0.f, b1v = 0.f;
  float b2v = 0.f, xres1 = 0.f;
  mlp_stage<0>(L, X, p, xf, w1f, w2p, cs1, b1v, xres1, [&]() {
    make_frags(L, X, w1r, w1f, 2048);
#pragma unroll
    for (int f = 0; f < 8; ++f) { w2p[f] = (t & 1) ? __builtin_amdgcn_perm(w2r[2 * f + 1], w2r[2 * f], 0x07060302u) : __builtin_amdgcn_perm(w2r[2 * f + 1], w2r[2 * f], 0x05040100u); pin(w2p[f]); }
    b1v = lo(b1r); pin(b1v);
    xres1 = lo(xres_r) + lo(b2_r); b2v = lo(b2_r); pin(xres1); pin(b2v); pin(xf[0]); pin(xf[1]);
    const u32x4 ones = {ONE2, ONE2, ONE2, ONE2};
    f32x4 S = mfma(w1f[0], ones, f32x4{0.f, 0.f, 0.f, 0.f});
    S = mfma(w1f[1], ones, S);
    if (c == 0) {
#pragma unroll
      for (int i = 0; i < 4; ++i) L.cs1p[4 * g + i][w] = S[i];
    }
  });
  TS(11);
  {
    const float x2 = c_gather<0>(L, X, p);
    const unsigned hh = pk2(x2, x2);
    const unsigned ll = pk2(x2 - lo(hh), 0.f);
    LP(u16, xhl, X.o_xw) = (u16)hh; LP(u16, xhl[0][1], X.o_xw) = (u16)ll;
    LWAIT();
    const u32x4 a0 = LP(const u32x4, xhl, X.o_xr), a1 = LP(const u32x4, xhl[0][0][32], X.o_xr);
    f32x4 d = mfma(a0, wqB[0], f32x4{0.f, 0.f, 0.f, 0.f});
    d = mfma(a1, wqB[1], d);
    const float q1 = d[0] + d[1];
    const unsigned pk = sanu(pk2(q1, dpp<XOR1>(q1)));
    const u32x4 v = {pk, dppu<SHL + 2>(pk), dppu<SHL + 4>(pk), dppu<SHL + 6>(pk)};
    st128(rws, X.aDq, 0, v);
    if (w == 0) {
      const unsigned xu = san(x2);
      const u32x4 vx = {xu, dppu<0x55>(xu), dppu<0xAA>(xu), dppu<0xFF>(xu)};
      st128(rws, X.aDx, 0, vx);
    } else if (w == 1) {
      const u32x4 ones = {ONE2, ONE2, ONE2, ONE2};
      const f32x4 z = {0.f, 0.f, 0.f, 0.f};
      f32x4 G = mfma(a0, a0, z), s1 = mfma(a0, ones, z);
      G = mfma(a1, a1, G); s1 = mfma(a1, ones, s1);
      float s2 = G[0] + G[1]; s2 += dpp<XOR1>(s2);
      st128(rws, X.aDx, 0, u32x4{san(s1[0] + s1[1]), san(s2), 0u, 0u});
    }
  }
  TS(13);
  {
    u32x4 r4 = {0u, 0u, 0u, 0u};
    pin(r4);
    BAR();
    __builtin_amdgcn_s_sleep(SLP_D);
    gather_4(r4, X.aDg, rws, 0, 0x1FFFFFFFFull);
    VMWAIT(0, "+v"(r4));
    {
      bool bad = mx4(r4) == SENT;
      if (UNLIKELY(__any(bad))) {
        for (unsigned sp_ = 0; sp_ < SPIN_MAX; ++sp_) {
          gather_4(r4, X.aDg, rws, 0, __ballot(bad));
          VMWAIT(0, "+v"(r4));
          bad = mx4(r4) == SENT;
          if (!__any(bad)) break;
        }
      }
    }
    TS(14);
    if (l <= 32) *(u32x4*)((unsigned char*)&L + X.o_dd) = r4;
    BAR();
    u32x4 qf[4];
#pragma unroll
    for (int i = 0; i < 4; ++i) qf[i] = LP(const u32x4, qrec[0][16 * i], X.o_qr);
    const f32x2 st2 = LP(const f32x2, st2, X.o_st);
    f32x4 sc[4];
#pragma unroll
    for (int T = 0; T < 4; ++T) sc[T] = f32x4{0.f, 0.f, 0.f, 0.f};
#pragma unroll
    for (int i = 0; i < 4; ++i)
#pragma unroll
      for (int T = 0; T < 4; ++T) sc[T] = mfma(qf[i], kf[4 * T + i], sc[T]);
    const float mean = sum8l(st2[0]) * (1.f / 512);
    const float ms = sum8l(st2[1]) * (1.f / 512);
    const float scale = __builtin_amdgcn_rsqf(fmx(ms - mean * mean, 0.f) + 1e-5f) * QS;
    const float ta = tr16(sum4(sc[0]), sum4(sc[1])), tb = tr16(sum4(sc[2]), sum4(sc[3]));
    const float tsel = ((g & 2) ? tb : ta) - mean * kcs;
    const float tmax = mx32r(mx16r(max16(tsel)));
    TS(15);
    pv_publish<1>(L, X, p, tsel, tmax, scale, vr, msk, u32x4{0u, 0u, 0u, 0u}, epoch);
  }
  TS(16);
  float xres2 = 0.f;
  wo_stage<1, 0>(L, X, p, wof, [&]() {
#pragma unroll
    for (int ci = 0; ci < 2; ++ci) {
      const f32x4 p0 = *(const f32x4*)&L.xfull[64 * w + 32 * ci + 8 * g], p1 = *(const f32x4*)&L.xfull[64 * w + 32 * ci + 8 * g + 4];
      const float xv8[8] = {p0[0], p0[1], p0[2], p0[3], p1[0], p1[1], p1[2], p1[3]};
#pragma unroll
      for (int j = 0; j < 4; ++j) {
        const unsigned hh = pk2(xv8[2 * j], xv8[2 * j + 1]);
        const unsigned ll = pk2(xv8[2 * j] - lo(hh), xv8[2 * j + 1] - hi(hh));
        xf[ci][j] = c == 8 ? hh : c == 9 ? ll : 0u;
      }
    }
    pin(xf[0]); pin(xf[1]);
    xres2 = L.xfull[64 * s + xcol] + b2v; pin(xres2);
  });
  TS(19);
  const bool fin = (xg == 0 && idx >= 24);
  if (u < 0 && !fin) { TSDUMP(); return; }
  mlp_stage<1>(L, X, p, xf, w1f, w2p, cs1, b1v, xres2, []() {});
  TS(21);
  if (!fin) { TSDUMP(); return; }
  const float o = c_gather<1>(L, X, p);
  if (w == 0) {
    p.out[64 * s + l] = (u16)(pk2(o, o) & 0xffffu);
    if (s == 0 && l == 0) *(u64*)p.ws = epoch;
  }
  TS(23);
  TSDUMP();
}

extern "C" void kernel_launch(void* const* d_in, const int* in_sizes, int n_in, void* d_out, int out_size, void* d_ws, size_t ws_size, hipStream_t stream) {
  Params p;
  p.x = (const u16*)d_in[0]; p.K = (const u16*)d_in[1]; p.V = (const u16*)d_in[2]; p.wq = (const u16*)d_in[3]; p.wo = (const u16*)d_in[4];
  p.w1 = (const u16*)d_in[6]; p.b1 = (const u16*)d_in[7]; p.w2 = (const u16*)d_in[8]; p.b2 = (const u16*)d_in[9];
  p.out = (u16*)d_out; p.ws = (unsigned char*)d_ws;
  hipLaunchKernelGGL(fused_kernel, dim3(NCU), dim3(NTHR), 0, stream, p);
}
```

```cpp
#include <hip/hip_runtime.h>
#include <stdint.h>

typedef unsigned short u16;
typedef unsigned long long u64;
typedef unsigned u32x4 __attribute__((ext_vector_type(4)));
typedef unsigned u32x2 __attribute__((ext_vector_type(2)));
typedef float f32x4 __attribute__((ext_vector_type(4)));
typedef float f32x2 __attribute__((ext_vector_type(2)));
typedef short bf16x8 __attribute__((ext_vector_type(8)));
typedef __bf16 bf16x2 __attribute__((ext_vector_type(2)));
typedef __amdgpu_buffer_rsrc_t rsrc_t;

#define KV_AUX 2
#define KVNT " nt"
#define W_AUX 2
#define SLP_A 4
#define SLP_B 4
#define SLP_AB 36
#define RSLP 0
#define PCLEAN 620u
#define DL_B1 110
#define DL_B2 140
#define DL_X 150
#define DL_C1 250
#define DL_C2 290
#define SLP_C 4
#define SLP_CN 40
#define SLP_D 4

constexpr int DM = 512, NH = 32, SK = 4096, DH = 128, FF = 1376;
constexpr int NCU = 256, NTHR = 512, NU = 86;
constexpr unsigned SENT = 0xFFFFFFFFu;
constexpr unsigned SPIN_MAX = 1u << 17;
constexpr int SC1 = 16;
constexpr float QS = 0.08838834764831845f * 1.4426950408889634f;
constexpr unsigned ONE2 = 0x3F803F80u;
constexpr int OOB = 0x40000000;

constexpr int CPY = 8192;
constexpr int A_REC = 384, A_SZ = 13 * 16384;
constexpr int WS_A = 0;
constexpr int B_SZ = 2 * 16384;
constexpr int WS_B = WS_A + 2 * A_SZ;
constexpr int C_SZ = 22 * 16384;
constexpr int WS_C = WS_B + 2 * B_SZ;
constexpr int D2_REC = 1536;
constexpr int WS_D2 = WS_C + 2 * C_SZ;
constexpr int WS_FLAGS = WS_D2 + 52 * 16384;
constexpr int WS_D1 = 2 << 20;
constexpr int WS_END = WS_D1 + NCU * 1024;
constexpr int WS_STAMP = 8 << 20;
static_assert(WS_FLAGS + 2048 <= WS_D1, "hand-off buffers fit one 2 MB page");
__device__ __forceinline__ int aoff(int r) { return WS_A + (r / 21) * 16384 + (r % 21) * A_REC; }
__device__ __forceinline__ int doff(int r) { return WS_D2 + (r / 5) * 16384 + (r % 5) * D2_REC; }

struct Params {
  const u16 *x, *K, *V, *wq, *wo, *w1, *b1, *w2, *b2;
  u16* out;
  unsigned char* ws;
};

__device__ __forceinline__ float lo(unsigned w) { return __builtin_bit_cast(float, w << 16); }
__device__ __forceinline__ float hi(unsigned w) { return __builtin_bit_cast(float, w & 0xffff0000u); }
__device__ __forceinline__ float asf(unsigned w) { return __builtin_bit_cast(float, w); }
__device__ __forceinline__ unsigned asu(float f) { return __builtin_bit_cast(unsigned, f); }
__device__ __forceinline__ unsigned pk2(float a, float b) { const bf16x2 v = {(__bf16)a, (__bf16)b}; return __builtin_bit_cast(unsigned, v); }
__device__ __forceinline__ unsigned sanu(unsigned u) { return u < 0xFFFFFFFEu ? u : 0xFFFFFFFEu; }
__device__ __forceinline__ unsigned san(float f) { return sanu(asu(f)); }
template <int CTRL> __device__ __forceinline__ unsigned dppu(unsigned x) { return (unsigned)__builtin_amdgcn_mov_dpp((int)x, CTRL, 0xf, 0xf, true); }
template <int CTRL> __device__ __forceinline__ float dpp(float x) { return asf(dppu<CTRL>(asu(x))); }
constexpr int XOR1 = 0xB1, XOR2 = 0x4E, HMIR = 0x141, MIR = 0x140, ROR8 = 0x128, BC = 0x150, SHL = 0x100;
__device__ __forceinline__ float tr32(float a, float b) {
  auto s = __builtin_amdgcn_permlane32_swap(asu(a), asu(b), false, false);
  return asf(s[0]) + asf(s[1]);
}
__device__ __forceinline__ float tr16(float a, float b) {
  auto s = __builtin_amdgcn_permlane16_swap(asu(a), asu(b), false, false);
  return asf(s[0]) + asf(s[1]);
}
template <int CTRL> __device__ __forceinline__ float trd(float a, float b, bool sel) {
  const float keep = sel ? b : a, send = sel ? a : b;
  return keep + dpp<CTRL>(send);
}
__device__ __forceinline__ float sum8l(float x) { x += dpp<XOR1>(x); x += dpp<XOR2>(x); x += dpp<HMIR>(x); return x; }
__device__ __forceinline__ float sum16(float x) { x = sum8l(x); x += dpp<MIR>(x); return x; }
__device__ __forceinline__ float sum64(float x) { x = sum16(x); x = tr16(x, x); x = tr32(x, x); return x; }
__device__ __forceinline__ float fmx(float a, float b) { return __builtin_amdgcn_fmed3f(a, b, __builtin_inff()); }
__device__ __forceinline__ float fmx3(float a, float b, float c) { return fmx(fmx(a, b), c); }
__device__ __forceinline__ float fmxa(float a, float b) { float r; asm("v_max_f32 %0, %1, %2" : "=v"(r) : "v"(a), "v"(b)); return r; }
#define MD(ctl) "s_nop 1\n\tv_max_f32_dpp %0, %0, %0 " ctl " row_mask:0xf bank_mask:0xf\n\t"
__device__ __forceinline__ float max8l(float x) { asm(MD("quad_perm:[1,0,3,2]") MD("quad_perm:[2,3,0,1]") MD("row_half_mirror") : "+v"(x)); return x; }
__device__ __forceinline__ float max16(float x) { asm(MD("quad_perm:[1,0,3,2]") MD("quad_perm:[2,3,0,1]") MD("row_half_mirror") MD("row_mirror") : "+v"(x)); return x; }
__device__ __forceinline__ float mx16r(float a) { auto s = __builtin_amdgcn_permlane16_swap(asu(a), asu(a), false, false); return fmxa(asf(s[0]), asf(s[1])); }
__device__ __forceinline__ float mx32r(float a) { auto s = __builtin_amdgcn_permlane32_swap(asu(a), asu(a), false, false); return fmxa(asf(s[0]), asf(s[1])); }
__device__ __forceinline__ float dot2(unsigned a, unsigned b, float c) {
  return __builtin_amdgcn_fdot2_f32_bf16(__builtin_bit_cast(bf16x2, a), __builtin_bit_cast(bf16x2, b), c, false);
}
__device__ __forceinline__ float ex2(float x) { return __builtin_amdgcn_exp2f(x); }
__device__ __forceinline__ f32x4 mfma(u32x4 a, u32x4 b, f32x4 c) {
  return __builtin_amdgcn_mfma_f32_16x16x32_bf16(__builtin_bit_cast(bf16x8, a), __builtin_bit_cast(bf16x8, b), c, 0, 0, 0);
}
__device__ __forceinline__ float sum4(f32x4 s) { return (s[0] + s[1]) + (s[2] + s[3]); }
__device__ __forceinline__ float sum8(f32x4 a, f32x4 b) { return sum4(a + b); }
__device__ __forceinline__ float sel4(int g, float a, float b, float c, float d) { const float x = (g & 1) ? b : a, y = (g & 1) ? d : c; return (g & 2) ? y : x; }

template <int AUX> __device__ __forceinline__ u32x4 ld128(rsrc_t r, int voff, int soff = 0) { return __builtin_amdgcn_raw_buffer_load_b128(r, voff, soff, AUX); }
template <int AUX> __device__ __forceinline__ u32x2 ld64(rsrc_t r, int voff, int soff = 0) { return __builtin_amdgcn_raw_buffer_load_b64(r, voff, soff, AUX); }
template <int AUX> __device__ __forceinline__ unsigned ld32(rsrc_t r, int voff, int soff = 0) { return __builtin_amdgcn_raw_buffer_load_b32(r, voff, soff, AUX); }
template <int AUX> __device__ __forceinline__ unsigned ld16(rsrc_t r, int voff, int soff = 0) { return __builtin_amdgcn_raw_buffer_load_b16(r, voff, soff, AUX); }
__device__ __forceinline__ void st128(rsrc_t r, int voff, int soff, u32x4 v) { __builtin_amdgcn_raw_buffer_store_b128(v, r, voff, soff, SC1); }
__device__ __forceinline__ void st128x2(rsrc_t r, int voff, int soff, u32x4 v) { st128(r, voff, soff, v); st128(r, voff, soff + CPY, v); }
__device__ __forceinline__ rsrc_t mk(const void* p, unsigned bytes) { return __builtin_amdgcn_make_buffer_rsrc((void*)p, 0, bytes, 0x00020000); }
__device__ __forceinline__ unsigned um(unsigned a, unsigned b) { return a > b ? a : b; }
__device__ __forceinline__ unsigned mx4(u32x4 v) { return um(um(v.x, v.y), um(v.z, v.w)); }
__device__ __forceinline__ unsigned mx2(u32x2 v) { return um(v.x, v.y); }
template <typename T> __device__ __forceinline__ void pin(T& x) { asm volatile("" : "+v"(x)); }

__device__ __forceinline__ u64 uni64(u64 m) { return ((u64)(unsigned)__builtin_amdgcn_readfirstlane((unsigned)(m >> 32)) << 32) | (unsigned)__builtin_amdgcn_readfirstlane((unsigned)m); }
#define LD4(dst, off, imm) "buffer_load_dwordx4 %[" dst "], %[" off "], %[rs], %[so] offen offset:" imm " sc1\n\t"
#define LD2(dst, off, imm) "buffer_load_dwordx2 %[" dst "], %[" off "], %[rs], %[so] offen offset:" imm " sc1\n\t"
__device__ __forceinline__ void gather_442(u32x4& a, u32x4& b, u32x2& c, int oa, int oc, rsrc_t rs, int so, u64 m) {
  asm volatile("s_mov_b64 exec, %[m]\n\t" LD4("a", "oa", "0") LD4("b", "oa", "64") LD2("c", "oc", "0") "s_mov_b64 exec, -1"
               : [a] "+v"(a), [b] "+v"(b), [c] "+v"(c) : [oa] "v"(oa), [oc] "v"(oc), [rs] "s"(rs), [so] "s"(so), [m] "s"(uni64(m)) : "memory");
}
__device__ __forceinline__ void gather_42(u32x4& a, u32x2& c, int oa, int oc, rsrc_t rs, int so, u64 m) {
  asm volatile("s_mov_b64 exec, %[m]\n\t" LD4("a", "oa", "0") LD2("c", "oc", "0") "s_mov_b64 exec, -1"
               : [a] "+v"(a), [c] "+v"(c) : [oa] "v"(oa), [oc] "v"(oc), [rs] "s"(rs), [so] "s"(so), [m] "s"(uni64(m)) : "memory");
}
__device__ __forceinline__ void gather_44s(u32x4& a, u32x4& b, int oa, rsrc_t rs, int so, int so2, u64 m) {
  asm volatile("s_mov_b64 exec, %[m]\n\t" LD4("a", "oa", "0") "buffer_load_dwordx4 %[b], %[oa], %[rs], %[so2] offen sc1\n\ts_mov_b64 exec, -1"
               : [a] "+v"(a), [b] "+v"(b) : [oa] "v"(oa), [rs] "s"(rs), [so] "s"(so), [so2] "s"(so2), [m] "s"(uni64(m)) : "memory");
}
__device__ __forceinline__ void gather_44(u32x4& a, u32x4& b, int oa, rsrc_t rs, int so, u64 m) {
  asm volatile("s_mov_b64 exec, %[m]\n\t" LD4("a", "oa", "0") LD4("b", "oa", "256") "s_mov_b64 exec, -1"
               : [a] "+v"(a), [b] "+v"(b) : [oa] "v"(oa), [rs] "s"(rs), [so] "s"(so), [m] "s"(uni64(m)) : "memory");
}
__device__ __forceinline__ void gather_4(u32x4& a, int oa, rsrc_t rs, int so, u64 m) {
  asm volatile("s_mov_b64 exec, %[m]\n\t" LD4("a", "oa", "0") "s_mov_b64 exec, -1"
               : [a] "+v"(a) : [oa] "v"(oa), [rs] "s"(rs), [so] "s"(so), [m] "s"(uni64(m)) : "memory");
}
__device__ __forceinline__ void gather_444(u32x4& a, u32x4& b, u32x4& c, int oa, int ob, int oc, rsrc_t rs, int so, u64 m) {
  asm volatile("s_mov_b64 exec, %[m]\n\t" LD4("a", "oa", "0") LD4("b", "ob", "0") LD4("c", "oc", "0") "s_mov_b64 exec, -1"
               : [a] "+v"(a), [b] "+v"(b), [c] "+v"(c) : [oa] "v"(oa), [ob] "v"(ob), [oc] "v"(oc), [rs] "s"(rs), [so] "s"(so), [m] "s"(uni64(m)) : "memory");
}
__device__ __forceinline__ u64 umask(bool on, u64 m) { const unsigned f = __builtin_amdgcn_readfirstlane(on ? ~0u : 0u); return m & (((u64)f << 32) | f); }
#define VMWAIT(N, ...) asm volatile("s_waitcnt vmcnt(" #N ")" : __VA_ARGS__ :: "memory")
#define TMO(code) do { } while (0)
#define UNLIKELY(x) __builtin_expect(!!(x), 0)

__device__ __forceinline__ int side_from(unsigned p0, unsigned p1) { return __builtin_amdgcn_readfirstlane(p0 < p1 ? 0 : CPY); }
__device__ __forceinline__ u64 rtime() { return __builtin_amdgcn_s_memrealtime(); }
__device__ __forceinline__ void wait_until(u64 t) { while ((long long)(rtime() - t) < 0) __builtin_amdgcn_s_sleep(1); }
#define FENCE() do { asm volatile("" ::: "memory"); __builtin_amdgcn_sched_barrier(0); } while (0)
#define BAR() do { asm volatile("s_waitcnt lgkmcnt(0)" ::: "memory"); __builtin_amdgcn_s_barrier(); asm volatile("" ::: "memory"); } while (0)
#define LWAIT() asm volatile("s_waitcnt lgkmcnt(0)" ::: "memory")
#define TS(i) do { } while (0)
#define TSV(i, v) do { } while (0)
#define TSDUMP() do { } while (0)

struct __attribute__((aligned(16))) Lds {
  union {
    float obuf[8][128];
    struct { float cbuf[64][8]; u16 xhl[8][2][64]; };
  };
  float ml[8][2];
  unsigned qb[64];
  unsigned csb[64];
  float ybuf[64][8];
  float sbuf[16][8];
  float s12[2][8];
  float cs1p[16][8];
  float xs[64];
  float xfull[512];
  unsigned qrec[8][72];
  float st2[8][4];
  unsigned prb[4];
  unsigned qflag;
  unsigned pad_[3];
  u32x4 wqs[16][64];
  unsigned char big[8][16384];
};
#define LP(T, field, off) (*(T*)((unsigned char*)&L.field + (off)))

struct Ctx {
  int t, b, w, l, g, c, xg, idx, h, s, cu, u;
  rsrc_t rws;
  int sd;
  int aPr, aA, aAml, aApub, aB0, aB2, aBpub, o_bw, o_br, o_yw2, aC0, aC1, aC2, aCpub, aDg, aDq, aDx;
  int o_ob, o_l8, o_ml, o_yw, o_c32, o_sw, o_cw, o_l32, o_l4, o_xw, o_xr, o_qr, o_st, o_dd, o_xs;
};
constexpr u64 M_C8 = 0x00FF00FF00FF00FFull;

__device__ __forceinline__ void make_frags(Lds& L, const Ctx& X, const u32x4 (&raw)[2], u32x4 (&fr)[2], int base) {
  unsigned char* st = &L.big[X.w][base];
#pragma unroll
  for (int i = 0; i < 2; ++i) *(u32x4*)(st + (32 * i + (X.l >> 1)) * 32 + (X.l & 1) * 16) = raw[i];
  LWAIT();
#pragma unroll
  for (int ci = 0; ci < 2; ++ci)
#pragma unroll
    for (int j2 = 0; j2 < 4; ++j2) {
      const unsigned a = *(const u16*)(st + (32 * ci + 8 * X.g + 2 * j2) * 32 + X.c * 2), bb = *(const u16*)(st + (32 * ci + 8 * X.g + 2 * j2 + 1) * 32 + X.c * 2);
      fr[ci][j2] = a | (bb << 16);
    }
  pin(fr[0]); pin(fr[1]);
}

template <int LAYER, typename HOOK>
__device__ __forceinline__ void pv_publish(Lds& L, const Ctx& X, const Params& p, float tsel, float tmax, float scale, const u32x4 (&vr)[16], const unsigned (&msk)[4], HOOK hook) {
  const int t = X.t, b = X.b, w = X.w, l = X.l;
  (void)b; (void)t;
  const float pe = ex2((tsel - tmax) * scale);
  const unsigned pd = pk2(pe, pe);
  if (LAYER == 0) TS(5);
  f32x4 o0 = {0.f, 0.f, 0.f, 0.f}, o1 = {0.f, 0.f, 0.f, 0.f};
#define PVS(n) { const unsigned pv = dppu<BC + n>(pd); const u32x4 a = {pv & msk[0], pv & msk[1], pv & msk[2], pv & msk[3]}; if (n & 1) o1 = mfma(a, vr[n], o1); else o0 = mfma(a, vr[n], o0); }
  PVS(0) PVS(1) PVS(2) PVS(3) PVS(4) PVS(5) PVS(6) PVS(7) PVS(8) PVS(9) PVS(10) PVS(11) PVS(12) PVS(13) PVS(14) PVS(15)
#undef PVS
  const float lsum = sum64(lo(pd));
  o0 += o1;
  if (l < 32) LP(f32x4, obuf, X.o_ob) = o0;
  if (l == 0) *(f32x2*)&L.ml[w][0] = f32x2{tmax * scale, lsum};
  hook();
  BAR();
  if (w == 0) {
    const f32x2 mlu = LP(const f32x2, ml, X.o_ml);
    f32x2 ob[8];
#pragma unroll
    for (int u = 0; u < 8; ++u) ob[u] = LP(const f32x2, obuf[u], X.o_l8);
    const float M = max8l(mlu[0]);
    const float f = ex2(mlu[0] - M);
    const float Ls = sum8l(f * mlu[1]);
    f32x2 o = dpp<BC + 0>(f) * ob[0];
    o += dpp<BC + 1>(f) * ob[1]; o += dpp<BC + 2>(f) * ob[2]; o += dpp<BC + 3>(f) * ob[3];
    o += dpp<BC + 4>(f) * ob[4]; o += dpp<BC + 5>(f) * ob[5]; o += dpp<BC + 6>(f) * ob[6]; o += dpp<BC + 7>(f) * ob[7];
    const unsigned pk = sanu(pk2(o[0], o[1]));
    u32x4 v = {pk, dppu<0x55>(pk), dppu<0xAA>(pk), dppu<0xFF>(pk)};
    if (l == 1) v = u32x4{san(M), san(Ls), 0u, 0u};
    st128x2(X.rws, X.aApub, LAYER * A_SZ, v);
  }
}

template <int LAYER, int NYOUNG, typename HOOK>
__device__ __forceinline__ void wo_stage(Lds& L, Ctx& X, const Params& p, const u32x4 (&wof)[4], HOOK hook) {
  const int t = X.t, b = X.b, w = X.w, l = X.l;
  (void)b; (void)t;
  const unsigned ini = X.c < 8 ? SENT : 0u;
  u32x4 a0 = {ini, ini, ini, ini}; u32x2 mlv = {ini, ini};
  pin(a0); pin(mlv);
  BAR();
  if (LAYER == 0) X.sd = side_from(L.prb[0], L.prb[1]);
  __builtin_amdgcn_s_sleep(SLP_A);
  TS(32 + LAYER * 8);
  gather_42(a0, mlv, X.aA, X.aAml, X.rws, LAYER * A_SZ + X.sd, M_C8);
  FENCE();
  hook();
  FENCE();
  VMWAIT(0, "+v"(a0), "+v"(mlv));
  {
    bool bad = um(mx4(a0), mx2(mlv)) == SENT;
    TSV(33 + LAYER * 8, __any(bad) ? 2 : 1);
    if (UNLIKELY(__any(bad))) {
      for (unsigned sp_ = 0; sp_ < SPIN_MAX; ++sp_) {
        TMO(1 + 10 * LAYER);
        gather_42(a0, mlv, X.aA, X.aAml, X.rws, LAYER * A_SZ + X.sd, __ballot(bad));
        VMWAIT(0, "+v"(a0), "+v"(mlv));
        bad = um(mx4(a0), mx2(mlv)) == SENT;
        if (!__any(bad)) break;
      }
    }
  }
  TS(LAYER * 10 + 8);
  f32x4 d[4];
#pragma unroll
  for (int n = 0; n < 4; ++n) d[n] = mfma(a0, wof[n], f32x4{0.f, 0.f, 0.f, 0.f});
  const float m = asf(mlv.x), ls = asf(mlv.y);
  const float M = max8l(m);
  const float e = ex2(m - M);
  const float f = e * __builtin_amdgcn_rcpf(sum8l(e * ls));
  const bool odd = (X.g & 1) != 0;
  const float F0 = odd ? dpp<BC + 4>(f) : dpp<BC + 0>(f), F1 = odd ? dpp<BC + 5>(f) : dpp<BC + 1>(f), F2 = odd ? dpp<BC + 6>(f) : dpp<BC + 2>(f), F3 = odd ? dpp<BC + 7>(f) : dpp<BC + 3>(f);
  float y[4];
#pragma unroll
  for (int n = 0; n < 4; ++n) y[n] = (F0 * d[n][0] + F1 * d[n][1]) + (F2 * d[n][2] + F3 * d[n][3]);
  const float ya = tr16(y[0], y[1]), yb = tr16(y[2], y[3]);
  if (l < 32) { LP(float, ybuf, X.o_yw) = ya; LP(float, ybuf[32], X.o_yw) = yb; }
  BAR();
  if (w == 0) {
    const float s = sum8(LP(const f32x4, ybuf, X.o_l32), LP(const f32x4, ybuf[0][4], X.o_l32));
    const unsigned pk = sanu(pk2(s, dpp<XOR1>(s)));
    const u32x4 v = {pk, dppu<SHL + 2>(pk), dppu<SHL + 4>(pk), dppu<SHL + 6>(pk)};
    st128x2(X.rws, X.aBpub, LAYER * B_SZ, v);
  }
}

template <int LAYER, typename HOOK>
__device__ __forceinline__ void mlp_stage(Lds& L, Ctx& X, const Params& p, u64 tref, bool wocu, const u32x4 (&xf)[2], const u32x4 (&wA)[4], float& cs1, const float& b1v, const float& xres, HOOK hook) {
  const int t = X.t, b = X.b, w = X.w, l = X.l;
  (void)b; (void)t;
  u32x4 a0 = {0u, 0u, 0u, 0u}, a1 = a0, a2 = a0, a3 = a0;
  pin(a0); pin(a1); pin(a2); pin(a3);
  const bool mlp = X.u >= 0;
  const u64 m01 = umask(mlp, ~0ull), m2 = umask(w == 7, ~0ull);
  BAR();
  if (LAYER == 0) X.sd = __builtin_amdgcn_readfirstlane(L.prb[3]);
  TSV(47, X.sd + 1);
  wait_until(tref + (mlp ? (LAYER == 0 ? DL_B1 : DL_B2) : DL_X));
  TS(34 + LAYER * 8);
  gather_44s(a0, a1, X.aB0, X.rws, LAYER * B_SZ + X.sd, LAYER * B_SZ + X.sd + 16384, m01);
  gather_44s(a2, a3, X.aB2, X.rws, LAYER * B_SZ + X.sd, LAYER * B_SZ + X.sd + 16384, m2);
  FENCE();
  hook();
  float S1x, s1x, s2x;
  {
    const u32x4 ones = {ONE2, ONE2, ONE2, ONE2};
    const f32x4 z = {0.f, 0.f, 0.f, 0.f};
    f32x4 S0 = mfma(wA[0], xf[0], z), G0 = mfma(xf[0], xf[0], z), s10 = mfma(ones, xf[0], z);
    S0 = mfma(wA[1], xf[1], S0); G0 = mfma(xf[1], xf[1], G0); s10 = mfma(ones, xf[1], s10);
    float S2[2];
#pragma unroll
    for (int j = 0; j < 2; ++j) S2[j] = trd<ROR8>(S0[j], S0[j + 2], (l & 8) != 0);
    S1x = trd<HMIR>(S2[0], S2[1], (l & 4) != 0);
    S1x += dpp<XOR2>(S1x); S1x += dpp<XOR1>(S1x);
    s1x = sum16(s10[0]);
    s2x = sum64(sum4(G0));
    pin(S1x); pin(s1x); pin(s2x);
  }
  FENCE();
  VMWAIT(0, "+v"(a0), "+v"(a1), "+v"(a2), "+v"(a3));
  {
    bool bad = um(um(mx4(a0), mx4(a1)), um(mx4(a2), mx4(a3))) == SENT;
    TSV(35 + LAYER * 8, __any(bad) ? 2 : 1);
    if (UNLIKELY(__any(bad))) {
      for (unsigned sp_ = 0; sp_ < SPIN_MAX; ++sp_) {
        TMO(2 + 10 * LAYER);
        const u64 bm = __ballot(bad);
        gather_44s(a0, a1, X.aB0, X.rws, LAYER * B_SZ + X.sd, LAYER * B_SZ + X.sd + 16384, bm & m01);
        gather_44s(a2, a3, X.aB2, X.rws, LAYER * B_SZ + X.sd, LAYER * B_SZ + X.sd + 16384, bm & m2);
        VMWAIT(0, "+v"(a0), "+v"(a1), "+v"(a2), "+v"(a3));
        bad = um(um(mx4(a0), mx4(a1)), um(mx4(a2), mx4(a3))) == SENT;
        if (!__any(bad)) break;
      }
    }
  }
  TS(LAYER * 10 + 10);
  if (mlp) {
    LP(u32x4, big, X.o_bw) = a0; LP(u32x4, big[0][8 * 144], X.o_bw) = a1;
    LWAIT();
    a0 = LP(const u32x4, big, X.o_br); a1 = LP(const u32x4, big[0][64], X.o_br);
    const u32x4 ones = {ONE2, ONE2, ONE2, ONE2};
    const f32x4 z = {0.f, 0.f, 0.f, 0.f};
    f32x4 S = mfma(wA[0], a0, z), G = mfma(a0, a0, z), Gx = mfma(xf[0], a0, z), s1 = mfma(ones, a0, z);
    S = mfma(wA[1], a1, S); G = mfma(a1, a1, G); Gx = mfma(xf[1], a1, Gx); s1 = mfma(ones, a1, s1);
    G += 2.f * Gx;
    float S2[2];
#pragma unroll
    for (int j = 0; j < 2; ++j) S2[j] = trd<ROR8>(S[j], S[j + 2], (l & 8) != 0);
    float S1 = trd<HMIR>(S2[0], S2[1], (l & 4) != 0);
    S1 += dpp<XOR2>(S1); S1 += dpp<XOR1>(S1);
    S1 += S1x;
    const float s2t = sum64(sum4(G)) + s2x;
    const float s1t = sum16(s1[0]) + s1x;
    if ((l & 3) == 0) LP(float, sbuf, X.o_sw) = S1;
    if (l == 0) { L.s12[0][w] = s1t; L.s12[1][w] = s2t; }
    BAR();
    const float St = sum8(LP(const f32x4, sbuf, X.o_c32), LP(const f32x4, sbuf[0][4], X.o_c32));
    if (LAYER == 0) { cs1 = sum8(LP(const f32x4, cs1p, X.o_c32), LP(const f32x4, cs1p[0][4], X.o_c32)); pin(cs1); }
    const float mean = sum8(*(const f32x4*)&L.s12[0][0], *(const f32x4*)&L.s12[0][4]) * (1.f / 512);
    const float ms = sum8(*(const f32x4*)&L.s12[1][0], *(const f32x4*)&L.s12[1][4]) * (1.f / 512);
    const float rstd = __builtin_amdgcn_rsqf(fmx(ms - mean * mean, 0.f) + 1e-5f);
    const float hp = rstd * (St - mean * cs1) + b1v;
    const float uu = hp * (0.7978845608028654f + 0.7978845608028654f * 0.044715f * hp * hp);
    const float ee = ex2(uu * (2.f * 1.4426950408889634f));
    const float hv = hp - hp * __builtin_amdgcn_rcpf(ee + 1.f);
    const unsigned hp2 = pk2(hv, dpp<XOR1>(hv));
    float acc0 = 0.f, acc1 = 0.f;
#define DSTEP(F) acc0 = dot2(dppu<BC + 2 * F>(hp2), wA[2 + (F >> 2)][F & 3], acc0); acc1 = dot2(dppu<BC + 2 * F + 2>(hp2), wA[2 + ((F + 1) >> 2)][(F + 1) & 3], acc1);
    DSTEP(0) DSTEP(2) DSTEP(4) DSTEP(6)
#undef DSTEP
    const unsigned av = san(acc0 + acc1);
    const u32x4 v = {av, dppu<0x55>(av), dppu<0xAA>(av), dppu<0xFF>(av)};
    st128x2(X.rws, X.aCpub, LAYER * C_SZ, v);
    TS(LAYER * 10 + 17);
  }
  if (w == 7) {
    float o[8];
#pragma unroll
    for (int j = 0; j < 4; ++j) { o[2 * j] = lo(a2[j]) + lo(a3[j]); o[2 * j + 1] = hi(a2[j]) + hi(a3[j]); }
    float o4[4], oo2[2];
#pragma unroll
    for (int j = 0; j < 4; ++j) o4[j] = tr32(o[j], o[j + 4]);
#pragma unroll
    for (int j = 0; j < 2; ++j) oo2[j] = tr16(o4[j], o4[j + 2]);
    const float v = trd<ROR8>(oo2[0], oo2[1], (l & 8) != 0);
    LP(float, xs, X.o_xs) = v + xres;
  }
  if (mlp) __builtin_amdgcn_s_sleep(SLP_C); else wait_until(tref + (LAYER == 0 ? DL_C1 : DL_C2));
}

template <int LAYER>
__device__ __forceinline__ float c_gather(Lds& L, const Ctx& X, const Params& p) {
  const int t = X.t, b = X.b, w = X.w;
  (void)b; (void)t; (void)w;
  u32x4 a0, a1, a2;
  asm volatile("" : "=v"(a0), "=v"(a1), "=v"(a2));
  TS(36 + LAYER * 8);
  gather_444(a0, a1, a2, X.aC0, X.aC1, X.aC2, X.rws, LAYER * C_SZ + X.sd, ~0ull);
  VMWAIT(0, "+v"(a0), "+v"(a1), "+v"(a2));
  {
    bool bad = um(um(mx4(a0), mx4(a1)), mx4(a2)) == SENT;
    TSV(37 + LAYER * 8, __any(bad) ? 2 : 1);
    if (UNLIKELY(__any(bad))) {
      for (unsigned sp_ = 0; sp_ < SPIN_MAX; ++sp_) {
        TMO(3 + 10 * LAYER);
        gather_444(a0, a1, a2, X.aC0, X.aC1, X.aC2, X.rws, LAYER * C_SZ + X.sd, __ballot(bad));
        VMWAIT(0, "+v"(a0), "+v"(a1), "+v"(a2));
        bad = um(um(mx4(a0), mx4(a1)), mx4(a2)) == SENT;
        if (!__any(bad)) break;
      }
    }
  }
  TS(LAYER * 10 + 12);
  float v[4];
#pragma unroll
  for (int j = 0; j < 4; ++j) v[j] = asf(a0[j]) + asf(a1[j]) + asf(a2[j]);
  const float v0 = tr32(v[0], v[2]), v1 = tr32(v[1], v[3]);
  LP(float, cbuf, X.o_cw) = tr16(v0, v1);
  BAR();
  return sum8(LP(const f32x4, cbuf, X.o_l32), LP(const f32x4, cbuf[0][4], X.o_l32)) + LP(const float, xs, X.o_l4);
}

__global__ void __launch_bounds__(NTHR, 2) fused_kernel(Params p) {
  __shared__ Lds L;
  Ctx X;
  const int t = threadIdx.x, b = blockIdx.x;
  const int w = __builtin_amdgcn_readfirstlane(t >> 6), l = t & 63, g = l >> 4, c = l & 15;
  const int xg = b & 7, idx = b >> 3, s = idx & 7, cu = xg * 32 + idx;
  const int hg = cu >> 3;
  const int h = xg + 8 * (idx >> 3);
  const bool wocu = idx >= 16;
  const int wpp = (idx >> 3) & 1, wj = idx & 7;
  int u = idx < 12 ? 4 * (xg + 8 * (idx >> 2)) + (idx & 3) : -1;
  if (u >= NU) u = -1;
  TS(0);
  unsigned char* const wsa = (unsigned char*)(((size_t)p.ws + (2u << 20)) & ~(size_t)((2u << 20) - 1));
  const rsrc_t rws = mk(wsa, WS_END), rep = mk(p.ws, 8);
  X.t = t; X.b = b; X.w = w; X.l = l; X.g = g; X.c = c; X.xg = xg; X.idx = idx; X.h = h; X.s = s; X.cu = cu; X.u = u; X.rws = rws;
  const rsrc_t rK = mk(p.K + (size_t)(h * 8 + s) * 65536, 131072), rV = mk(p.V + (size_t)(h * 8 + s) * 65536, 131072);
  const rsrc_t rx = mk(p.x, 1024), rwq = mk(p.wq, DM * NH * DH * 2), rwo = mk(p.wo, wocu ? DM * NH * DH * 2 : 0);
  const unsigned mz = u >= 0 ? 1u : 0u;
  const rsrc_t rw1 = mk(p.w1, mz * DM * FF * 2), rw2 = mk(p.w2, mz * DM * FF * 2), rb1 = mk(p.b1, mz * FF * 2), rb2 = mk(p.b2, DM * 2);
  const rsrc_t rd1 = mk(wsa + WS_D1, w == 0 ? NCU * 1024 : 0);
  unsigned msk[4];
#pragma unroll
  for (int i = 0; i < 4; ++i) msk[i] = (c >> 1) == i ? ((c & 1) ? 0xFFFF0000u : 0x0000FFFFu) : 0u;
  const u32x2 ep = ld64<0>(rep, 0);
  if (w == 0) {
    const u32x4 xv = ld128<0>(rx, l * 16);
    const unsigned xq = ld16<0>(rx, (64 * s + 4 * c + g) * 2);
    u32x4 wqr[16];
#pragma unroll
    for (int n = 0; n < 16; ++n) wqr[n] = ld128<W_AUX>(rwq, (64 * s + g) * 8192 + h * 256 + c * 16, n * 4 * 8192);
    FENCE();
    __builtin_amdgcn_s_barrier();
    FENCE();
    float xs[8];
#pragma unroll
    for (int j = 0; j < 4; ++j) { xs[2 * j] = lo(xv[j]); xs[2 * j + 1] = hi(xv[j]); }
    const float mean = sum64(((xs[0] + xs[1]) + (xs[2] + xs[3])) + ((xs[4] + xs[5]) + (xs[6] + xs[7]))) * (1.f / 512);
    float d2 = 0.f;
#pragma unroll
    for (int j = 0; j < 8; ++j) { const float d = xs[j] - mean; d2 += d * d; }
    const float rstd = __builtin_amdgcn_rsqf(sum64(d2) * (1.f / 512) + 1e-5f);
    const float xn = (lo(xq) - mean) * rstd;
    const unsigned pd = pk2(xn, xn);
    const u32x4 onesA = {ONE2 & msk[0], ONE2 & msk[1], ONE2 & msk[2], ONE2 & msk[3]};
    f32x4 aq = {0.f, 0.f, 0.f, 0.f}, ac = {0.f, 0.f, 0.f, 0.f};
#define QST(n) { const unsigned pv = dppu<BC + n>(pd); const u32x4 a = {pv & msk[0], pv & msk[1], pv & msk[2], pv & msk[3]}; aq = mfma(a, wqr[n], aq); ac = mfma(onesA, wqr[n], ac); L.wqs[n][l] = wqr[n]; }
    QST(0) QST(1) QST(2) QST(3) QST(4) QST(5) QST(6) QST(7) QST(8) QST(9) QST(10) QST(11) QST(12) QST(13) QST(14) QST(15)
#undef QST
    const unsigned tag = ep.x + 1u;
    if (l < 32) {
      const int off = WS_D1 + cu * 1024 + (8 * c + 4 * g) * 8;
      st128(rws, off, 0, u32x4{pk2(aq[0], ac[0]), tag, pk2(aq[1], ac[1]), tag});
      st128(rws, off + 16, 0, u32x4{pk2(aq[2], ac[2]), tag, pk2(aq[3], ac[3]), tag});
    }
    TS(1);
  }
  if (w == 1) {
#pragma unroll
    for (int k = 0; k < 6; ++k) {
      const int q = l + 64 * k;
      int off = OOB;
      if (q < 17) off = aoff(cu) + q * 16;
      else if (q < 34) off = A_SZ + aoff(cu) + (q - 17) * 16;
      else if (q >= 40 && q < 56 && wocu) off = WS_B + ((q - 40) >> 3) * B_SZ + xg * 0 + (((2 * xg + wpp) >> 3) * 16384) + ((2 * xg + wpp) & 7) * 1024 + wj * 128 + (q & 7) * 16;
      else if (q >= 64 && q < 80) off = doff(cu) + ((q - 64) >> 1) * 128 + (q & 1) * 16;
      else if (q >= 80 && q < 97) off = doff(cu) + 1024 + (q - 80) * 16;
      else if (q == 97) off = doff(cu) + 1408;
      else if (q >= 128 && u >= 0) off = WS_C + ((q - 128) >> 7) * C_SZ + (u >> 2) * 16384 + (u & 3) * 2048 + ((q - 128) & 127) * 16;
      st128x2(rws, off, 0, u32x4{SENT, SENT, SENT, SENT});
    }
    FENCE();
    __builtin_amdgcn_s_barrier();
    asm volatile("s_waitcnt vmcnt(0)" ::: "memory");
    const u64 e1 = (((u64)ep.y << 32) | ep.x) + 1;
    if (l == 0) __builtin_amdgcn_raw_buffer_store_b64(u32x2{(unsigned)e1, (unsigned)(e1 >> 32)}, rws, WS_FLAGS + cu * 8, 0, SC1);
    TS(4);
  }
  if (t == 128) { L.qflag = 0u; L.prb[2] = 0u; }
  if (w >= 2) { LWAIT(); __builtin_amdgcn_s_barrier(); }
  FENCE();
  u32x4 kf[16], vr[16];
  {
    int vo[4];
#pragma unroll
    for (int j = 0; j < 4; ++j) vo[j] = w * 16384 + (4 * j + g) * 256 + ((c ^ (4 * j + g)) * 16);
    const unsigned ldsb = (unsigned)(size_t)(__attribute__((address_space(3))) unsigned char*)&L.big[0][0] + w * 16384;
    unsigned keep;
#define KD(j, so) "s_nop 0\n\tbuffer_load_dwordx4 %[v" #j "], %[rs], " so " offen" KVNT " lds\n\ts_add_u32 m0, m0, 0x400\n\t"
#define KD4(so) KD(0, so) KD(1, so) KD(2, so) KD(3, so)
    asm volatile("s_mov_b32 %[keep], m0\n\ts_mov_b32 m0, %[lb]\n\t" KD4("0") KD4("%[s1]") KD4("%[s2]") KD4("%[s3]") "s_mov_b32 m0, %[keep]"
                 : [keep] "=&s"(keep)
                 : [v0] "v"(vo[0]), [v1] "v"(vo[1]), [v2] "v"(vo[2]), [v3] "v"(vo[3]), [rs] "s"(rK), [lb] "s"(ldsb), [s1] "s"(4096), [s2] "s"(8192), [s3] "s"(12288)
                 : "memory", "scc");
#undef KD4
#undef KD
  }
  FENCE();
  u32x4 d1[8];
#pragma unroll
  for (int i = 0; i < 8; ++i) d1[i] = ld128<SC1>(rd1, hg * 8192 + l * 16, i * 1024);
  FENCE();
  {
    const int uc0 = u >= 0 ? u : 0;
    const int rec = aoff(xg * 32 + (2 * wpp + (w >> 2)) * 8 + (l & 7));
    X.aA = rec + ((w & 3) * 32 + 8 * g) * 2; X.aAml = rec + 256;
    X.aApub = (l & 3) == 0 ? aoff(cu) + l * 4 : l == 1 ? aoff(cu) + 256 : OOB;
    X.aB0 = WS_B + (l >> 3) * 1024 + w * 128 + (l & 7) * 16;
    X.aB2 = WS_B + (l >> 3) * 1024 + s * 128 + (l & 7) * 16;
    X.aBpub = (l & 7) == 0 ? WS_B + ((2 * xg + wpp) >> 3) * 16384 + ((2 * xg + wpp) & 7) * 1024 + wj * 128 + l * 2 : OOB;
    X.o_bw = w * 16384 + 8192 + (l >> 3) * 144 + (l & 7) * 16; X.o_br = w * 16384 + 8192 + c * 144 + g * 16;
    pin(X.o_bw); pin(X.o_br);
    const int src = w * 4 + g, cc = WS_C + w * 16384 + g * 2048 + (64 * s + 4 * c) * 4;
    X.aC0 = cc; X.aC1 = cc + 8 * 16384; X.aC2 = src + 64 < NU ? cc + 16 * 16384 : OOB;
    X.aCpub = (l & 3) == 0 ? WS_C + (uc0 >> 2) * 16384 + (uc0 & 3) * 2048 + t * 4 : OOB;
    X.aPr = doff(cu) + 1408;
    X.aDg = doff(hg * 8 + w) + (l < 16 ? (l >> 1) * 128 + (l & 1) * 16 : l < 32 ? 1024 + (l - 16) * 16 : 1280);
    X.aDq = (l == 0 || l == 8) ? doff(cu) + w * 128 + l * 2 : OOB;
    X.aDx = w == 0 ? ((l & 3) == 0 ? doff(cu) + 1024 + l * 4 : OOB) : (l == 0 ? doff(cu) + 1280 : OOB);
    pin(X.aA); pin(X.aAml); pin(X.aApub); pin(X.aB0); pin(X.aB2); pin(X.aBpub); pin(X.aC0); pin(X.aC1); pin(X.aC2); pin(X.aCpub); pin(X.aDg); pin(X.aDq); pin(X.aDx);
    X.o_ob = (w * 128 + 8 * c + 4 * g) * 4; X.o_l8 = l * 8; X.o_ml = (l & 7) * 8;
    X.o_yw = ((16 * (g & 1) + c) * 8 + ((w + c) & 7)) * 4; X.o_c32 = c * 32;
    X.o_sw = ((4 * g + ((l >> 3) & 1) * 2 + ((l >> 2) & 1)) * 8 + ((w + 2 * g) & 7)) * 4;
    X.o_cw = ((4 * c + g) * 8 + ((w + c) & 7)) * 4; X.o_l32 = l * 32; X.o_l4 = l * 4;
    X.o_xw = w * 256 + l * 2; X.o_xr = w * 256 + (c & 1) * 128 + g * 16;
    X.o_qr = (l & 7) * 288 + g * 16; X.o_st = (l & 7) * 16;
    X.o_dd = l < 16 ? (int)__builtin_offsetof(Lds, qrec) + w * 288 + l * 16 : l < 32 ? (int)__builtin_offsetof(Lds, xfull) + w * 256 + (l - 16) * 16 : (int)__builtin_offsetof(Lds, st2) + w * 16;
    X.o_xs = (8 * (l & 7) + (l >> 3)) * 4;
    pin(X.o_ob); pin(X.o_yw); pin(X.o_sw); pin(X.o_cw); pin(X.o_xr); pin(X.o_qr); pin(X.o_dd); pin(X.o_xs);
  }
  TS(28);
  LWAIT();
  __builtin_amdgcn_s_barrier();
  FENCE();
#pragma unroll
  for (int i = 0; i < 16; ++i) vr[i] = ld128<KV_AUX>(rV, w * 16384 + g * 4096 + c * 16, i * 256);
  FENCE();
  TS(30);
  const u64 epoch = (((u64)ep.y << 32) | ep.x) + 1;
  asm volatile("s_waitcnt vmcnt(24)" ::: "memory");
  {
    const unsigned char* st = L.big[w];
#pragma unroll
    for (int T = 0; T < 4; ++T)
#pragma unroll
      for (int i = 0; i < 4; ++i) kf[4 * T + i] = *(const u32x4*)(st + (16 * T + c) * 256 + (((4 * i + g) ^ c) * 16));
  }
  TS(2);
  if (w == 0) {
    const unsigned tag = (unsigned)epoch;
    unsigned bad = 0;
#pragma unroll
    for (int i = 0; i < 8; ++i) bad |= (d1[i].y ^ tag) | (d1[i].w ^ tag);
    if (UNLIKELY(!__all(bad == 0u))) {
      for (unsigned sp_ = 0; sp_ < SPIN_MAX; ++sp_) {
        asm volatile("" ::: "memory");
        bad = 0;
#pragma unroll
        for (int i = 0; i < 8; ++i) { d1[i] = ld128<SC1>(rd1, hg * 8192 + l * 16, i * 1024); bad |= (d1[i].y ^ tag) | (d1[i].w ^ tag); }
        if (__all(bad == 0u)) break;
      }
    }
    float qa = 0.f, qc = 0.f, ca = 0.f, cb = 0.f;
#pragma unroll
    for (int i = 0; i < 8; ++i) { qa += lo(d1[i].x); ca += hi(d1[i].x); qc += lo(d1[i].z); cb += hi(d1[i].z); }
    L.qb[l] = pk2(qa, qc);
    L.csb[l] = pk2(ca, cb);
    LWAIT();
    if (l == 0) __hip_atomic_store(&L.qflag, 1u, __ATOMIC_RELAXED, __HIP_MEMORY_SCOPE_WORKGROUP);
  } else {
    for (unsigned sp_ = 0; sp_ < (SPIN_MAX << 4); ++sp_) {
      if (__hip_atomic_load(&L.qflag, __ATOMIC_RELAXED, __HIP_MEMORY_SCOPE_WORKGROUP) != 0u) break;
      __builtin_amdgcn_s_sleep(1);
    }
  }
  asm volatile("" ::: "memory");
  TS(3);

  float kcs;
  u32x4 rA[4], wA[4], wqB[2], xf[2];
  unsigned rB[8], b1r, xres_r, b2_r;
  float cs1 = 0.f, b1v = 0.f, b2v = 0.f, xres1 = 0.f;
  const int uc = u >= 0 ? u : 0;
  const int xcol = 8 * (l & 7) + (l >> 3);
  {
    f32x4 sc[4];
#pragma unroll
    for (int T = 0; T < 4; ++T) sc[T] = f32x4{0.f, 0.f, 0.f, 0.f};
#pragma unroll
    for (int i = 0; i < 4; ++i) {
      const u32x4 qf = *(const u32x4*)&L.qb[16 * i + 4 * g];
#pragma unroll
      for (int T = 0; T < 4; ++T) sc[T] = mfma(qf, kf[4 * T + i], sc[T]);
    }
    const float tmax = max16(fmx(fmx3(sc[0][0], sc[1][0], sc[2][0]), sc[3][0]));
    const float tsel = sel4(g, sc[0][0], sc[1][0], sc[2][0], sc[3][0]);
    if (wocu) {
#pragma unroll
      for (int i = 0; i < 4; ++i) rA[i] = ld128<W_AUX>(rwo, ((xg + 8 * (2 * wpp + (w >> 2))) * 128 + 32 * (w & 3) + 8 * i + (l >> 3)) * 1024 + wj * 128 + (l & 7) * 16);
#pragma unroll
      for (int f = 0; f < 8; ++f) rB[f] = 0u;
    } else {
#pragma unroll
      for (int i = 0; i < 2; ++i) rA[i] = ld128<W_AUX>(rw1, (64 * w + 32 * i + (l >> 1)) * (FF * 2) + uc * 32 + (l & 1) * 16);
#pragma unroll
      for (int f = 0; f < 8; ++f) rA[2 + (f >> 2)][f & 3] = ld32<W_AUX>(rw2, (16 * uc + f) * 1024 + (t >> 1) * 4);
#pragma unroll
      for (int f = 0; f < 8; ++f) rB[f] = ld32<W_AUX>(rw2, (16 * uc + 8 + f) * 1024 + (t >> 1) * 4);
    }
    FENCE();
    if (w == 7) {
      const unsigned char* pw = wsa + WS_A + (cu / 21) * 16384 + 8064;
      const unsigned char* pl = wsa + doff(cu) + 1408;
      u64 t0, t1, t2; unsigned d0, d1, d2;
      asm volatile("s_load_dword %0, %1, 0x0\n\ts_waitcnt lgkmcnt(0)" : "=s"(d0) : "s"(pw) : "memory");
      asm volatile("s_memtime %0\n\ts_waitcnt lgkmcnt(0)" : "=s"(t0) :: "memory");
      asm volatile("s_load_dword %0, %2, 0x0\n\ts_waitcnt lgkmcnt(0)\n\ts_memtime %1\n\ts_waitcnt lgkmcnt(0)" : "=s"(d1), "=s"(t1) : "s"(pl) : "memory");
      asm volatile("s_load_dword %0, %2, 0x2000\n\ts_waitcnt lgkmcnt(0)\n\ts_memtime %1\n\ts_waitcnt lgkmcnt(0)" : "=s"(d2), "=s"(t2) : "s"(pl) : "memory");
      if (l == 0) { L.prb[0] = (unsigned)(t1 - t0); L.prb[1] = (unsigned)(t2 - t1); }
      LWAIT();
      if (l == 0) __hip_atomic_store(&L.prb[2], 1u, __ATOMIC_RELAXED, __HIP_MEMORY_SCOPE_WORKGROUP);
      TSV(45, (t1 - t0) * 10000 + (t2 - t1));
    }
    FENCE();
    pv_publish<0>(L, X, p, tsel, tmax, QS, vr, msk, [&]() {
      if (w >= 6) {
        const unsigned e0 = (unsigned)epoch, e1 = (unsigned)(epoch >> 32);
        u32x4 fl;
        for (unsigned sp_ = 0; sp_ < SPIN_MAX; ++sp_) {
          asm volatile("" ::: "memory");
          fl = ld128<SC1>(rws, WS_FLAGS + (t & 127) * 16);
          if (__all(((fl.x ^ e0) | (fl.y ^ e1) | (fl.z ^ e0) | (fl.w ^ e1)) == 0u)) break;
          TMO(5);
          __builtin_amdgcn_s_sleep(16);
        }
      }
    });
  }
  u64 tref = rtime();
  TS(6);
  auto shadow1 = [&]() {
    b1r = ld16<0>(rb1, (16 * uc + c) * 2);
    xres_r = ld16<0>(rx, (64 * s + xcol) * 2); b2_r = ld16<0>(rb2, (64 * s + xcol) * 2);
#pragma unroll
    for (int ci = 0; ci < 2; ++ci) xf[ci] = ld128<0>(rx, c == 0 ? (64 * w + 32 * ci + 8 * g) * 2 : 1024);
    {
      f32x4 sk[4];
#pragma unroll
      for (int T = 0; T < 4; ++T) sk[T] = f32x4{0.f, 0.f, 0.f, 0.f};
#pragma unroll
      for (int i = 0; i < 4; ++i) {
        const u32x4 cf = *(const u32x4*)&L.csb[16 * i + 4 * g];
#pragma unroll
        for (int T = 0; T < 4; ++T) sk[T] = mfma(cf, kf[4 * T + i], sk[T]);
      }
      kcs = sel4(g, sk[0][0], sk[1][0], sk[2][0], sk[3][0]); pin(kcs);
    }
    {
      const unsigned char* st = (const unsigned char*)&L.wqs[0][0];
#pragma unroll
      for (int ci = 0; ci < 2; ++ci)
#pragma unroll
        for (int j2 = 0; j2 < 4; ++j2) {
          const int d0 = 32 * ci + 8 * g + 2 * j2;
          const unsigned a = *(const u16*)(st + (d0 >> 2) * 1024 + (d0 & 3) * 256 + (16 * w + c) * 2), bb = *(const u16*)(st + ((d0 + 1) >> 2) * 1024 + ((d0 + 1) & 3) * 256 + (16 * w + c) * 2);
          wqB[ci][j2] = a | (bb << 16);
        }
      pin(wqB[0]); pin(wqB[1]);
    }
    b2v = lo(b2_r); xres1 = lo(xres_r) + b2v; pin(b2v); pin(xres1);
  };
  if (wocu) {
    {
      unsigned char* st = L.big[w];
#pragma unroll
      for (int i = 0; i < 4; ++i) *(u32x4*)(st + (8 * i + (l >> 3)) * 128 + (l & 7) * 16) = rA[i];
      LWAIT();
#pragma unroll
      for (int n = 0; n < 4; ++n) {
        typedef short s16x4 __attribute__((ext_vector_type(4)));
        const unsigned base = (unsigned)(size_t)(__attribute__((address_space(3))) unsigned char*)st + (8 * g + (c >> 2)) * 128 + (16 * n + 4 * (c & 3)) * 2;
        const s16x4 lo4 = __builtin_amdgcn_ds_read_tr16_b64_v4i16((__attribute__((address_space(3))) s16x4*)(size_t)base), hi4 = __builtin_amdgcn_ds_read_tr16_b64_v4i16((__attribute__((address_space(3))) s16x4*)(size_t)(base + 512));
        const u32x2 l2 = __builtin_bit_cast(u32x2, lo4), h2 = __builtin_bit_cast(u32x2, hi4);
        wA[n] = u32x4{l2.x, l2.y, h2.x, h2.y}; pin(wA[n]);
      }
    }
    wo_stage<0, 0>(L, X, p, wA, shadow1);
  } else {
    shadow1();
  }
  TS(9);
  if (!wocu) {
    {
      const u32x4 w1r[2] = {rA[0], rA[1]};
      u32x4 fr[2];
      make_frags(L, X, w1r, fr, 2048);
      wA[0] = fr[0]; wA[1] = fr[1];
    }
#pragma unroll
    for (int f = 0; f < 8; ++f) {
      const unsigned r0 = f < 4 ? rA[2 + (f >> 1)][(2 * f) & 3] : rB[2 * f - 8], r1 = f < 4 ? rA[2 + (f >> 1)][(2 * f + 1) & 3] : rB[2 * f - 7];
      wA[2 + (f >> 2)][f & 3] = (t & 1) ? __builtin_amdgcn_perm(r1, r0, 0x07060302u) : __builtin_amdgcn_perm(r1, r0, 0x05040100u);
    }
    pin(wA[0]); pin(wA[1]); pin(wA[2]); pin(wA[3]);
    b1v = lo(b1r); pin(b1v);
    const u32x4 ones = {ONE2, ONE2, ONE2, ONE2};
    f32x4 S = mfma(wA[0], ones, f32x4{0.f, 0.f, 0.f, 0.f});
    S = mfma(wA[1], ones, S);
    if (c == 0) {
#pragma unroll
      for (int i = 0; i < 4; ++i) L.cs1p[4 * g + i][w] = S[i];
    }
  }
  if (w == 6) {
    unsigned pa, pb;
    {
      LWAIT();
      while (__hip_atomic_load(&L.prb[2], __ATOMIC_RELAXED, __HIP_MEMORY_SCOPE_WORKGROUP) == 0u) __builtin_amdgcn_s_sleep(1);
      const unsigned p0 = L.prb[0], p1 = L.prb[1];
      const bool near0 = p0 > 250u && p0 < PCLEAN && p1 > p0 + 100u, near1 = p1 > 250u && p1 < PCLEAN && p0 > p1 + 100u;
      pa = near0 ? 1u : 2u; pb = near0 ? 2u : 1u;
      if (near0 || near1) goto probed;
    }
    for (int tries = 0; tries < 8; ++tries) {
      u64 t0, t1, t2;
      st128x2(rws, l == 0 ? X.aPr : OOB, 0, u32x4{SENT, SENT, SENT, SENT});
      asm volatile("s_waitcnt vmcnt(0)\n\ts_memtime %0\n\ts_waitcnt lgkmcnt(0)" : "=s"(t0) :: "memory");
      const unsigned v0 = ld32<SC1>(rws, X.aPr, 0);
      asm volatile("s_waitcnt vmcnt(0)\n\ts_memtime %0\n\ts_waitcnt lgkmcnt(0)" : "=s"(t1) : "v"(v0) : "memory");
      const unsigned v1 = ld32<SC1>(rws, X.aPr, CPY);
      asm volatile("s_waitcnt vmcnt(0)\n\ts_memtime %0\n\ts_waitcnt lgkmcnt(0)" : "=s"(t2) : "v"(v1) : "memory");
      pa = (unsigned)(t1 - t0); pb = (unsigned)(t2 - t1);
      const unsigned mn = pa < pb ? pa : pb, mxp = pa < pb ? pb : pa;
      if ((mn < 600u && mxp > mn + 90u && mxp < 1000u) || (long long)(rtime() - (tref + 400)) > 0) break;
    }
  probed:
    if (l == 0) { L.prb[3] = pa < pb ? 0u : (unsigned)CPY; }
    TSV(46, pa * 10000 + pb);
  }
  mlp_stage<0>(L, X, p, tref, wocu, xf, wA, cs1, b1v, xres1, []() {});
  TS(11);
  {
    const float x2 = c_gather<0>(L, X, p);
    const unsigned hh = pk2(x2, x2);
    const unsigned ll = pk2(x2 - lo(hh), 0.f);
    LP(u16, xhl, X.o_xw) = (u16)hh; LP(u16, xhl[0][1], X.o_xw) = (u16)ll;
    LWAIT();
    const u32x4 a0 = LP(const u32x4, xhl, X.o_xr), a1 = LP(const u32x4, xhl[0][0][32], X.o_xr);
    f32x4 d = mfma(a0, wqB[0], f32x4{0.f, 0.f, 0.f, 0.f});
    d = mfma(a1, wqB[1], d);
    const float q1 = d[0] + d[1];
    const unsigned pk = sanu(pk2(q1, dpp<XOR1>(q1)));
    const u32x4 v = {pk, dppu<SHL + 2>(pk), dppu<SHL + 4>(pk), dppu<SHL + 6>(pk)};
    st128x2(rws, X.aDq, 0, v);
    if (w == 0) {
      const unsigned xu = san(x2);
      const u32x4 vx = {xu, dppu<0x55>(xu), dppu<0xAA>(xu), dppu<0xFF>(xu)};
      st128x2(rws, X.aDx, 0, vx);
    } else if (w == 1) {
      const u32x4 ones = {ONE2, ONE2, ONE2, ONE2};
      const f32x4 z = {0.f, 0.f, 0.f, 0.f};
      f32x4 G = mfma(a0, a0, z), s1 = mfma(a0, ones, z);
      G = mfma(a1, a1, G); s1 = mfma(a1, ones, s1);
      float s2 = G[0] + G[1]; s2 += dpp<XOR1>(s2);
      st128x2(rws, X.aDx, 0, u32x4{san(s1[0] + s1[1]), san(s2), 0u, 0u});
    }
  }
  TS(13);
  {
    u32x4 r4 = {0u, 0u, 0u, 0u};
    pin(r4);
    BAR();
    __builtin_amdgcn_s_sleep(SLP_D);
    TS(38);
    gather_4(r4, X.aDg, rws, X.sd, 0x1FFFFFFFFull);
    VMWAIT(0, "+v"(r4));
    {
      bool bad = mx4(r4) == SENT;
      TSV(39, __any(bad) ? 2 : 1);
      if (UNLIKELY(__any(bad))) {
        for (unsigned sp_ = 0; sp_ < SPIN_MAX; ++sp_) {
          TMO(4);
          gather_4(r4, X.aDg, rws, X.sd, __ballot(bad));
          VMWAIT(0, "+v"(r4));
          bad = mx4(r4) == SENT;
          if (!__any(bad)) break;
        }
      }
    }
    TS(14);
    if (l <= 32) *(u32x4*)((unsigned char*)&L + X.o_dd) = r4;
    BAR();
    u32x4 qf[4];
#pragma unroll
    for (int i = 0; i < 4; ++i) qf[i] = LP(const u32x4, qrec[0][16 * i], X.o_qr);
    const f32x2 st2 = LP(const f32x2, st2, X.o_st);
    f32x4 sc[4];
#pragma unroll
    for (int T = 0; T < 4; ++T) sc[T] = f32x4{0.f, 0.f, 0.f, 0.f};
#pragma unroll
    for (int i = 0; i < 4; ++i)
#pragma unroll
      for (int T = 0; T < 4; ++T) sc[T] = mfma(qf[i], kf[4 * T + i], sc[T]);
    const float mean = sum8l(st2[0]) * (1.f / 512);
    const float ms = sum8l(st2[1]) * (1.f / 512);
    const float scale = __builtin_amdgcn_rsqf(fmx(ms - mean * mean, 0.f) + 1e-5f) * QS;
    const float ta = tr16(sum4(sc[0]), sum4(sc[1])), tb = tr16(sum4(sc[2]), sum4(sc[3]));
    const float tsel = ((g & 2) ? tb : ta) - mean * kcs;
    const float tmax = mx32r(mx16r(max16(tsel)));
    TS(15);
    pv_publish<1>(L, X, p, tsel, tmax, scale, vr, msk, []() {});
  }
  tref = rtime();
  TS(16);
  float xres2 = 0.f;
  auto shadow2 = [&]() {
    xres2 = L.xfull[64 * s + xcol] + b2v; pin(xres2);
    if (!wocu) {
#pragma unroll
      for (int ci = 0; ci < 2; ++ci) {
        const f32x4 p0 = *(const f32x4*)&L.xfull[64 * w + 32 * ci + 8 * g], p1 = *(const f32x4*)&L.xfull[64 * w + 32 * ci + 8 * g + 4];
        const float xv8[8] = {p0[0], p0[1], p0[2], p0[3], p1[0], p1[1], p1[2], p1[3]};
#pragma unroll
        for (int j = 0; j < 4; ++j) {
          const unsigned hh = pk2(xv8[2 * j], xv8[2 * j + 1]);
          const unsigned ll = pk2(xv8[2 * j] - lo(hh), xv8[2 * j + 1] - hi(hh));
          xf[ci][j] = c == 0 ? hh : c == 1 ? ll : 0u;
        }
      }
      pin(xf[0]); pin(xf[1]);
    }
  };
  if (wocu) wo_stage<1, 0>(L, X, p, wA, shadow2); else shadow2();
  TS(19);
  const bool fin = (xg == 0 && idx >= 24);
  if (u < 0 && !fin) { TSDUMP(); return; }
  mlp_stage<1>(L, X, p, tref, wocu, xf, wA, cs1, b1v, xres2, []() {});
  TS(21);
  if (!fin) { TSDUMP(); return; }
  const float o = c_gather<1>(L, X, p);
  if (w == 0) {
    p.out[64 * s + l] = (u16)(pk2(o, o) & 0xffffu);
    if (s == 0 && l == 0) *(u64*)p.ws = epoch;
  }
  TS(23);
  TSDUMP();
}

extern "C" void kernel_launch(void* const* d_in, const int* in_sizes, int n_in, void* d_out, int out_size, void* d_ws, size_t ws_size, hipStream_t stream) {
  Params p;
  p.x = (const u16*)d_in[0]; p.K = (const u16*)d_in[1]; p.V = (const u16*)d_in[2]; p.wq = (const u16*)d_in[3]; p.wo = (const u16*)d_in[4];
  p.w1 = (const u16*)d_in[6]; p.b1 = (const u16*)d_in[7]; p.w2 = (const u16*)d_in[8]; p.b2 = (const u16*)d_in[9];
  p.out = (u16*)d_out; p.ws = (unsigned char*)d_ws;
  hipLaunchKernelGGL(fused_kernel, dim3(NCU), dim3(NTHR), 0, stream, p);
}
```

```cpp
#include <hip/hip_runtime.h>
#include <stdint.h>

typedef unsigned short u16;
typedef unsigned long long u64;
typedef unsigned u32x4 __attribute__((ext_vector_type(4)));
typedef unsigned u32x2 __attribute__((ext_vector_type(2)));
typedef float f32x4 __attribute__((ext_vector_type(4)));
typedef float f32x2 __attribute__((ext_vector_type(2)));
typedef short bf16x8 __attribute__((ext_vector_type(8)));
typedef __bf16 bf16x2 __attribute__((ext_vector_type(2)));
typedef __amdgpu_buffer_rsrc_t rsrc_t;

#define KV_AUX 2
#define KVNT " nt"
#define W_AUX 2
#define SLP_A 4
#define SLP_B 4
#define SLP_AB 36
#define RSLP 0
#define PDL 30
#define PCLEAN 620u
#define DL_B1 110
#define DL_B2 150
#define DL_X 150
#define DL_CP 115
#define DL_CB 70
#define SLP_C 4
#define SLP_CN 40
#define SLP_D 4

constexpr int DM = 512, NH = 32, SK = 4096, DH = 128, FF = 1376;
constexpr int NCU = 256, NTHR = 512, NU = 86;
constexpr unsigned SENT = 0xFFFFFFFFu;
constexpr unsigned SPIN_MAX = 1u << 17;
constexpr int SC1 = 16;
constexpr float QS = 0.08838834764831845f * 1.4426950408889634f;
constexpr unsigned ONE2 = 0x3F803F80u;
constexpr int OOB = 0x40000000;

constexpr int CPY = 8192;
constexpr int A_REC = 384, A_SZ = 13 * 16384;
constexpr int WS_A = 0;
constexpr int B_SZ = 2 * 16384;
constexpr int WS_B = WS_A + 2 * A_SZ;
constexpr int C_SZ = 22 * 16384;
constexpr int WS_C = WS_B + 2 * B_SZ;
constexpr int D2_REC = 1536;
constexpr int WS_D2 = WS_C + 2 * C_SZ;
constexpr int WS_FLAGS = WS_D2 + 52 * 16384;
constexpr int WS_D1 = 2 << 20;
constexpr int WS_END = WS_D1 + NCU * 1024;
constexpr int WS_STAMP = 8 << 20;
static_assert(WS_FLAGS + 2048 <= WS_D1, "hand-off buffers fit one 2 MB page");
__device__ __forceinline__ int aoff(int r) { return WS_A + (r / 21) * 16384 + (r % 21) * A_REC; }
__device__ __forceinline__ int doff(int r) { return WS_D2 + (r / 5) * 16384 + (r % 5) * D2_REC; }

struct Params {
  const u16 *x, *K, *V, *wq, *wo, *w1, *b1, *w2, *b2;
  u16* out;
  unsigned char* ws;
};

__device__ __forceinline__ float lo(unsigned w) { return __builtin_bit_cast(float, w << 16); }
__device__ __forceinline__ float hi(unsigned w) { return __builtin_bit_cast(float, w & 0xffff0000u); }
__device__ __forceinline__ float asf(unsigned w) { return __builtin_bit_cast(float, w); }
__device__ __forceinline__ unsigned asu(float f) { return __builtin_bit_cast(unsigned, f); }
__device__ __forceinline__ unsigned pk2(float a, float b) { const bf16x2 v = {(__bf16)a, (__bf16)b}; return __builtin_bit_cast(unsigned, v); }
__device__ __forceinline__ unsigned sanu(unsigned u) { return u < 0xFFFFFFFEu ? u : 0xFFFFFFFEu; }
__device__ __forceinline__ unsigned san(float f) { return sanu(asu(f)); }
template <int CTRL> __device__ __forceinline__ unsigned dppu(unsigned x) { return (unsigned)__builtin_amdgcn_mov_dpp((int)x, CTRL, 0xf, 0xf, true); }
template <int CTRL> __device__ __forceinline__ float dpp(float x) { return asf(dppu<CTRL>(asu(x))); }
constexpr int XOR1 = 0xB1, XOR2 = 0x4E, HMIR = 0x141, MIR = 0x140, ROR8 = 0x128, BC = 0x150, SHL = 0x100;
__device__ __forceinline__ float tr32(float a, float b) {
  auto s = __builtin_amdgcn_permlane32_swap(asu(a), asu(b), false, false);
  return asf(s[0]) + asf(s[1]);
}
__device__ __forceinline__ float tr16(float a, float b) {
  auto s = __builtin_amdgcn_permlane16_swap(asu(a), asu(b), false, false);
  return asf(s[0]) + asf(s[1]);
}
template <int CTRL> __device__ __forceinline__ float trd(float a, float b, bool sel) {
  const float keep = sel ? b : a, send = sel ? a : b;
  return keep + dpp<CTRL>(send);
}
__device__ __forceinline__ float sum8l(float x) { x += dpp<XOR1>(x); x += dpp<XOR2>(x); x += dpp<HMIR>(x); return x; }
__device__ __forceinline__ float sum16(float x) { x = sum8l(x); x += dpp<MIR>(x); return x; }
__device__ __forceinline__ float sum64(float x) { x = sum16(x); x = tr16(x, x); x = tr32(x, x); return x; }
__device__ __forceinline__ float fmx(float a, float b) { return __builtin_amdgcn_fmed3f(a, b, __builtin_inff()); }
__device__ __forceinline__ float fmx3(float a, float b, float c) { return fmx(fmx(a, b), c); }
__device__ __forceinline__ float fmxa(float a, float b) { float r; asm("v_max_f32 %0, %1, %2" : "=v"(r) : "v"(a), "v"(b)); return r; }
#define MD(ctl) "s_nop 1\n\tv_max_f32_dpp %0, %0, %0 " ctl " row_mask:0xf bank_mask:0xf\n\t"
__device__ __forceinline__ float max8l(float x) { asm(MD("quad_perm:[1,0,3,2]") MD("quad_perm:[2,3,0,1]") MD("row_half_mirror") : "+v"(x)); return x; }
__device__ __forceinline__ float max16(float x) { asm(MD("quad_perm:[1,0,3,2]") MD("quad_perm:[2,3,0,1]") MD("row_half_mirror") MD("row_mirror") : "+v"(x)); return x; }
__device__ __forceinline__ float mx16r(float a) { auto s = __builtin_amdgcn_permlane16_swap(asu(a), asu(a), false, false); return fmxa(asf(s[0]), asf(s[1])); }
__device__ __forceinline__ float mx32r(float a) { auto s = __builtin_amdgcn_permlane32_swap(asu(a), asu(a), false, false); return fmxa(asf(s[0]), asf(s[1])); }
__device__ __forceinline__ float dot2(unsigned a, unsigned b, float c) {
  return __builtin_amdgcn_fdot2_f32_bf16(__builtin_bit_cast(bf16x2, a), __builtin_bit_cast(bf16x2, b), c, false);
}
__device__ __forceinline__ float ex2(float x) { return __builtin_amdgcn_exp2f(x); }
__device__ __forceinline__ f32x4 mfma(u32x4 a, u32x4 b, f32x4 c) {
  return __builtin_amdgcn_mfma_f32_16x16x32_bf16(__builtin_bit_cast(bf16x8, a), __builtin_bit_cast(bf16x8, b), c, 0, 0, 0);
}
__device__ __forceinline__ float sum4(f32x4 s) { return (s[0] + s[1]) + (s[2] + s[3]); }
__device__ __forceinline__ float sum8(f32x4 a, f32x4 b) { return sum4(a + b); }
__device__ __forceinline__ float sel4(int g, float a, float b, float c, float d) { const float x = (g & 1) ? b : a, y = (g & 1) ? d : c; return (g & 2) ? y : x; }

template <int AUX> __device__ __forceinline__ u32x4 ld128(rsrc_t r, int voff, int soff = 0) { return __builtin_amdgcn_raw_buffer_load_b128(r, voff, soff, AUX); }
template <int AUX> __device__ __forceinline__ u32x2 ld64(rsrc_t r, int voff, int soff = 0) { return __builtin_amdgcn_raw_buffer_load_b64(r, voff, soff, AUX); }
template <int AUX> __device__ __forceinline__ unsigned ld32(rsrc_t r, int voff, int soff = 0) { return __builtin_amdgcn_raw_buffer_load_b32(r, voff, soff, AUX); }
template <int AUX> __device__ __forceinline__ unsigned ld16(rsrc_t r, int voff, int soff = 0) { return __builtin_amdgcn_raw_buffer_load_b16(r, voff, soff, AUX); }
__device__ __forceinline__ void st128(rsrc_t r, int voff, int soff, u32x4 v) { __builtin_amdgcn_raw_buffer_store_b128(v, r, voff, soff, SC1); }
__device__ __forceinline__ void st128x2(rsrc_t r, int voff, int soff, u32x4 v) { st128(r, voff, soff, v); st128(r, voff, soff + CPY, v); }
__device__ __forceinline__ rsrc_t mk(const void* p, unsigned bytes) { return __builtin_amdgcn_make_buffer_rsrc((void*)p, 0, bytes, 0x00020000); }
__device__ __forceinline__ unsigned um(unsigned a, unsigned b) { return a > b ? a : b; }
__device__ __forceinline__ unsigned mx4(u32x4 v) { return um(um(v.x, v.y), um(v.z, v.w)); }
__device__ __forceinline__ unsigned mx2(u32x2 v) { return um(v.x, v.y); }
template <typename T> __device__ __forceinline__ void pin(T& x) { asm volatile("" : "+v"(x)); }

__device__ __forceinline__ u64 uni64(u64 m) { return ((u64)(unsigned)__builtin_amdgcn_readfirstlane((unsigned)(m >> 32)) << 32) | (unsigned)__builtin_amdgcn_readfirstlane((unsigned)m); }
#define LD4(dst, off, imm) "buffer_load_dwordx4 %[" dst "], %[" off "], %[rs], %[so] offen offset:" imm " sc1\n\t"
#define LD2(dst, off, imm) "buffer_load_dwordx2 %[" dst "], %[" off "], %[rs], %[so] offen offset:" imm " sc1\n\t"
__device__ __forceinline__ void gather_442(u32x4& a, u32x4& b, u32x2& c, int oa, int oc, rsrc_t rs, int so, u64 m) {
  asm volatile("s_mov_b64 exec, %[m]\n\t" LD4("a", "oa", "0") LD4("b", "oa", "64") LD2("c", "oc", "0") "s_mov_b64 exec, -1"
               : [a] "+v"(a), [b] "+v"(b), [c] "+v"(c) : [oa] "v"(oa), [oc] "v"(oc), [rs] "s"(rs), [so] "s"(so), [m] "s"(uni64(m)) : "memory");
}
__device__ __forceinline__ void gather_42(u32x4& a, u32x2& c, int oa, int oc, rsrc_t rs, int so, u64 m) {
  asm volatile("s_mov_b64 exec, %[m]\n\t" LD4("a", "oa", "0") LD2("c", "oc", "0") "s_mov_b64 exec, -1"
               : [a] "+v"(a), [c] "+v"(c) : [oa] "v"(oa), [oc] "v"(oc), [rs] "s"(rs), [so] "s"(so), [m] "s"(uni64(m)) : "memory");
}
__device__ __forceinline__ void gather_44s(u32x4& a, u32x4& b, int oa, rsrc_t rs, int so, int so2, u64 m) {
  asm volatile("s_mov_b64 exec, %[m]\n\t" LD4("a", "oa", "0") "buffer_load_dwordx4 %[b], %[oa], %[rs], %[so2] offen sc1\n\ts_mov_b64 exec, -1"
               : [a] "+v"(a), [b] "+v"(b) : [oa] "v"(oa), [rs] "s"(rs), [so] "s"(so), [so2] "s"(so2), [m] "s"(uni64(m)) : "memory");
}
__device__ __forceinline__ void gather_44(u32x4& a, u32x4& b, int oa, rsrc_t rs, int so, u64 m) {
  asm volatile("s_mov_b64 exec, %[m]\n\t" LD4("a", "oa", "0") LD4("b", "oa", "256") "s_mov_b64 exec, -1"
               : [a] "+v"(a), [b] "+v"(b) : [oa] "v"(oa), [rs] "s"(rs), [so] "s"(so), [m] "s"(uni64(m)) : "memory");
}
__device__ __forceinline__ void gather_4(u32x4& a, int oa, rsrc_t rs, int so, u64 m) {
  asm volatile("s_mov_b64 exec, %[m]\n\t" LD4("a", "oa", "0") "s_mov_b64 exec, -1"
               : [a] "+v"(a) : [oa] "v"(oa), [rs] "s"(rs), [so] "s"(so), [m] "s"(uni64(m)) : "memory");
}
__device__ __forceinline__ void gather_444(u32x4& a, u32x4& b, u32x4& c, int oa, int ob, int oc, rsrc_t rs, int so, u64 m) {
  asm volatile("s_mov_b64 exec, %[m]\n\t" LD4("a", "oa", "0") LD4("b", "ob", "0") LD4("c", "oc", "0") "s_mov_b64 exec, -1"
               : [a] "+v"(a), [b] "+v"(b), [c] "+v"(c) : [oa] "v"(oa), [ob] "v"(ob), [oc] "v"(oc), [rs] "s"(rs), [so] "s"(so), [m] "s"(uni64(m)) : "memory");
}
__device__ __forceinline__ u64 umask(bool on, u64 m) { const unsigned f = __builtin_amdgcn_readfirstlane(on ? ~0u : 0u); return m & (((u64)f << 32) | f); }
#define VMWAIT(N, ...) asm volatile("s_waitcnt vmcnt(" #N ")" : __VA_ARGS__ :: "memory")
#define TMO(code) do { } while (0)
#define UNLIKELY(x) __builtin_expect(!!(x), 0)

__device__ __forceinline__ int side_from(unsigned p0, unsigned p1) { return __builtin_amdgcn_readfirstlane(p0 < p1 ? 0 : CPY); }
__device__ __forceinline__ u64 rtime() { return __builtin_amdgcn_s_memrealtime(); }
__device__ __forceinline__ void wait_until(u64 t) { while ((long long)(rtime() - t) < 0) __builtin_amdgcn_s_sleep(1); }
#define FENCE() do { asm volatile("" ::: "memory"); __builtin_amdgcn_sched_barrier(0); } while (0)
#define BAR() do { asm volatile("s_waitcnt lgkmcnt(0)" ::: "memory"); __builtin_amdgcn_s_barrier(); asm volatile("" ::: "memory"); } while (0)
#define LWAIT() asm volatile("s_waitcnt lgkmcnt(0)" ::: "memory")
#define TS(i) do { } while (0)
#define TSV(i, v) do { } while (0)
#define TSDUMP() do { } while (0)

struct __attribute__((aligned(16))) Lds {
  union {
    float obuf[8][128];
    struct { float cbuf[64][8]; u16 xhl[8][2][64]; };
  };
  float ml[8][2];
  unsigned qb[64];
  unsigned csb[64];
  float ybuf[64][8];
  float sbuf[16][8];
  float s12[2][8];
  float cs1p[16][8];
  float xs[64];
  float xfull[512];
  unsigned qrec[8][72];
  float st2[8][4];
  unsigned prb[4];
  u64 tB[2];
  unsigned tBf[2];
  unsigned qflag;
  unsigned pad_[1];
  u32x4 wqs[16][64];
  unsigned char big[8][16384];
};
#define LP(T, field, off) (*(T*)((unsigned char*)&L.field + (off)))

struct Ctx {
  int t, b, w, l, g, c, xg, idx, h, s, cu, u;
  rsrc_t rws;
  int sd;
  const unsigned char* pl;
  int aPr, aA, aAml, aApub, aB0, aB2, aBpub, o_bw, o_br, o_yw2, aC0, aC1, aC2, aCpub, aDg, aDq, aDx;
  int o_ob, o_l8, o_ml, o_yw, o_c32, o_sw, o_cw, o_l32, o_l4, o_xw, o_xr, o_qr, o_st, o_dd, o_xs;
};
constexpr u64 M_C8 = 0x00FF00FF00FF00FFull;

__device__ __forceinline__ void make_frags(Lds& L, const Ctx& X, const u32x4 (&raw)[2], u32x4 (&fr)[2], int base) {
  unsigned char* st = &L.big[X.w][base];
#pragma unroll
  for (int i = 0; i < 2; ++i) *(u32x4*)(st + (32 * i + (X.l >> 1)) * 32 + (X.l & 1) * 16) = raw[i];
  LWAIT();
#pragma unroll
  for (int ci = 0; ci < 2; ++ci)
#pragma unroll
    for (int j2 = 0; j2 < 4; ++j2) {
      const unsigned a = *(const u16*)(st + (32 * ci + 8 * X.g + 2 * j2) * 32 + X.c * 2), bb = *(const u16*)(st + (32 * ci + 8 * X.g + 2 * j2 + 1) * 32 + X.c * 2);
      fr[ci][j2] = a | (bb << 16);
    }
  pin(fr[0]); pin(fr[1]);
}

template <int LAYER, typename HOOK>
__device__ __forceinline__ void pv_publish(Lds& L, const Ctx& X, const Params& p, float tsel, float tmax, float scale, const u32x4 (&vr)[16], const unsigned (&msk)[4], HOOK hook) {
  const int t = X.t, b = X.b, w = X.w, l = X.l;
  (void)b; (void)t;
  const float pe = ex2((tsel - tmax) * scale);
  const unsigned pd = pk2(pe, pe);
  if (LAYER == 0) TS(5);
  f32x4 o0 = {0.f, 0.f, 0.f, 0.f}, o1 = {0.f, 0.f, 0.f, 0.f};
#define PVS(n) { const unsigned pv = dppu<BC + n>(pd); const u32x4 a = {pv & msk[0], pv & msk[1], pv & msk[2], pv & msk[3]}; if (n & 1) o1 = mfma(a, vr[n], o1); else o0 = mfma(a, vr[n], o0); }
  PVS(0) PVS(1) PVS(2) PVS(3) PVS(4) PVS(5) PVS(6) PVS(7) PVS(8) PVS(9) PVS(10) PVS(11) PVS(12) PVS(13) PVS(14) PVS(15)
#undef PVS
  const float lsum = sum64(lo(pd));
  o0 += o1;
  if (l < 32) LP(f32x4, obuf, X.o_ob) = o0;
  if (l == 0) *(f32x2*)&L.ml[w][0] = f32x2{tmax * scale, lsum};
  hook();
  BAR();
  if (LAYER == 0 && w == 3) {
    u64 t0, t1, t2; unsigned d1, d2;
    asm volatile("s_memtime %0\n\ts_waitcnt lgkmcnt(0)" : "=s"(t0) :: "memory");
    asm volatile("s_load_dword %0, %2, 0x0\n\ts_waitcnt lgkmcnt(0)\n\ts_memtime %1\n\ts_waitcnt lgkmcnt(0)" : "=s"(d1), "=s"(t1) : "s"(X.pl) : "memory");
    asm volatile("s_load_dword %0, %2, 0x2000\n\ts_waitcnt lgkmcnt(0)\n\ts_memtime %1\n\ts_waitcnt lgkmcnt(0)" : "=s"(d2), "=s"(t2) : "s"(X.pl) : "memory");
    if (l == 0) { L.prb[0] = (unsigned)(t1 - t0); L.prb[1] = (unsigned)(t2 - t1); }
    LWAIT();
    if (l == 0) __hip_atomic_store(&L.prb[2], 1u, __ATOMIC_RELAXED, __HIP_MEMORY_SCOPE_WORKGROUP);
    TSV(45, (t1 - t0) * 10000 + (t2 - t1));
  }
  if (w == 0) {
    const f32x2 mlu = LP(const f32x2, ml, X.o_ml);
    f32x2 ob[8];
#pragma unroll
    for (int u = 0; u < 8; ++u) ob[u] = LP(const f32x2, obuf[u], X.o_l8);
    const float M = max8l(mlu[0]);
    const float f = ex2(mlu[0] - M);
    const float Ls = sum8l(f * mlu[1]);
    f32x2 o = dpp<BC + 0>(f) * ob[0];
    o += dpp<BC + 1>(f) * ob[1]; o += dpp<BC + 2>(f) * ob[2]; o += dpp<BC + 3>(f) * ob[3];
    o += dpp<BC + 4>(f) * ob[4]; o += dpp<BC + 5>(f) * ob[5]; o += dpp<BC + 6>(f) * ob[6]; o += dpp<BC + 7>(f) * ob[7];
    const unsigned pk = sanu(pk2(o[0], o[1]));
    u32x4 v = {pk, dppu<0x55>(pk), dppu<0xAA>(pk), dppu<0xFF>(pk)};
    if (l == 1) v = u32x4{san(M), san(Ls), 0u, 0u};
    st128x2(X.rws, X.aApub, LAYER * A_SZ, v);
  }
}

template <int LAYER, int NYOUNG, typename HOOK>
__device__ __forceinline__ void wo_stage(Lds& L, Ctx& X, const Params& p, const u32x4 (&wof)[4], HOOK hook) {
  const int t = X.t, b = X.b, w = X.w, l = X.l;
  (void)b; (void)t;
  const unsigned ini = X.c < 8 ? SENT : 0u;
  u32x4 a0 = {ini, ini, ini, ini}; u32x2 mlv = {ini, ini};
  pin(a0); pin(mlv);
  BAR();
  if (LAYER == 0) X.sd = side_from(L.prb[0], L.prb[1]);
  __builtin_amdgcn_s_sleep(SLP_A);
  TS(32 + LAYER * 8);
  gather_42(a0, mlv, X.aA, X.aAml, X.rws, LAYER * A_SZ + X.sd, M_C8);
  FENCE();
  hook();
  FENCE();
  VMWAIT(0, "+v"(a0), "+v"(mlv));
  {
    bool bad = um(mx4(a0), mx2(mlv)) == SENT;
    TSV(33 + LAYER * 8, __any(bad) ? 2 : 1);
    if (UNLIKELY(__any(bad))) {
      for (unsigned sp_ = 0; sp_ < SPIN_MAX; ++sp_) {
        TMO(1 + 10 * LAYER);
        gather_42(a0, mlv, X.aA, X.aAml, X.rws, LAYER * A_SZ + X.sd, __ballot(bad));
        VMWAIT(0, "+v"(a0), "+v"(mlv));
        bad = um(mx4(a0), mx2(mlv)) == SENT;
        if (!__any(bad)) break;
      }
    }
  }
  TS(LAYER * 10 + 8);
  f32x4 d[4];
#pragma unroll
  for (int n = 0; n < 4; ++n) d[n] = mfma(a0, wof[n], f32x4{0.f, 0.f, 0.f, 0.f});
  const float m = asf(mlv.x), ls = asf(mlv.y);
  const float M = max8l(m);
  const float e = ex2(m - M);
  const float f = e * __builtin_amdgcn_rcpf(sum8l(e * ls));
  const bool odd = (X.g & 1) != 0;
  const float F0 = odd ? dpp<BC + 4>(f) : dpp<BC + 0>(f), F1 = odd ? dpp<BC + 5>(f) : dpp<BC + 1>(f), F2 = odd ? dpp<BC + 6>(f) : dpp<BC + 2>(f), F3 = odd ? dpp<BC + 7>(f) : dpp<BC + 3>(f);
  float y[4];
#pragma unroll
  for (int n = 0; n < 4; ++n) y[n] = (F0 * d[n][0] + F1 * d[n][1]) + (F2 * d[n][2] + F3 * d[n][3]);
  const float ya = tr16(y[0], y[1]), yb = tr16(y[2], y[3]);
  if (l < 32) { LP(float, ybuf, X.o_yw) = ya; LP(float, ybuf[32], X.o_yw) = yb; }
  BAR();
  if (w == 0) {
    const float s = sum8(LP(const f32x4, ybuf, X.o_l32), LP(const f32x4, ybuf[0][4], X.o_l32));
    const unsigned pk = sanu(pk2(s, dpp<XOR1>(s)));
    const u32x4 v = {pk, dppu<SHL + 2>(pk), dppu<SHL + 4>(pk), dppu<SHL + 6>(pk)};
    st128x2(X.rws, X.aBpub, LAYER * B_SZ, v);
  }
}

template <int LAYER, typename HOOK>
__device__ __forceinline__ void mlp_stage(Lds& L, Ctx& X, const Params& p, u64 tref, u64 tpub, bool wocu, const u32x4 (&xf)[2], const u32x4 (&wA)[4], float& cs1, const float& b1v, const float& xres, HOOK hook) {
  const int t = X.t, b = X.b, w = X.w, l = X.l;
  (void)b; (void)t;
  u32x4 a0 = {0u, 0u, 0u, 0u}, a1 = a0, a2 = a0, a3 = a0;
  pin(a0); pin(a1); pin(a2); pin(a3);
  const bool mlp = X.u >= 0;
  const u64 m01 = umask(mlp, ~0ull), m2 = umask(w == 7, ~0ull);
  BAR();
  if (LAYER == 0) X.sd = __builtin_amdgcn_readfirstlane(L.prb[3]);
  TSV(47, X.sd + 1);
  wait_until(tref + (mlp ? (LAYER == 0 ? DL_B1 : DL_B2) : DL_X));
  TS(34 + LAYER * 8);
  gather_44s(a0, a1, X.aB0, X.rws, LAYER * B_SZ + X.sd, LAYER * B_SZ + X.sd + 16384, m01);
  gather_44s(a2, a3, X.aB2, X.rws, LAYER * B_SZ + X.sd, LAYER * B_SZ + X.sd + 16384, m2);
  FENCE();
  hook();
  float S1x, s1x, s2x;
  {
    const u32x4 ones = {ONE2, ONE2, ONE2, ONE2};
    const f32x4 z = {0.f, 0.f, 0.f, 0.f};
    f32x4 S0 = mfma(wA[0], xf[0], z), G0 = mfma(xf[0], xf[0], z), s10 = mfma(ones, xf[0], z);
    S0 = mfma(wA[1], xf[1], S0); G0 = mfma(xf[1], xf[1], G0); s10 = mfma(ones, xf[1], s10);
    float S2[2];
#pragma unroll
    for (int j = 0; j < 2; ++j) S2[j] = trd<ROR8>(S0[j], S0[j + 2], (l & 8) != 0);
    S1x = trd<HMIR>(S2[0], S2[1], (l & 4) != 0);
    S1x += dpp<XOR2>(S1x); S1x += dpp<XOR1>(S1x);
    s1x = sum16(s10[0]);
    s2x = sum64(sum4(G0));
    pin(S1x); pin(s1x); pin(s2x);
  }
  FENCE();
  VMWAIT(0, "+v"(a0), "+v"(a1), "+v"(a2), "+v"(a3));
  bool waited;
  {
    bool bad = um(um(mx4(a0), mx4(a1)), um(mx4(a2), mx4(a3))) == SENT;
    TSV(35 + LAYER * 8, __any(bad) ? 2 : 1);
    waited = __any(bad);
    if (UNLIKELY(__any(bad))) {
      for (unsigned sp_ = 0; sp_ < SPIN_MAX; ++sp_) {
        TMO(2 + 10 * LAYER);
        const u64 bm = __ballot(bad);
        gather_44s(a0, a1, X.aB0, X.rws, LAYER * B_SZ + X.sd, LAYER * B_SZ + X.sd + 16384, bm & m01);
        gather_44s(a2, a3, X.aB2, X.rws, LAYER * B_SZ + X.sd, LAYER * B_SZ + X.sd + 16384, bm & m2);
        VMWAIT(0, "+v"(a0), "+v"(a1), "+v"(a2), "+v"(a3));
        bad = um(um(mx4(a0), mx4(a1)), um(mx4(a2), mx4(a3))) == SENT;
        if (!__any(bad)) break;
      }
    }
  }
  if (!mlp && w == 7) {
    const u64 ta = waited ? rtime() + DL_CB : 0ull, tc = wocu ? tpub + DL_CP : 0ull, tb = ta > tc ? ta : tc;
    if (l == 0) { L.tB[LAYER] = tb; } LWAIT(); if (l == 0) __hip_atomic_store(&L.tBf[LAYER], 1u, __ATOMIC_RELAXED, __HIP_MEMORY_SCOPE_WORKGROUP); }
  TS(LAYER * 10 + 10);
  if (mlp) {
    LP(u32x4, big, X.o_bw) = a0; LP(u32x4, big[0][8 * 144], X.o_bw) = a1;
    LWAIT();
    a0 = LP(const u32x4, big, X.o_br); a1 = LP(const u32x4, big[0][64], X.o_br);
    const u32x4 ones = {ONE2, ONE2, ONE2, ONE2};
    const f32x4 z = {0.f, 0.f, 0.f, 0.f};
    f32x4 S = mfma(wA[0], a0, z), G = mfma(a0, a0, z), Gx = mfma(xf[0], a0, z), s1 = mfma(ones, a0, z);
    S = mfma(wA[1], a1, S); G = mfma(a1, a1, G); Gx = mfma(xf[1], a1, Gx); s1 = mfma(ones, a1, s1);
    G += 2.f * Gx;
    float S2[2];
#pragma unroll
    for (int j = 0; j < 2; ++j) S2[j] = trd<ROR8>(S[j], S[j + 2], (l & 8) != 0);
    float S1 = trd<HMIR>(S2[0], S2[1], (l & 4) != 0);
    S1 += dpp<XOR2>(S1); S1 += dpp<XOR1>(S1);
    S1 += S1x;
    const float s2t = sum64(sum4(G)) + s2x;
    const float s1t = sum16(s1[0]) + s1x;
    if ((l & 3) == 0) LP(float, sbuf, X.o_sw) = S1;
    if (l == 0) { L.s12[0][w] = s1t; L.s12[1][w] = s2t; }
    BAR();
    const float St = sum8(LP(const f32x4, sbuf, X.o_c32), LP(const f32x4, sbuf[0][4], X.o_c32));
    if (LAYER == 0) { cs1 = sum8(LP(const f32x4, cs1p, X.o_c32), LP(const f32x4, cs1p[0][4], X.o_c32)); pin(cs1); }
    const float mean = sum8(*(const f32x4*)&L.s12[0][0], *(const f32x4*)&L.s12[0][4]) * (1.f / 512);
    const float ms = sum8(*(const f32x4*)&L.s12[1][0], *(const f32x4*)&L.s12[1][4]) * (1.f / 512);
    const float rstd = __builtin_amdgcn_rsqf(fmx(ms - mean * mean, 0.f) + 1e-5f);
    const float hp = rstd * (St - mean * cs1) + b1v;
    const float uu = hp * (0.7978845608028654f + 0.7978845608028654f * 0.044715f * hp * hp);
    const float ee = ex2(uu * (2.f * 1.4426950408889634f));
    const float hv = hp - hp * __builtin_amdgcn_rcpf(ee + 1.f);
    const unsigned hp2 = pk2(hv, dpp<XOR1>(hv));
    float acc0 = 0.f, acc1 = 0.f;
#define DSTEP(F) acc0 = dot2(dppu<BC + 2 * F>(hp2), wA[2 + (F >> 2)][F & 3], acc0); acc1 = dot2(dppu<BC + 2 * F + 2>(hp2), wA[2 + ((F + 1) >> 2)][(F + 1) & 3], acc1);
    DSTEP(0) DSTEP(2) DSTEP(4) DSTEP(6)
#undef DSTEP
    const unsigned av = san(acc0 + acc1);
    const u32x4 v = {av, dppu<0x55>(av), dppu<0xAA>(av), dppu<0xFF>(av)};
    st128x2(X.rws, X.aCpub, LAYER * C_SZ, v);
    TS(LAYER * 10 + 17);
  }
  if (w == 7) {
    float o[8];
#pragma unroll
    for (int j = 0; j < 4; ++j) { o[2 * j] = lo(a2[j]) + lo(a3[j]); o[2 * j + 1] = hi(a2[j]) + hi(a3[j]); }
    float o4[4], oo2[2];
#pragma unroll
    for (int j = 0; j < 4; ++j) o4[j] = tr32(o[j], o[j + 4]);
#pragma unroll
    for (int j = 0; j < 2; ++j) oo2[j] = tr16(o4[j], o4[j + 2]);
    const float v = trd<ROR8>(oo2[0], oo2[1], (l & 8) != 0);
    LP(float, xs, X.o_xs) = v + xres;
  }
  if (mlp) __builtin_amdgcn_s_sleep(SLP_C);
  else {
    for (unsigned sp_ = 0; sp_ < (SPIN_MAX << 4); ++sp_) { if (__hip_atomic_load(&L.tBf[LAYER], __ATOMIC_RELAXED, __HIP_MEMORY_SCOPE_WORKGROUP) != 0u) break; __builtin_amdgcn_s_sleep(2); }
    asm volatile("" ::: "memory");
    const u64 tb = L.tB[LAYER];
    wait_until(uni64(tb));
  }
}

template <int LAYER, typename HOOK>
__device__ __forceinline__ float c_gather(Lds& L, const Ctx& X, const Params& p, HOOK hook) {
  const int t = X.t, b = X.b, w = X.w;
  (void)b; (void)t; (void)w;
  u32x4 a0, a1, a2;
  asm volatile("" : "=v"(a0), "=v"(a1), "=v"(a2));
  TS(36 + LAYER * 8);
  gather_444(a0, a1, a2, X.aC0, X.aC1, X.aC2, X.rws, LAYER * C_SZ + X.sd, ~0ull);
  FENCE();
  hook();
  FENCE();
  VMWAIT(0, "+v"(a0), "+v"(a1), "+v"(a2));
  {
    bool bad = um(um(mx4(a0), mx4(a1)), mx4(a2)) == SENT;
    TSV(37 + LAYER * 8, __any(bad) ? 2 : 1);
    if (UNLIKELY(__any(bad))) {
      for (unsigned sp_ = 0; sp_ < SPIN_MAX; ++sp_) {
        TMO(3 + 10 * LAYER);
        gather_444(a0, a1, a2, X.aC0, X.aC1, X.aC2, X.rws, LAYER * C_SZ + X.sd, __ballot(bad));
        VMWAIT(0, "+v"(a0), "+v"(a1), "+v"(a2));
        bad = um(um(mx4(a0), mx4(a1)), mx4(a2)) == SENT;
        if (!__any(bad)) break;
      }
    }
  }
  TS(LAYER * 10 + 12);
  float v[4];
#pragma unroll
  for (int j = 0; j < 4; ++j) v[j] = asf(a0[j]) + asf(a1[j]) + asf(a2[j]);
  const float v0 = tr32(v[0], v[2]), v1 = tr32(v[1], v[3]);
  LP(float, cbuf, X.o_cw) = tr16(v0, v1);
  BAR();
  return sum8(LP(const f32x4, cbuf, X.o_l32), LP(const f32x4, cbuf[0][4], X.o_l32)) + LP(const float, xs, X.o_l4);
}

__global__ void __launch_bounds__(NTHR, 2) fused_kernel(Params p) {
  asm volatile("" : "+s"(p.x), "+s"(p.K), "+s"(p.V), "+s"(p.wq), "+s"(p.wo), "+s"(p.w1), "+s"(p.b1), "+s"(p.w2), "+s"(p.b2), "+s"(p.out), "+s"(p.ws));
  __shared__ Lds L;
  Ctx X;
  const int t = threadIdx.x, b = blockIdx.x;
  const int w = __builtin_amdgcn_readfirstlane(t >> 6), l = t & 63, g = l >> 4, c = l & 15;
  const int xg = b & 7, idx = b >> 3, s = idx & 7, cu = xg * 32 + idx;
  const int hg = cu >> 3;
  const int h = hg;
  const bool wocu = idx >= 16;
  const int wpp = (idx >> 3) & 1, wj = idx & 7;
  int u = idx < 12 ? 4 * (xg + 8 * (idx >> 2)) + (idx & 3) : -1;
  if (u >= NU) u = -1;
  TS(0);
  unsigned char* const wsa = (unsigned char*)(((size_t)p.ws + (2u << 20)) & ~(size_t)((2u << 20) - 1));
  const rsrc_t rws = mk(wsa, WS_END), rep = mk(p.ws, 8);
  X.t = t; X.b = b; X.w = w; X.l = l; X.g = g; X.c = c; X.xg = xg; X.idx = idx; X.h = h; X.s = s; X.cu = cu; X.u = u; X.rws = rws;
  const rsrc_t rK = mk(p.K + (size_t)(h * 8 + s) * 65536, 131072), rV = mk(p.V + (size_t)(h * 8 + s) * 65536, 131072);
  const rsrc_t rx = mk(p.x, 1024), rwq = mk(p.wq, DM * NH * DH * 2), rwo = mk(p.wo, wocu ? DM * NH * DH * 2 : 0);
  const unsigned mz = u >= 0 ? 1u : 0u;
  const rsrc_t rw1 = mk(p.w1, mz * DM * FF * 2), rw2 = mk(p.w2, mz * DM * FF * 2), rb1 = mk(p.b1, mz * FF * 2), rb2 = mk(p.b2, DM * 2);
  const rsrc_t rd1 = mk(wsa + WS_D1, w == 0 ? NCU * 1024 : 0);
  unsigned msk[4];
#pragma unroll
  for (int i = 0; i < 4; ++i) msk[i] = (c >> 1) == i ? ((c & 1) ? 0xFFFF0000u : 0x0000FFFFu) : 0u;
  const u32x2 ep = ld64<0>(rep, 0);
  if (w == 0) {
    const u32x4 xv = ld128<0>(rx, l * 16);
    const unsigned xq = ld16<0>(rx, (64 * s + 4 * c + g) * 2);
    u32x4 wqr[16];
#pragma unroll
    for (int n = 0; n < 16; ++n) wqr[n] = ld128<W_AUX>(rwq, (64 * s + g) * 8192 + h * 256 + c * 16, n * 4 * 8192);
    FENCE();
    __builtin_amdgcn_s_barrier();
    FENCE();
    float xs[8];
#pragma unroll
    for (int j = 0; j < 4; ++j) { xs[2 * j] = lo(xv[j]); xs[2 * j + 1] = hi(xv[j]); }
    const float mean = sum64(((xs[0] + xs[1]) + (xs[2] + xs[3])) + ((xs[4] + xs[5]) + (xs[6] + xs[7]))) * (1.f / 512);
    float d2 = 0.f;
#pragma unroll
    for (int j = 0; j < 8; ++j) { const float d = xs[j] - mean; d2 += d * d; }
    const float rstd = __builtin_amdgcn_rsqf(sum64(d2) * (1.f / 512) + 1e-5f);
    const float xn = (lo(xq) - mean) * rstd;
    const unsigned pd = pk2(xn, xn);
    const u32x4 onesA = {ONE2 & msk[0], ONE2 & msk[1], ONE2 & msk[2], ONE2 & msk[3]};
    f32x4 aq = {0.f, 0.f, 0.f, 0.f}, ac = {0.f, 0.f, 0.f, 0.f};
#define QST(n) { const unsigned pv = dppu<BC + n>(pd); const u32x4 a = {pv & msk[0], pv & msk[1], pv & msk[2], pv & msk[3]}; aq = mfma(a, wqr[n], aq); ac = mfma(onesA, wqr[n], ac); L.wqs[n][l] = wqr[n]; }
    QST(0) QST(1) QST(2) QST(3) QST(4) QST(5) QST(6) QST(7) QST(8) QST(9) QST(10) QST(11) QST(12) QST(13) QST(14) QST(15)
#undef QST
    const unsigned tag = ep.x + 1u;
    if (l < 32) {
      const int off = WS_D1 + cu * 1024 + (8 * c + 4 * g) * 8;
      st128(rws, off, 0, u32x4{pk2(aq[0], ac[0]), tag, pk2(aq[1], ac[1]), tag});
      st128(rws, off + 16, 0, u32x4{pk2(aq[2], ac[2]), tag, pk2(aq[3], ac[3]), tag});
    }
    TS(1);
  }
  if (w == 1) {
#pragma unroll
    for (int k = 0; k < 6; ++k) {
      const int q = l + 64 * k;
      int off = OOB;
      if (q < 17) off = aoff(cu) + q * 16;
      else if (q < 34) off = A_SZ + aoff(cu) + (q - 17) * 16;
      else if (q >= 40 && q < 56 && wocu) off = WS_B + ((q - 40) >> 3) * B_SZ + xg * 0 + (((2 * xg + wpp) >> 3) * 16384) + ((2 * xg + wpp) & 7) * 1024 + wj * 128 + (q & 7) * 16;
      else if (q >= 64 && q < 80) off = doff(cu) + ((q - 64) >> 1) * 128 + (q & 1) * 16;
      else if (q >= 80 && q < 97) off = doff(cu) + 1024 + (q - 80) * 16;
      else if (q == 97) off = doff(cu) + 1408;
      else if (q >= 128 && u >= 0) off = WS_C + ((q - 128) >> 7) * C_SZ + (u >> 2) * 16384 + (u & 3) * 2048 + ((q - 128) & 127) * 16;
      st128x2(rws, off, 0, u32x4{SENT, SENT, SENT, SENT});
    }
    FENCE();
    __builtin_amdgcn_s_barrier();
    asm volatile("s_waitcnt vmcnt(0)" ::: "memory");
    const u64 e1 = (((u64)ep.y << 32) | ep.x) + 1;
    if (l == 0) __builtin_amdgcn_raw_buffer_store_b64(u32x2{(unsigned)e1, (unsigned)(e1 >> 32)}, rws, WS_FLAGS + cu * 8, 0, SC1);
    TS(4);
  }
  if (t == 128) { L.qflag = 0u; L.prb[2] = 0u; L.tBf[0] = 0u; L.tBf[1] = 0u; }
  if (w >= 2) { LWAIT(); __builtin_amdgcn_s_barrier(); }
  FENCE();
  u32x4 kf[16], vr[16];
  {
    int vo[4];
#pragma unroll
    for (int j = 0; j < 4; ++j) vo[j] = w * 16384 + (4 * j + g) * 256 + ((c ^ (4 * j + g)) * 16);
    const unsigned ldsb = (unsigned)(size_t)(__attribute__((address_space(3))) unsigned char*)&L.big[0][0] + w * 16384;
    unsigned keep;
#define KD(j, so) "s_nop 0\n\tbuffer_load_dwordx4 %[v" #j "], %[rs], " so " offen" KVNT " lds\n\ts_add_u32 m0, m0, 0x400\n\t"
#define KD4(so) KD(0, so) KD(1, so) KD(2, so) KD(3, so)
    asm volatile("s_mov_b32 %[keep], m0\n\ts_mov_b32 m0, %[lb]\n\t" KD4("0") KD4("%[s1]") KD4("%[s2]") KD4("%[s3]") "s_mov_b32 m0, %[keep]"
                 : [keep] "=&s"(keep)
                 : [v0] "v"(vo[0]), [v1] "v"(vo[1]), [v2] "v"(vo[2]), [v3] "v"(vo[3]), [rs] "s"(rK), [lb] "s"(ldsb), [s1] "s"(4096), [s2] "s"(8192), [s3] "s"(12288)
                 : "memory", "scc");
#undef KD4
#undef KD
  }
  FENCE();
  u32x4 d1[8];
#pragma unroll
  for (int i = 0; i < 8; ++i) d1[i] = ld128<SC1>(rd1, hg * 8192 + l * 16, i * 1024);
  FENCE();
  {
    const int uc0 = u >= 0 ? u : 0;
    const int rec = aoff(xg * 32 + (2 * wpp + (w >> 2)) * 8 + (l & 7));
    X.aA = rec + ((w & 3) * 32 + 8 * g) * 2; X.aAml = rec + 256;
    X.aApub = (l & 3) == 0 ? aoff(cu) + l * 4 : l == 1 ? aoff(cu) + 256 : OOB;
    X.aB0 = WS_B + (l >> 3) * 1024 + w * 128 + (l & 7) * 16;
    X.aB2 = WS_B + (l >> 3) * 1024 + s * 128 + (l & 7) * 16;
    X.aBpub = (l & 7) == 0 ? WS_B + ((2 * xg + wpp) >> 3) * 16384 + ((2 * xg + wpp) & 7) * 1024 + wj * 128 + l * 2 : OOB;
    X.o_bw = w * 16384 + 8192 + (l >> 3) * 144 + (l & 7) * 16; X.o_br = w * 16384 + 8192 + c * 144 + g * 16;
    pin(X.o_bw); pin(X.o_br);
    const int src = w * 4 + g, cc = WS_C + w * 16384 + g * 2048 + (64 * s + 4 * c) * 4;
    X.aC0 = cc; X.aC1 = cc + 8 * 16384; X.aC2 = src + 64 < NU ? cc + 16 * 16384 : OOB;
    X.aCpub = (l & 3) == 0 ? WS_C + (uc0 >> 2) * 16384 + (uc0 & 3) * 2048 + t * 4 : OOB;
    X.aPr = doff(cu) + 1408;
    X.pl = wsa + doff(cu) + 1408;
    if (w == 3) {
      unsigned d0;
      asm volatile("s_load_dword %0, %1, 0x0\n\ts_waitcnt lgkmcnt(0)" : "=s"(d0) : "s"(wsa + WS_A + (cu / 21) * 16384 + 8064) : "memory");
    }
    X.aDg = doff(hg * 8 + w) + (l < 16 ? (l >> 1) * 128 + (l & 1) * 16 : l < 32 ? 1024 + (l - 16) * 16 : 1280);
    X.aDq = (l == 0 || l == 8) ? doff(cu) + w * 128 + l * 2 : OOB;
    X.aDx = w == 0 ? ((l & 3) == 0 ? doff(cu) + 1024 + l * 4 : OOB) : (l == 0 ? doff(cu) + 1280 : OOB);
    pin(X.aA); pin(X.aAml); pin(X.aApub); pin(X.aB0); pin(X.aB2); pin(X.aBpub); pin(X.aC0); pin(X.aC1); pin(X.aC2); pin(X.aCpub); pin(X.aDg); pin(X.aDq); pin(X.aDx);
    X.o_ob = (w * 128 + 8 * c + 4 * g) * 4; X.o_l8 = l * 8; X.o_ml = (l & 7) * 8;
    X.o_yw = ((16 * (g & 1) + c) * 8 + ((w + c) & 7)) * 4; X.o_c32 = c * 32;
    X.o_sw = ((4 * g + ((l >> 3) & 1) * 2 + ((l >> 2) & 1)) * 8 + ((w + 2 * g) & 7)) * 4;
    X.o_cw = ((4 * c + g) * 8 + ((w + c) & 7)) * 4; X.o_l32 = l * 32; X.o_l4 = l * 4;
    X.o_xw = w * 256 + l * 2; X.o_xr = w * 256 + (c & 1) * 128 + g * 16;
    X.o_qr = (l & 7) * 288 + g * 16; X.o_st = (l & 7) * 16;
    X.o_dd = l < 16 ? (int)__builtin_offsetof(Lds, qrec) + w * 288 + l * 16 : l < 32 ? (int)__builtin_offsetof(Lds, xfull) + w * 256 + (l - 16) * 16 : (int)__builtin_offsetof(Lds, st2) + w * 16;
    X.o_xs = (8 * (l & 7) + (l >> 3)) * 4;
    pin(X.o_ob); pin(X.o_yw); pin(X.o_sw); pin(X.o_cw); pin(X.o_xr); pin(X.o_qr); pin(X.o_dd); pin(X.o_xs);
  }
  TS(28);
  LWAIT();
  __builtin_amdgcn_s_barrier();
  FENCE();
#pragma unroll
  for (int i = 0; i < 16; ++i) vr[i] = ld128<KV_AUX>(rV, w * 16384 + g * 4096 + c * 16, i * 256);
  FENCE();
  TS(30);
  const u64 epoch = (((u64)ep.y << 32) | ep.x) + 1;
  asm volatile("s_waitcnt vmcnt(24)" ::: "memory");
  {
    const unsigned char* st = L.big[w];
#pragma unroll
    for (int T = 0; T < 4; ++T)
#pragma unroll
      for (int i = 0; i < 4; ++i) kf[4 * T + i] = *(const u32x4*)(st + (16 * T + c) * 256 + (((4 * i + g) ^ c) * 16));
  }
  TS(2);
  if (w == 0) {
    const unsigned tag = (unsigned)epoch;
    unsigned bad = 0;
#pragma unroll
    for (int i = 0; i < 8; ++i) bad |= (d1[i].y ^ tag) | (d1[i].w ^ tag);
    if (UNLIKELY(!__all(bad == 0u))) {
      for (unsigned sp_ = 0; sp_ < SPIN_MAX; ++sp_) {
        asm volatile("" ::: "memory");
        bad = 0;
#pragma unroll
        for (int i = 0; i < 8; ++i) { d1[i] = ld128<SC1>(rd1, hg * 8192 + l * 16, i * 1024); bad |= (d1[i].y ^ tag) | (d1[i].w ^ tag); }
        if (__all(bad == 0u)) break;
      }
    }
    float qa = 0.f, qc = 0.f, ca = 0.f, cb = 0.f;
#pragma unroll
    for (int i = 0; i < 8; ++i) { qa += lo(d1[i].x); ca += hi(d1[i].x); qc += lo(d1[i].z); cb += hi(d1[i].z); }
    L.qb[l] = pk2(qa, qc);
    L.csb[l] = pk2(ca, cb);
    LWAIT();
    if (l == 0) __hip_atomic_store(&L.qflag, 1u, __ATOMIC_RELAXED, __HIP_MEMORY_SCOPE_WORKGROUP);
  } else {
    for (unsigned sp_ = 0; sp_ < (SPIN_MAX << 4); ++sp_) {
      if (__hip_atomic_load(&L.qflag, __ATOMIC_RELAXED, __HIP_MEMORY_SCOPE_WORKGROUP) != 0u) break;
      __builtin_amdgcn_s_sleep(1);
    }
  }
  asm volatile("" ::: "memory");
  TS(3);

  float kcs;
  u32x4 rA[4], wA[4], wqB[2], xf[2];
  unsigned rB[8], b1r, xres_r, b2_r;
  float cs1 = 0.f, b1v = 0.f, b2v = 0.f, xres1 = 0.f;
  const int uc = u >= 0 ? u : 0;
  const int xcol = 8 * (l & 7) + (l >> 3);
  {
    f32x4 sc[4];
#pragma unroll
    for (int T = 0; T < 4; ++T) sc[T] = f32x4{0.f, 0.f, 0.f, 0.f};
#pragma unroll
    for (int i = 0; i < 4; ++i) {
      const u32x4 qf = *(const u32x4*)&L.qb[16 * i + 4 * g];
#pragma unroll
      for (int T = 0; T < 4; ++T) sc[T] = mfma(qf, kf[4 * T + i], sc[T]);
    }
    const float tmax = max16(fmx(fmx3(sc[0][0], sc[1][0], sc[2][0]), sc[3][0]));
    const float tsel = sel4(g, sc[0][0], sc[1][0], sc[2][0], sc[3][0]);
    const rsrc_t rfl = mk(wsa + WS_FLAGS, w >= 6 ? 2048 : 0);
    u32x4 fl = ld128<SC1>(rfl, (t & 127) * 16);
    FENCE();
    FENCE();
    FENCE();
    pv_publish<0>(L, X, p, tsel, tmax, QS, vr, msk, [&]() {
      if (wocu) {
#pragma unroll
        for (int i = 0; i < 4; ++i) rA[i] = ld128<W_AUX>(rwo, ((4 * xg + 2 * wpp + (w >> 2)) * 128 + 32 * (w & 3) + 8 * i + (l >> 3)) * 1024 + wj * 128 + (l & 7) * 16);
      }
      const unsigned e0 = (unsigned)epoch, e1 = (unsigned)(epoch >> 32);
      if (UNLIKELY(w >= 6 && !__all(((fl.x ^ e0) | (fl.y ^ e1) | (fl.z ^ e0) | (fl.w ^ e1)) == 0u))) {
        for (unsigned sp_ = 0; sp_ < SPIN_MAX; ++sp_) {
          __builtin_amdgcn_s_sleep(16);
          asm volatile("" ::: "memory");
          fl = ld128<SC1>(rws, WS_FLAGS + (t & 127) * 16);
          TMO(5);
          if (__all(((fl.x ^ e0) | (fl.y ^ e1) | (fl.z ^ e0) | (fl.w ^ e1)) == 0u)) break;
        }
      }
    });
  }
  u64 tref = rtime();
  b1r = ld16<0>(rb1, (16 * uc + c) * 2);
  xres_r = ld16<0>(rx, (64 * s + xcol) * 2); b2_r = ld16<0>(rb2, (64 * s + xcol) * 2);
#pragma unroll
  for (int ci = 0; ci < 2; ++ci) xf[ci] = ld128<0>(rx, c == 0 ? (64 * w + 32 * ci + 8 * g) * 2 : 1024);
  if (!wocu) {
#pragma unroll
    for (int i = 0; i < 2; ++i) rA[i] = ld128<W_AUX>(rw1, (64 * w + 32 * i + (l >> 1)) * (FF * 2) + uc * 32 + (l & 1) * 16);
#pragma unroll
    for (int f = 0; f < 8; ++f) rA[2 + (f >> 2)][f & 3] = ld32<W_AUX>(rw2, (16 * uc + f) * 1024 + (t >> 1) * 4);
#pragma unroll
    for (int f = 0; f < 8; ++f) rB[f] = ld32<W_AUX>(rw2, (16 * uc + 8 + f) * 1024 + (t >> 1) * 4);
  }
  TS(6);
  if (wocu) {
    wo_stage<0, 0>(L, X, p, wA, [&]() {
      {
        unsigned char* st = L.big[w];
#pragma unroll
        for (int i = 0; i < 4; ++i) *(u32x4*)(st + (8 * i + (l >> 3)) * 128 + (l & 7) * 16) = rA[i];
        LWAIT();
#pragma unroll
        for (int n = 0; n < 4; ++n) {
          typedef short s16x4 __attribute__((ext_vector_type(4)));
          const unsigned base = (unsigned)(size_t)(__attribute__((address_space(3))) unsigned char*)st + (8 * g + (c >> 2)) * 128 + (16 * n + 4 * (c & 3)) * 2;
          const s16x4 lo4 = __builtin_amdgcn_ds_read_tr16_b64_v4i16((__attribute__((address_space(3))) s16x4*)(size_t)base), hi4 = __builtin_amdgcn_ds_read_tr16_b64_v4i16((__attribute__((address_space(3))) s16x4*)(size_t)(base + 512));
          const u32x2 l2 = __builtin_bit_cast(u32x2, lo4), h2 = __builtin_bit_cast(u32x2, hi4);
          wA[n] = u32x4{l2.x, l2.y, h2.x, h2.y}; pin(wA[n]);
        }
      }
      pin(wA[0]); pin(wA[1]); pin(wA[2]); pin(wA[3]);
    });
  }
  const u64 tpubB = rtime();
  b2v = lo(b2_r); xres1 = lo(xres_r) + b2v; pin(b2v); pin(xres1);
  TS(9);
  if (!wocu) {
    {
      const u32x4 w1r[2] = {rA[0], rA[1]};
      u32x4 fr[2];
      make_frags(L, X, w1r, fr, 2048);
      wA[0] = fr[0]; wA[1] = fr[1];
    }
#pragma unroll
    for (int f = 0; f < 8; ++f) {
      const unsigned r0 = f < 4 ? rA[2 + (f >> 1)][(2 * f) & 3] : rB[2 * f - 8], r1 = f < 4 ? rA[2 + (f >> 1)][(2 * f + 1) & 3] : rB[2 * f - 7];
      wA[2 + (f >> 2)][f & 3] = (t & 1) ? __builtin_amdgcn_perm(r1, r0, 0x07060302u) : __builtin_amdgcn_perm(r1, r0, 0x05040100u);
    }
    pin(wA[0]); pin(wA[1]); pin(wA[2]); pin(wA[3]);
    b1v = lo(b1r); pin(b1v);
    const u32x4 ones = {ONE2, ONE2, ONE2, ONE2};
    f32x4 S = mfma(wA[0], ones, f32x4{0.f, 0.f, 0.f, 0.f});
    S = mfma(wA[1], ones, S);
    if (c == 0) {
#pragma unroll
      for (int i = 0; i < 4; ++i) L.cs1p[4 * g + i][w] = S[i];
    }
  }
  if (w == 6) {
    unsigned pa, pb;
    {
      LWAIT();
      while (__hip_atomic_load(&L.prb[2], __ATOMIC_RELAXED, __HIP_MEMORY_SCOPE_WORKGROUP) == 0u) __builtin_amdgcn_s_sleep(1);
      const unsigned p0 = L.prb[0], p1 = L.prb[1];
      const bool near0 = p0 > 250u && p0 < PCLEAN && p1 > p0 + 100u, near1 = p1 > 250u && p1 < PCLEAN && p0 > p1 + 100u;
      pa = near0 ? 1u : 2u; pb = near0 ? 2u : 1u;
      if (near0 || near1) goto probed;
      pa = p0; pb = p1;
      if (wocu) goto probed;
    }
    for (int tries = 0; tries < 8; ++tries) {
      u64 t0, t1, t2;
      st128x2(rws, l == 0 ? X.aPr : OOB, 0, u32x4{SENT, SENT, SENT, SENT});
      asm volatile("s_waitcnt vmcnt(0)\n\ts_memtime %0\n\ts_waitcnt lgkmcnt(0)" : "=s"(t0) :: "memory");
      const unsigned v0 = ld32<SC1>(rws, X.aPr, 0);
      asm volatile("s_waitcnt vmcnt(0)\n\ts_memtime %0\n\ts_waitcnt lgkmcnt(0)" : "=s"(t1) : "v"(v0) : "memory");
      const unsigned v1 = ld32<SC1>(rws, X.aPr, CPY);
      asm volatile("s_waitcnt vmcnt(0)\n\ts_memtime %0\n\ts_waitcnt lgkmcnt(0)" : "=s"(t2) : "v"(v1) : "memory");
      pa = (unsigned)(t1 - t0); pb = (unsigned)(t2 - t1);
      const unsigned mn = pa < pb ? pa : pb, mxp = pa < pb ? pb : pa;
      if ((mn < 600u && mxp > mn + 90u && mxp < 1000u) || (long long)(rtime() - (tref + PDL)) > 0) break;
    }
  probed:
    if (l == 0) { L.prb[3] = pa < pb ? 0u : (unsigned)CPY; }
    TSV(46, pa * 10000 + pb);
  }
  mlp_stage<0>(L, X, p, tref, tpubB, wocu, xf, wA, cs1, b1v, xres1, []() {});
  TS(11);
  {
    const float x2 = c_gather<0>(L, X, p, [&]() {
      const unsigned char* st = (const unsigned char*)&L.wqs[0][0];
#pragma unroll
      for (int ci = 0; ci < 2; ++ci)
#pragma unroll
        for (int j2 = 0; j2 < 4; ++j2) {
          const int d0 = 32 * ci + 8 * g + 2 * j2;
          const unsigned a = *(const u16*)(st + (d0 >> 2) * 1024 + (d0 & 3) * 256 + (16 * w + c) * 2), bb = *(const u16*)(st + ((d0 + 1) >> 2) * 1024 + ((d0 + 1) & 3) * 256 + (16 * w + c) * 2);
          wqB[ci][j2] = a | (bb << 16);
        }
      pin(wqB[0]); pin(wqB[1]);
    });
    const unsigned hh = pk2(x2, x2);
    const unsigned ll = pk2(x2 - lo(hh), 0.f);
    LP(u16, xhl, X.o_xw) = (u16)hh; LP(u16, xhl[0][1], X.o_xw) = (u16)ll;
    LWAIT();
    const u32x4 a0 = LP(const u32x4, xhl, X.o_xr), a1 = LP(const u32x4, xhl[0][0][32], X.o_xr);
    f32x4 d = mfma(a0, wqB[0], f32x4{0.f, 0.f, 0.f, 0.f});
    d = mfma(a1, wqB[1], d);
    const float q1 = d[0] + d[1];
    const unsigned pk = sanu(pk2(q1, dpp<XOR1>(q1)));
    const u32x4 v = {pk, dppu<SHL + 2>(pk), dppu<SHL + 4>(pk), dppu<SHL + 6>(pk)};
    st128x2(rws, X.aDq, 0, v);
    if (w == 0) {
      const unsigned xu = san(x2);
      const u32x4 vx = {xu, dppu<0x55>(xu), dppu<0xAA>(xu), dppu<0xFF>(xu)};
      st128x2(rws, X.aDx, 0, vx);
    } else if (w == 1) {
      const u32x4 ones = {ONE2, ONE2, ONE2, ONE2};
      const f32x4 z = {0.f, 0.f, 0.f, 0.f};
      f32x4 G = mfma(a0, a0, z), s1 = mfma(a0, ones, z);
      G = mfma(a1, a1, G); s1 = mfma(a1, ones, s1);
      float s2 = G[0] + G[1]; s2 += dpp<XOR1>(s2);
      st128x2(rws, X.aDx, 0, u32x4{san(s1[0] + s1[1]), san(s2), 0u, 0u});
    }
  }
  TS(13);
  {
    u32x4 r4 = {0u, 0u, 0u, 0u};
    pin(r4);
    BAR();
    __builtin_amdgcn_s_sleep(SLP_D);
    TS(38);
    gather_4(r4, X.aDg, rws, X.sd, 0x1FFFFFFFFull);
    FENCE();
    {
      f32x4 sk[4];
#pragma unroll
      for (int T = 0; T < 4; ++T) sk[T] = f32x4{0.f, 0.f, 0.f, 0.f};
#pragma unroll
      for (int i = 0; i < 4; ++i) {
        const u32x4 cf = *(const u32x4*)&L.csb[16 * i + 4 * g];
#pragma unroll
        for (int T = 0; T < 4; ++T) sk[T] = mfma(cf, kf[4 * T + i], sk[T]);
      }
      kcs = sel4(g, sk[0][0], sk[1][0], sk[2][0], sk[3][0]); pin(kcs);
    }
    FENCE();
    VMWAIT(0, "+v"(r4));
    {
      bool bad = mx4(r4) == SENT;
      TSV(39, __any(bad) ? 2 : 1);
      if (UNLIKELY(__any(bad))) {
        for (unsigned sp_ = 0; sp_ < SPIN_MAX; ++sp_) {
          TMO(4);
          gather_4(r4, X.aDg, rws, X.sd, __ballot(bad));
          VMWAIT(0, "+v"(r4));
          bad = mx4(r4) == SENT;
          if (!__any(bad)) break;
        }
      }
    }
    TS(14);
    if (l <= 32) *(u32x4*)((unsigned char*)&L + X.o_dd) = r4;
    BAR();
    u32x4 qf[4];
#pragma unroll
    for (int i = 0; i < 4; ++i) qf[i] = LP(const u32x4, qrec[0][16 * i], X.o_qr);
    const f32x2 st2 = LP(const f32x2, st2, X.o_st);
    f32x4 sc[4];
#pragma unroll
    for (int T = 0; T < 4; ++T) sc[T] = f32x4{0.f, 0.f, 0.f, 0.f};
#pragma unroll
    for (int i = 0; i < 4; ++i)
#pragma unroll
      for (int T = 0; T < 4; ++T) sc[T] = mfma(qf[i], kf[4 * T + i], sc[T]);
    const float mean = sum8l(st2[0]) * (1.f / 512);
    const float ms = sum8l(st2[1]) * (1.f / 512);
    const float scale = __builtin_amdgcn_rsqf(fmx(ms - mean * mean, 0.f) + 1e-5f) * QS;
    const float ta = tr16(sum4(sc[0]), sum4(sc[1])), tb = tr16(sum4(sc[2]), sum4(sc[3]));
    const float tsel = ((g & 2) ? tb : ta) - mean * kcs;
    const float tmax = mx32r(mx16r(max16(tsel)));
    TS(15);
    pv_publish<1>(L, X, p, tsel, tmax, scale, vr, msk, []() {});
  }
  tref = rtime();
  TS(16);
  float xres2 = 0.f;
  auto shadow2 = [&]() {
    xres2 = L.xfull[64 * s + xcol] + b2v; pin(xres2);
    if (!wocu) {
#pragma unroll
      for (int ci = 0; ci < 2; ++ci) {
        const f32x4 p0 = *(const f32x4*)&L.xfull[64 * w + 32 * ci + 8 * g], p1 = *(const f32x4*)&L.xfull[64 * w + 32 * ci + 8 * g + 4];
        const float xv8[8] = {p0[0], p0[1], p0[2], p0[3], p1[0], p1[1], p1[2], p1[3]};
#pragma unroll
        for (int j = 0; j < 4; ++j) {
          const unsigned hh = pk2(xv8[2 * j], xv8[2 * j + 1]);
          const unsigned ll = pk2(xv8[2 * j] - lo(hh), xv8[2 * j + 1] - hi(hh));
          xf[ci][j] = c == 0 ? hh : c == 1 ? ll : 0u;
        }
      }
      pin(xf[0]); pin(xf[1]);
    }
  };
  if (wocu) wo_stage<1, 0>(L, X, p, wA, shadow2); else shadow2();
  const u64 tpubB2 = rtime();
  TS(19);
  const bool fin = (xg == 0 && idx >= 24);
  if (u < 0 && !fin) { TSDUMP(); return; }
  mlp_stage<1>(L, X, p, tref, tpubB2, wocu, xf, wA, cs1, b1v, xres2, []() {});
  TS(21);
  if (!fin) { TSDUMP(); return; }
  const float o = c_gather<1>(L, X, p, []() {});
  if (w == 0) {
    p.out[64 * s + l] = (u16)(pk2(o, o) & 0xffffu);
    if (s == 0 && l == 0) *(u64*)p.ws = epoch;
  }
  TS(23);
  TSDUMP();
}

extern "C" void kernel_launch(void* const* d_in, const int* in_sizes, int n_in, void* d_out, int out_size, void* d_ws, size_t ws_size, hipStream_t stream) {
  Params p;
  p.x = (const u16*)d_in[0]; p.K = (const u16*)d_in[1]; p.V = (const u16*)d_in[2]; p.wq = (const u16*)d_in[3]; p.wo = (const u16*)d_in[4];
  p.w1 = (const u16*)d_in[6]; p.b1 = (const u16*)d_in[7]; p.w2 = (const u16*)d_in[8]; p.b2 = (const u16*)d_in[9];
  p.out = (u16*)d_out; p.ws = (unsigned char*)d_ws;
  hipLaunchKernelGGL(fused_kernel, dim3(NCU), dim3(NTHR), 0, stream, p);
}
```

```cpp
#include <hip/hip_runtime.h>
#include <stdint.h>
typedef unsigned short u16;
typedef unsigned u32x4 __attribute__((ext_vector_type(4)));
constexpr int D = 512, H = 32, SK = 4096, DH = 128, F = 1376;
#define NBLK 1
#define LDSPAD 16
__device__ __forceinline__ float bf2f(u16 b) { return __builtin_bit_cast(float, (unsigned)b << 16); }
__device__ __forceinline__ float lo(unsigned w) { return __builtin_bit_cast(float, w << 16); }
__device__ __forceinline__ float hi(unsigned w) { return __builtin_bit_cast(float, w & 0xffff0000u); }
__device__ __forceinline__ u16 f2bf(float f) { unsigned u = __builtin_bit_cast(unsigned, f); return (u16)((u + 0x7fffu + ((u >> 16) & 1u)) >> 16); }
__device__ float bsum(float v, float* red) { for (int o = 32; o > 0; o >>= 1) v += __shfl_xor(v, o); __syncthreads(); if ((threadIdx.x & 63) == 0) red[threadIdx.x >> 6] = v; __syncthreads(); float s = 0; for (int i = 0; i < 8; ++i) s += red[i]; return s; }
__device__ float bmax(float v, float* red) { for (int o = 32; o > 0; o >>= 1) v = fmaxf(v, __shfl_xor(v, o)); __syncthreads(); if ((threadIdx.x & 63) == 0) red[threadIdx.x >> 6] = v; __syncthreads(); float s = red[0]; for (int i = 1; i < 8; ++i) s = fmaxf(s, red[i]); return s; }
__global__ void __launch_bounds__(512) k_one(const u16* x, const u16* K, const u16* V, const u16* wq, const u16* wo, const u16* w1, const u16* b1, const u16* w2, const u16* b2, u16* out) {
  const unsigned long long t0 = __builtin_amdgcn_s_memrealtime();
  __shared__ float xs[512], xn[512], q[128], o[4096], sc[4096], hh[1376], red[8], acc[32][128];
  __shared__ char pad[LDSPAD];
  const int t = threadIdx.x;
  if (blockIdx.x == 0) {
  pad[t % LDSPAD] = 1;
  xs[t] = bf2f(x[t]);
  __syncthreads();
  for (int L = 0; L < 2; ++L) {
    { float m = bsum(xs[t], red) / 512; float d = xs[t] - m; float v = bsum(d * d, red) / 512; xn[t] = d / sqrtf(v + 1e-5f); __syncthreads(); }
    for (int h = 0; h < H; ++h) {
      { int c = t & 127, r0 = (t >> 7) * 128; float a = 0; for (int r = r0; r < r0 + 128; ++r) a += xn[r] * bf2f(wq[(size_t)r * 4096 + h * 128 + c]); acc[t >> 7][c] = a; __syncthreads(); if (t < 128) q[t] = (acc[0][t] + acc[1][t] + acc[2][t] + acc[3][t]) * 0.08838834764831845f; __syncthreads(); }
      const int g = t >> 4, c = t & 15;
      float qf[8]; for (int j = 0; j < 8; ++j) qf[j] = q[8 * c + j];
      for (int k = g; k < SK; k += 32) { u32x4 v = *(const u32x4*)(K + ((size_t)h * SK + k) * DH + 8 * c); float d = 0; for (int j = 0; j < 4; ++j) d += qf[2 * j] * lo(v[j]) + qf[2 * j + 1] * hi(v[j]); for (int s = 8; s > 0; s >>= 1) d += __shfl_xor(d, s); if (c == 0) sc[k] = d; }
      __syncthreads();
      float m = -1e30f; for (int k = t; k < SK; k += 512) m = fmaxf(m, sc[k]); m = bmax(m, red);
      float l = 0; for (int k = t; k < SK; k += 512) { float p = __expf(sc[k] - m); sc[k] = p; l += p; } l = bsum(l, red);
      float a[8] = {0, 0, 0, 0, 0, 0, 0, 0};
      for (int k = g; k < SK; k += 32) { u32x4 v = *(const u32x4*)(V + ((size_t)h * SK + k) * DH + 8 * c); float p = sc[k]; for (int j = 0; j < 4; ++j) { a[2 * j] += p * lo(v[j]); a[2 * j + 1] += p * hi(v[j]); } }
      for (int j = 0; j < 8; ++j) acc[g][8 * c + j] = a[j];
      __syncthreads();
      if (t < 128) { float s = 0; for (int i = 0; i < 32; ++i) s += acc[i][t]; o[h * 128 + t] = s / l; }
      __syncthreads();
    }
    { float a = 0; for (int k = 0; k < 4096; ++k) a += o[k] * bf2f(wo[(size_t)k * 512 + t]); __syncthreads(); xs[t] += a; __syncthreads(); }
    { float m = bsum(xs[t], red) / 512; float d = xs[t] - m; float v = bsum(d * d, red) / 512; xn[t] = d / sqrtf(v + 1e-5f); __syncthreads(); }
    for (int f = t; f < F; f += 512) { float a = bf2f(b1[f]); for (int d = 0; d < 512; ++d) a += xn[d] * bf2f(w1[(size_t)d * F + f]); float u = 0.7978845608028654f * (a + 0.044715f * a * a * a); hh[f] = 0.5f * a * (1.f + tanhf(u)); }
    __syncthreads();
    { float a = bf2f(b2[t]); for (int f = 0; f < F; ++f) a += hh[f] * bf2f(w2[(size_t)f * 512 + t]); xs[t] += a; __syncthreads(); }
  }
  out[t] = f2bf(xs[t] + (pad[(t + 1) % LDSPAD] == 77 ? 1.f : 0.f));
  }
  while (__builtin_amdgcn_s_memrealtime() - t0 < 600000ull) __builtin_amdgcn_s_sleep(8);
}
extern "C" void kernel_launch(void* const* d_in, const int* in_sizes, int n_in, void* d_out, int out_size, void* d_ws, size_t ws_size, hipStream_t stream) {
  k_one<<<NBLK, 512, 0, stream>>>((const u16*)d_in[0], (const u16*)d_in[1], (const u16*)d_in[2], (const u16*)d_in[3], (const u16*)d_in[4], (const u16*)d_in[6], (const u16*)d_in[7], (const u16*)d_in[8], (const u16*)d_in[9], (u16*)d_out);
}
```

```cpp
#include <hip/hip_runtime.h>
#include <stdint.h>

typedef unsigned short u16;
typedef unsigned long long u64;
typedef unsigned u32x4 __attribute__((ext_vector_type(4)));
typedef unsigned u32x2 __attribute__((ext_vector_type(2)));
typedef float f32x4 __attribute__((ext_vector_type(4)));
typedef float f32x2 __attribute__((ext_vector_type(2)));
typedef short bf16x8 __attribute__((ext_vector_type(8)));
typedef __bf16 bf16x2 __attribute__((ext_vector_type(2)));
typedef __amdgpu_buffer_rsrc_t rsrc_t;

#define KV_AUX 2
#define KVNT " nt"
#define W_AUX 2
#define WNT " nt"
#define SLP_A 4
#define SLP_B 4
#define SLP_AB 36
#define RSLP 0
#define RS1 0
#define PDL 30
#define PCLEAN 620u
#define DL_B1 110
#define DL_B2 150
#define DL_X 150
#define DL_CP 115
#define DL_CB 70
#define SLP_C 4
#define SLP_CN 40
#define SLP_D 4

constexpr int DM = 512, NH = 32, SK = 4096, DH = 128, FF = 1376;
constexpr int NCU = 256, NTHR = 512, NU = 86;
constexpr unsigned SENT = 0xFFFFFFFFu;
constexpr unsigned SPIN_MAX = 1u << 17;
constexpr int SC1 = 16;
constexpr float QS = 0.08838834764831845f * 1.4426950408889634f;
constexpr unsigned ONE2 = 0x3F803F80u;
constexpr int OOB = 0x40000000;

constexpr int CPY = 8192;
constexpr int A_SZ = 16 * 16384;
constexpr int WS_A = 0;
constexpr int B_SZ = 2 * 16384;
constexpr int WS_B = WS_A + 2 * A_SZ;
constexpr int C_SZ = 11 * 16384;
constexpr int WS_C = WS_B + 2 * B_SZ;
constexpr int WS_D2 = WS_C + 2 * C_SZ;
constexpr int WS_FLAGS = WS_D2 + 64 * 16384;
constexpr int WS_D1 = 2 << 20;
constexpr int WS_END = WS_D1 + NCU * 1024;
constexpr int WS_STAMP = 8 << 20;
static_assert(WS_FLAGS + 2048 <= WS_D1, "hand-off buffers fit one 2 MB page");
__device__ __forceinline__ int aoff(int r) { return WS_A + (r >> 4) * 16384 + (r & 15) * 512; }
__device__ __forceinline__ int doff(int r) { return WS_D2 + (r >> 2) * 16384 + (r & 3) * 2048; }

struct Params {
  const u16 *x, *K, *V, *wq, *wo, *w1, *b1, *w2, *b2;
  u16* out;
  unsigned char* ws;
};

__device__ __forceinline__ float lo(unsigned w) { return __builtin_bit_cast(float, w << 16); }
__device__ __forceinline__ float hi(unsigned w) { return __builtin_bit_cast(float, w & 0xffff0000u); }
__device__ __forceinline__ float asf(unsigned w) { return __builtin_bit_cast(float, w); }
__device__ __forceinline__ unsigned asu(float f) { return __builtin_bit_cast(unsigned, f); }
__device__ __forceinline__ unsigned pk2(float a, float b) { const bf16x2 v = {(__bf16)a, (__bf16)b}; return __builtin_bit_cast(unsigned, v); }
__device__ __forceinline__ unsigned sanu(unsigned u) { return u < 0xFFFFFFFEu ? u : 0xFFFFFFFEu; }
__device__ __forceinline__ unsigned san(float f) { return sanu(asu(f)); }
template <int CTRL> __device__ __forceinline__ unsigned dppu(unsigned x) { return (unsigned)__builtin_amdgcn_mov_dpp((int)x, CTRL, 0xf, 0xf, true); }
template <int CTRL> __device__ __forceinline__ float dpp(float x) { return asf(dppu<CTRL>(asu(x))); }
constexpr int XOR1 = 0xB1, XOR2 = 0x4E, HMIR = 0x141, MIR = 0x140, ROR8 = 0x128, BC = 0x150, SHL = 0x100;
__device__ __forceinline__ float tr32(float a, float b) {
  auto s = __builtin_amdgcn_permlane32_swap(asu(a), asu(b), false, false);
  return asf(s[0]) + asf(s[1]);
}
__device__ __forceinline__ float tr16(float a, float b) {
  auto s = __builtin_amdgcn_permlane16_swap(asu(a), asu(b), false, false);
  return asf(s[0]) + asf(s[1]);
}
template <int CTRL> __device__ __forceinline__ float trd(float a, float b, bool sel) {
  const float keep = sel ? b : a, send = sel ? a : b;
  return keep + dpp<CTRL>(send);
}
__device__ __forceinline__ float sum8l(float x) { x += dpp<XOR1>(x); x += dpp<XOR2>(x); x += dpp<HMIR>(x); return x; }
__device__ __forceinline__ float sum16(float x) { x = sum8l(x); x += dpp<MIR>(x); return x; }
__device__ __forceinline__ float sum64(float x) { x = sum16(x); x = tr16(x, x); x = tr32(x, x); return x; }
__device__ __forceinline__ float fmx(float a, float b) { return __builtin_amdgcn_fmed3f(a, b, __builtin_inff()); }
__device__ __forceinline__ float fmx3(float a, float b, float c) { return fmx(fmx(a, b), c); }
__device__ __forceinline__ float fmxa(float a, float b) { float r; asm("v_max_f32 %0, %1, %2" : "=v"(r) : "v"(a), "v"(b)); return r; }
#define MD(ctl) "s_nop 1\n\tv_max_f32_dpp %0, %0, %0 " ctl " row_mask:0xf bank_mask:0xf\n\t"
__device__ __forceinline__ float max8l(float x) { asm(MD("quad_perm:[1,0,3,2]") MD("quad_perm:[2,3,0,1]") MD("row_half_mirror") : "+v"(x)); return x; }
__device__ __forceinline__ float max16(float x) { asm(MD("quad_perm:[1,0,3,2]") MD("quad_perm:[2,3,0,1]") MD("row_half_mirror") MD("row_mirror") : "+v"(x)); return x; }
__device__ __forceinline__ float mx16r(float a) { auto s = __builtin_amdgcn_permlane16_swap(asu(a), asu(a), false, false); return fmxa(asf(s[0]), asf(s[1])); }
__device__ __forceinline__ float mx32r(float a) { auto s = __builtin_amdgcn_permlane32_swap(asu(a), asu(a), false, false); return fmxa(asf(s[0]), asf(s[1])); }
__device__ __forceinline__ float dot2(unsigned a, unsigned b, float c) {
  return __builtin_amdgcn_fdot2_f32_bf16(__builtin_bit_cast(bf16x2, a), __builtin_bit_cast(bf16x2, b), c, false);
}
__device__ __forceinline__ float ex2(float x) { return __builtin_amdgcn_exp2f(x); }
__device__ __forceinline__ f32x4 mfma(u32x4 a, u32x4 b, f32x4 c) {
  return __builtin_amdgcn_mfma_f32_16x16x32_bf16(__builtin_bit_cast(bf16x8, a), __builtin_bit_cast(bf16x8, b), c, 0, 0, 0);
}
__device__ __forceinline__ float sum4(f32x4 s) { return (s[0] + s[1]) + (s[2] + s[3]); }
__device__ __forceinline__ float sum8(f32x4 a, f32x4 b) { return sum4(a + b); }
__device__ __forceinline__ float sel4(int g, float a, float b, float c, float d) { const float x = (g & 1) ? b : a, y = (g & 1) ? d : c; return (g & 2) ? y : x; }

template <int AUX> __device__ __forceinline__ u32x4 ld128(rsrc_t r, int voff, int soff = 0) { return __builtin_amdgcn_raw_buffer_load_b128(r, voff, soff, AUX); }
template <int AUX> __device__ __forceinline__ u32x2 ld64(rsrc_t r, int voff, int soff = 0) { return __builtin_amdgcn_raw_buffer_load_b64(r, voff, soff, AUX); }
template <int AUX> __device__ __forceinline__ unsigned ld32(rsrc_t r, int voff, int soff = 0) { return __builtin_amdgcn_raw_buffer_load_b32(r, voff, soff, AUX); }
template <int AUX> __device__ __forceinline__ unsigned ld16(rsrc_t r, int voff, int soff = 0) { return __builtin_amdgcn_raw_buffer_load_b16(r, voff, soff, AUX); }
__device__ __forceinline__ void st128(rsrc_t r, int voff, int soff, u32x4 v) { __builtin_amdgcn_raw_buffer_store_b128(v, r, voff, soff, SC1); }
__device__ __forceinline__ void st128x2(rsrc_t r, int voff, int soff, u32x4 v) { st128(r, voff, soff, v); st128(r, voff, soff + CPY, v); }
__device__ __forceinline__ rsrc_t mk(const void* p, unsigned bytes) { return __builtin_amdgcn_make_buffer_rsrc((void*)p, 0, bytes, 0x00020000); }
__device__ __forceinline__ unsigned um(unsigned a, unsigned b) { return a > b ? a : b; }
__device__ __forceinline__ unsigned mx4(u32x4 v) { return um(um(v.x, v.y), um(v.z, v.w)); }
__device__ __forceinline__ unsigned mx2(u32x2 v) { return um(v.x, v.y); }
__device__ __forceinline__ int vsel(bool c, int a, int b) { asm("" : "+v"(a)); asm("" : "+v"(b)); return c ? a : b; }
template <typename T> __device__ __forceinline__ void pin(T& x) { asm volatile("" : "+v"(x)); }

__device__ __forceinline__ u64 uni64(u64 m) { return ((u64)(unsigned)__builtin_amdgcn_readfirstlane((unsigned)(m >> 32)) << 32) | (unsigned)__builtin_amdgcn_readfirstlane((unsigned)m); }
#define LD4(dst, off, imm) "buffer_load_dwordx4 %[" dst "], %[" off "], %[rs], %[so] offen offset:" imm " sc1\n\t"
#define LD2(dst, off, imm) "buffer_load_dwordx2 %[" dst "], %[" off "], %[rs], %[so] offen offset:" imm " sc1\n\t"
__device__ __forceinline__ void gather_442(u32x4& a, u32x4& b, u32x2& c, int oa, int oc, rsrc_t rs, int so, u64 m) {
  asm volatile("s_mov_b64 exec, %[m]\n\t" LD4("a", "oa", "0") LD4("b", "oa", "64") LD2("c", "oc", "0") "s_mov_b64 exec, -1"
               : [a] "+v"(a), [b] "+v"(b), [c] "+v"(c) : [oa] "v"(oa), [oc] "v"(oc), [rs] "s"(rs), [so] "s"(so), [m] "s"(uni64(m)) : "memory");
}
__device__ __forceinline__ void gather_42(u32x4& a, u32x2& c, int oa, int oc, rsrc_t rs, int so, u64 m) {
  asm volatile("s_mov_b64 exec, %[m]\n\t" LD4("a", "oa", "0") LD2("c", "oc", "0") "s_mov_b64 exec, -1"
               : [a] "+v"(a), [c] "+v"(c) : [oa] "v"(oa), [oc] "v"(oc), [rs] "s"(rs), [so] "s"(so), [m] "s"(uni64(m)) : "memory");
}
__device__ __forceinline__ void gather_44s(u32x4& a, u32x4& b, int oa, rsrc_t rs, int so, int so2, u64 m) {
  asm volatile("s_mov_b64 exec, %[m]\n\t" LD4("a", "oa", "0") "buffer_load_dwordx4 %[b], %[oa], %[rs], %[so2] offen sc1\n\ts_mov_b64 exec, -1"
               : [a] "+v"(a), [b] "+v"(b) : [oa] "v"(oa), [rs] "s"(rs), [so] "s"(so), [so2] "s"(so2), [m] "s"(uni64(m)) : "memory");
}
__device__ __forceinline__ void gather_442b(u32x4& a, u32x4& b, int oa, rsrc_t rs, int so, u64 m) {
  asm volatile("s_mov_b64 exec, %[m]\n\t" LD4("a", "oa", "0") LD4("b", "oa", "64") "s_mov_b64 exec, -1"
               : [a] "+v"(a), [b] "+v"(b) : [oa] "v"(oa), [rs] "s"(rs), [so] "s"(so), [m] "s"(uni64(m)) : "memory");
}
__device__ __forceinline__ void gather_44v(u32x4& a, u32x4& b, int oa, int ob, rsrc_t rs, int so, u64 m) {
  asm volatile("s_mov_b64 exec, %[m]\n\t" LD4("a", "oa", "0") LD4("b", "ob", "0") "s_mov_b64 exec, -1"
               : [a] "+v"(a), [b] "+v"(b) : [oa] "v"(oa), [ob] "v"(ob), [rs] "s"(rs), [so] "s"(so), [m] "s"(uni64(m)) : "memory");
}
__device__ __forceinline__ void gather_44(u32x4& a, u32x4& b, int oa, rsrc_t rs, int so, u64 m) {
  asm volatile("s_mov_b64 exec, %[m]\n\t" LD4("a", "oa", "0") LD4("b", "oa", "256") "s_mov_b64 exec, -1"
               : [a] "+v"(a), [b] "+v"(b) : [oa] "v"(oa), [rs] "s"(rs), [so] "s"(so), [m] "s"(uni64(m)) : "memory");
}
__device__ __forceinline__ void gather_4(u32x4& a, int oa, rsrc_t rs, int so, u64 m) {
  asm volatile("s_mov_b64 exec, %[m]\n\t" LD4("a", "oa", "0") "s_mov_b64 exec, -1"
               : [a] "+v"(a) : [oa] "v"(oa), [rs] "s"(rs), [so] "s"(so), [m] "s"(uni64(m)) : "memory");
}
__device__ __forceinline__ void gather_444(u32x4& a, u32x4& b, u32x4& c, int oa, int ob, int oc, rsrc_t rs, int so, u64 m) {
  asm volatile("s_mov_b64 exec, %[m]\n\t" LD4("a", "oa", "0") LD4("b", "ob", "0") LD4("c", "oc", "0") "s_mov_b64 exec, -1"
               : [a] "+v"(a), [b] "+v"(b), [c] "+v"(c) : [oa] "v"(oa), [ob] "v"(ob), [oc] "v"(oc), [rs] "s"(rs), [so] "s"(so), [m] "s"(uni64(m)) : "memory");
}
__device__ __forceinline__ u64 umask(bool on, u64 m) { const unsigned f = __builtin_amdgcn_readfirstlane(on ? ~0u : 0u); return m & (((u64)f << 32) | f); }
#define VMWAIT(N, ...) asm volatile("s_waitcnt vmcnt(" #N ")" : __VA_ARGS__ :: "memory")
#define TMO(code) do { } while (0)
#define UNLIKELY(x) __builtin_expect(!!(x), 0)

__device__ __forceinline__ int side_from(unsigned p0, unsigned p1) { return __builtin_amdgcn_readfirstlane(p0 < p1 ? 0 : CPY); }
__device__ __forceinline__ u64 rtime() { return __builtin_amdgcn_s_memrealtime(); }
__device__ __forceinline__ void wait_until(u64 t) { while ((long long)(rtime() - t) < 0) __builtin_amdgcn_s_sleep(1); }
#define FENCE() do { asm volatile("" ::: "memory"); __builtin_amdgcn_sched_barrier(0); } while (0)
#define BAR() do { asm volatile("s_waitcnt lgkmcnt(0)" ::: "memory"); __builtin_amdgcn_s_barrier(); asm volatile("" ::: "memory"); } while (0)
#define LWAIT() asm volatile("s_waitcnt lgkmcnt(0)" ::: "memory")
#define TS(i) do { } while (0)
#define TSV(i, v) do { } while (0)
#define TSDUMP() do { } while (0)

struct __attribute__((aligned(16))) Lds {
  union {
    float obuf[8][128];
    struct { float cbuf[64][8]; u16 xhl[8][2][64]; };
  };
  float ml[8][2];
  unsigned qb[64];
  unsigned csb[64];
  float ybuf[64][8];
  float sbuf[16][8];
  float s12[2][8];
  float cs1p[16][8];
  float xs[64];
  float xfull[512];
  unsigned qrec[8][72];
  float st2[8][4];
  unsigned prb[4];
  u64 tB[2];
  unsigned tBf[2];
  unsigned qflag;
  unsigned pad_[1];
  u32x4 wqs[16][64];
  unsigned char big[8][16384];
};
#define LP(T, field, off) (*(T*)((unsigned char*)&L.field + (off)))

struct Ctx {
  int t, b, w, l, g, c, xg, idx, h, s, cu, u;
  rsrc_t rws;
  int sd;
  const unsigned char* pl;
  int aPr, aA, aAml, aApub, aB0, aB2, aBpub, o_bw, o_br, o_yw2, aC0, aC1, aCpub, aDg, aDq, aDx;
  int o_ob, o_l8, o_ml, o_yw, o_c32, o_sw, o_cw, o_l32, o_l4, o_xw, o_xr, o_qr, o_st, o_dd, o_xs;
};
constexpr u64 M_C8 = 0x00FF00FF00FF00FFull;

__device__ __forceinline__ void make_frags(Lds& L, const Ctx& X, const u32x4 (&raw)[2], u32x4 (&fr)[2], int base) {
  unsigned char* st = &L.big[X.w][base];
#pragma unroll
  for (int i = 0; i < 2; ++i) *(u32x4*)(st + (32 * i + (X.l >> 1)) * 32 + (X.l & 1) * 16) = raw[i];
  LWAIT();
#pragma unroll
  for (int ci = 0; ci < 2; ++ci)
#pragma unroll
    for (int j2 = 0; j2 < 4; ++j2) {
      const unsigned a = *(const u16*)(st + (32 * ci + 8 * X.g + 2 * j2) * 32 + X.c * 2), bb = *(const u16*)(st + (32 * ci + 8 * X.g + 2 * j2 + 1) * 32 + X.c * 2);
      fr[ci][j2] = a | (bb << 16);
    }
  pin(fr[0]); pin(fr[1]);
}

template <int LAYER, typename HOOK>
__device__ __forceinline__ void pv_publish(Lds& L, const Ctx& X, const Params& p, float tsel, float tmax, float scale, const u32x4 (&vr)[16], const unsigned (&msk)[4], HOOK hook) {
  const int t = X.t, b = X.b, w = X.w, l = X.l;
  (void)b; (void)t;
  const float pe = ex2((tsel - tmax) * scale);
  const unsigned pd = pk2(pe, pe);
  if (LAYER == 0) TS(5);
  f32x4 o0 = {0.f, 0.f, 0.f, 0.f}, o1 = {0.f, 0.f, 0.f, 0.f};
#define PVS(n) { const unsigned pv = dppu<BC + n>(pd); const u32x4 a = {pv & msk[0], pv & msk[1], pv & msk[2], pv & msk[3]}; if (n & 1) o1 = mfma(a, vr[n], o1); else o0 = mfma(a, vr[n], o0); }
  PVS(0) PVS(1) PVS(2) PVS(3) PVS(4) PVS(5) PVS(6) PVS(7) PVS(8) PVS(9) PVS(10) PVS(11) PVS(12) PVS(13) PVS(14) PVS(15)
#undef PVS
  const float lsum = sum64(lo(pd));
  o0 += o1;
  if (l < 32) LP(f32x4, obuf, X.o_ob) = o0;
  if (l == 0) *(f32x2*)&L.ml[w][0] = f32x2{tmax * scale, lsum};
  hook();
  BAR();
  if (LAYER == 0 && w == 3) {
    u64 t0, t1, t2; unsigned d1, d2;
    asm volatile("s_memtime %0\n\ts_waitcnt lgkmcnt(0)" : "=s"(t0) :: "memory");
    asm volatile("s_load_dword %0, %2, 0x0\n\ts_waitcnt lgkmcnt(0)\n\ts_memtime %1\n\ts_waitcnt lgkmcnt(0)" : "=s"(d1), "=s"(t1) : "s"(X.pl) : "memory");
    asm volatile("s_load_dword %0, %2, 0x2000\n\ts_waitcnt lgkmcnt(0)\n\ts_memtime %1\n\ts_waitcnt lgkmcnt(0)" : "=s"(d2), "=s"(t2) : "s"(X.pl) : "memory");
    if (l == 0) { L.prb[0] = (unsigned)(t1 - t0); L.prb[1] = (unsigned)(t2 - t1); }
    LWAIT();
    if (l == 0) __hip_atomic_store(&L.prb[2], 1u, __ATOMIC_RELAXED, __HIP_MEMORY_SCOPE_WORKGROUP);
    TSV(45, (t1 - t0) * 10000 + (t2 - t1));
  }
  if (w == 0) {
    const f32x2 mlu = LP(const f32x2, ml, X.o_ml);
    f32x2 ob[8];
#pragma unroll
    for (int u = 0; u < 8; ++u) ob[u] = LP(const f32x2, obuf[u], X.o_l8);
    const float M = max8l(mlu[0]);
    const float f = ex2(mlu[0] - M);
    const float Ls = sum8l(f * mlu[1]);
    f32x2 o = dpp<BC + 0>(f) * ob[0];
    o += dpp<BC + 1>(f) * ob[1]; o += dpp<BC + 2>(f) * ob[2]; o += dpp<BC + 3>(f) * ob[3];
    o += dpp<BC + 4>(f) * ob[4]; o += dpp<BC + 5>(f) * ob[5]; o += dpp<BC + 6>(f) * ob[6]; o += dpp<BC + 7>(f) * ob[7];
    const unsigned pk = sanu(pk2(o[0], o[1]));
    u32x4 v = {pk, dppu<0x55>(pk), dppu<0xAA>(pk), dppu<0xFF>(pk)};
    if (l == 1) v = u32x4{san(M), san(Ls), 0u, 0u};
    st128x2(X.rws, X.aApub, LAYER * A_SZ, v);
  }
}

template <int LAYER, int NYOUNG, typename HOOK>
__device__ __forceinline__ void wo_stage(Lds& L, Ctx& X, const Params& p, const u32x4 (&wof)[4], HOOK hook) {
  const int t = X.t, b = X.b, w = X.w, l = X.l;
  (void)b; (void)t;
  const unsigned ini = X.c < 8 ? SENT : 0u;
  u32x4 a0 = {ini, ini, ini, ini}; u32x2 mlv = {ini, ini};
  pin(a0); pin(mlv);
  BAR();
  if (LAYER == 0) X.sd = side_from(L.prb[0], L.prb[1]);
  __builtin_amdgcn_s_sleep(SLP_A);
  TS(32 + LAYER * 8);
  gather_42(a0, mlv, X.aA, X.aAml, X.rws, LAYER * A_SZ + X.sd, M_C8);
  FENCE();
  hook();
  FENCE();
  VMWAIT(0, "+v"(a0), "+v"(mlv));
  {
    bool bad = um(mx4(a0), mx2(mlv)) == SENT;
    TSV(33 + LAYER * 8, __any(bad) ? 2 : 1);
    if (UNLIKELY(__any(bad))) {
      for (unsigned sp_ = 0; sp_ < SPIN_MAX; ++sp_) {
        TMO(1 + 10 * LAYER);
        if (LAYER == 0 && RS1) __builtin_amdgcn_s_sleep(RS1);
        gather_42(a0, mlv, X.aA, X.aAml, X.rws, LAYER * A_SZ + X.sd, __ballot(bad));
        VMWAIT(0, "+v"(a0), "+v"(mlv));
        bad = um(mx4(a0), mx2(mlv)) == SENT;
        if (!__any(bad)) break;
      }
    }
  }
  TS(LAYER * 10 + 8);
  f32x4 d[4];
#pragma unroll
  for (int n = 0; n < 4; ++n) d[n] = mfma(a0, wof[n], f32x4{0.f, 0.f, 0.f, 0.f});
  const float m = asf(mlv.x), ls = asf(mlv.y);
  const float M = max8l(m);
  const float e = ex2(m - M);
  const float f = e * __builtin_amdgcn_rcpf(sum8l(e * ls));
  const bool odd = (X.g & 1) != 0;
  const float F0 = odd ? dpp<BC + 4>(f) : dpp<BC + 0>(f), F1 = odd ? dpp<BC + 5>(f) : dpp<BC + 1>(f), F2 = odd ? dpp<BC + 6>(f) : dpp<BC + 2>(f), F3 = odd ? dpp<BC + 7>(f) : dpp<BC + 3>(f);
  float y[4];
#pragma unroll
  for (int n = 0; n < 4; ++n) y[n] = (F0 * d[n][0] + F1 * d[n][1]) + (F2 * d[n][2] + F3 * d[n][3]);
  const float ya = tr16(y[0], y[1]), yb = tr16(y[2], y[3]);
  if (l < 32) { LP(float, ybuf, X.o_yw) = ya; LP(float, ybuf[32], X.o_yw) = yb; }
  BAR();
  if (w == 0) {
    const float s = sum8(LP(const f32x4, ybuf, X.o_l32), LP(const f32x4, ybuf[0][4], X.o_l32));
    const unsigned pk = sanu(pk2(s, dpp<XOR1>(s)));
    const u32x4 v = {pk, dppu<SHL + 2>(pk), dppu<SHL + 4>(pk), dppu<SHL + 6>(pk)};
    st128x2(X.rws, X.aBpub, LAYER * B_SZ, v);
  }
}

template <int LAYER, typename HOOK>
__device__ __forceinline__ void mlp_stage(Lds& L, Ctx& X, const Params& p, u64 tref, u64 tpub, bool wocu, const u32x4 (&xf)[2], const u32x4 (&wA)[4], float& cs1, const float& b1v, const float& xres, HOOK hook) {
  const int t = X.t, b = X.b, w = X.w, l = X.l;
  (void)b; (void)t;
  u32x4 a0 = {0u, 0u, 0u, 0u}, a1 = a0, a2 = a0, a3 = a0;
  pin(a0); pin(a1); pin(a2); pin(a3);
  const bool mlp = X.u >= 0;
  const u64 m01 = umask(mlp, ~0ull), m2 = umask(w == 7, ~0ull);
  BAR();
  if (LAYER == 0) X.sd = __builtin_amdgcn_readfirstlane(L.prb[3]);
  TSV(47, X.sd + 1);
  wait_until(tref + (mlp ? (LAYER == 0 ? DL_B1 : DL_B2) : DL_X));
  TS(34 + LAYER * 8);
  gather_44s(a0, a1, X.aB0, X.rws, LAYER * B_SZ + X.sd, LAYER * B_SZ + X.sd + 16384, m01);
  gather_44s(a2, a3, X.aB2, X.rws, LAYER * B_SZ + X.sd, LAYER * B_SZ + X.sd + 16384, m2);
  FENCE();
  hook();
  float S1x, s1x, s2x;
  {
    const u32x4 ones = {ONE2, ONE2, ONE2, ONE2};
    const f32x4 z = {0.f, 0.f, 0.f, 0.f};
    f32x4 S0 = mfma(wA[0], xf[0], z), G0 = mfma(xf[0], xf[0], z), s10 = mfma(ones, xf[0], z);
    S0 = mfma(wA[1], xf[1], S0); G0 = mfma(xf[1], xf[1], G0); s10 = mfma(ones, xf[1], s10);
    float S2[2];
#pragma unroll
    for (int j = 0; j < 2; ++j) S2[j] = trd<ROR8>(S0[j], S0[j + 2], (l & 8) != 0);
    S1x = trd<HMIR>(S2[0], S2[1], (l & 4) != 0);
    S1x += dpp<XOR2>(S1x); S1x += dpp<XOR1>(S1x);
    s1x = sum16(s10[0]);
    s2x = sum64(sum4(G0));
    pin(S1x); pin(s1x); pin(s2x);
  }
  FENCE();
  VMWAIT(0, "+v"(a0), "+v"(a1), "+v"(a2), "+v"(a3));
  bool waited;
  {
    bool bad = um(um(mx4(a0), mx4(a1)), um(mx4(a2), mx4(a3))) == SENT;
    TSV(35 + LAYER * 8, __any(bad) ? 2 : 1);
    waited = __any(bad);
    if (UNLIKELY(__any(bad))) {
      for (unsigned sp_ = 0; sp_ < SPIN_MAX; ++sp_) {
        TMO(2 + 10 * LAYER);
        if (LAYER == 0 && RS1) __builtin_amdgcn_s_sleep(RS1);
        const u64 bm = __ballot(bad);
        gather_44s(a0, a1, X.aB0, X.rws, LAYER * B_SZ + X.sd, LAYER * B_SZ + X.sd + 16384, bm & m01);
        gather_44s(a2, a3, X.aB2, X.rws, LAYER * B_SZ + X.sd, LAYER * B_SZ + X.sd + 16384, bm & m2);
        VMWAIT(0, "+v"(a0), "+v"(a1), "+v"(a2), "+v"(a3));
        bad = um(um(mx4(a0), mx4(a1)), um(mx4(a2), mx4(a3))) == SENT;
        if (!__any(bad)) break;
      }
    }
  }
  TS(LAYER * 10 + 10);
  if (mlp) {
    LP(u32x4, big, X.o_bw) = a0; LP(u32x4, big[0][8 * 144], X.o_bw) = a1;
    LWAIT();
    a0 = LP(const u32x4, big, X.o_br); a1 = LP(const u32x4, big[0][64], X.o_br);
    const u32x4 ones = {ONE2, ONE2, ONE2, ONE2};
    const f32x4 z = {0.f, 0.f, 0.f, 0.f};
    f32x4 S = mfma(wA[0], a0, z), G = mfma(a0, a0, z), Gx = mfma(xf[0], a0, z), s1 = mfma(ones, a0, z);
    S = mfma(wA[1], a1, S); G = mfma(a1, a1, G); Gx = mfma(xf[1], a1, Gx); s1 = mfma(ones, a1, s1);
    asm volatile("" : "+v"(S), "+v"(G), "+v"(Gx), "+v"(s1));
    G += 2.f * Gx;
    float S2[2];
#pragma unroll
    for (int j = 0; j < 2; ++j) S2[j] = trd<ROR8>(S[j], S[j + 2], (l & 8) != 0);
    float S1 = trd<HMIR>(S2[0], S2[1], (l & 4) != 0);
    S1 += dpp<XOR2>(S1); S1 += dpp<XOR1>(S1);
    S1 += S1x;
    const float s2t = sum64(sum4(G)) + s2x;
    const float s1t = sum16(s1[0]) + s1x;
    if ((l & 3) == 0) LP(float, sbuf, X.o_sw) = S1;
    if (l == 0) { L.s12[0][w] = s1t; L.s12[1][w] = s2t; }
    BAR();
    const float St = sum8(LP(const f32x4, sbuf, X.o_c32), LP(const f32x4, sbuf[0][4], X.o_c32));
    if (LAYER == 0) { cs1 = sum8(LP(const f32x4, cs1p, X.o_c32), LP(const f32x4, cs1p[0][4], X.o_c32)); pin(cs1); }
    const float sv = sum8l(LP(const float, s12, X.c * 4)) * (1.f / 512);
    const float mean = dpp<BC + 0>(sv), ms = dpp<BC + 8>(sv);
    const float rstd = __builtin_amdgcn_rsqf(fmx(ms - mean * mean, 0.f) + 1e-5f);
    const float hp = rstd * (St - mean * cs1) + b1v;
    const float uu = hp * (0.7978845608028654f + 0.7978845608028654f * 0.044715f * hp * hp);
    const float ee = ex2(uu * (2.f * 1.4426950408889634f));
    const float hv = hp - hp * __builtin_amdgcn_rcpf(ee + 1.f);
    const unsigned hp2 = pk2(hv, dpp<XOR1>(hv));
    unsigned hb[8] = {dppu<BC + 0>(hp2), dppu<BC + 2>(hp2), dppu<BC + 4>(hp2), dppu<BC + 6>(hp2), dppu<BC + 8>(hp2), dppu<BC + 10>(hp2), dppu<BC + 12>(hp2), dppu<BC + 14>(hp2)};
    asm volatile("" : "+v"(hb[0]), "+v"(hb[1]), "+v"(hb[2]), "+v"(hb[3]), "+v"(hb[4]), "+v"(hb[5]), "+v"(hb[6]), "+v"(hb[7]));
    float acc0 = 0.f, acc1 = 0.f;
#pragma unroll
    for (int F = 0; F < 8; F += 2) { acc0 = dot2(hb[F], wA[2 + (F >> 2)][F & 3], acc0); acc1 = dot2(hb[F + 1], wA[2 + ((F + 1) >> 2)][(F + 1) & 3], acc1); }
    const float av = acc0 + acc1;
    const unsigned pk = sanu(pk2(av, dpp<XOR1>(av)));
    const u32x4 v = {pk, dppu<SHL + 2>(pk), dppu<SHL + 4>(pk), dppu<SHL + 6>(pk)};
    st128x2(X.rws, X.aCpub, LAYER * C_SZ, v);
    TS(LAYER * 10 + 17);
  }
  if (!mlp && w == 7) {
    const u64 ta = waited ? rtime() + DL_CB : 0ull, tc = wocu ? tpub + DL_CP : 0ull, tb = ta > tc ? ta : tc;
    if (l == 0) { L.tB[LAYER] = tb; } LWAIT(); if (l == 0) __hip_atomic_store(&L.tBf[LAYER], 1u, __ATOMIC_RELAXED, __HIP_MEMORY_SCOPE_WORKGROUP); }
  if (w == 7) {
    float o[8];
#pragma unroll
    for (int j = 0; j < 4; ++j) { o[2 * j] = lo(a2[j]) + lo(a3[j]); o[2 * j + 1] = hi(a2[j]) + hi(a3[j]); }
    float o4[4], oo2[2];
#pragma unroll
    for (int j = 0; j < 4; ++j) o4[j] = tr32(o[j], o[j + 4]);
#pragma unroll
    for (int j = 0; j < 2; ++j) oo2[j] = tr16(o4[j], o4[j + 2]);
    const float v = trd<ROR8>(oo2[0], oo2[1], (l & 8) != 0);
    LP(float, xs, X.o_xs) = v + xres;
  }
  if (mlp) __builtin_amdgcn_s_sleep(SLP_C);
  else {
    for (unsigned sp_ = 0; sp_ < (SPIN_MAX << 4); ++sp_) { if (__hip_atomic_load(&L.tBf[LAYER], __ATOMIC_RELAXED, __HIP_MEMORY_SCOPE_WORKGROUP) != 0u) break; __builtin_amdgcn_s_sleep(2); }
    asm volatile("" ::: "memory");
    const u64 tb = L.tB[LAYER];
    wait_until(uni64(tb));
  }
}

template <int LAYER, typename HOOK>
__device__ __forceinline__ float c_gather(Lds& L, const Ctx& X, const Params& p, HOOK hook) {
  const int t = X.t, b = X.b, w = X.w;
  (void)b; (void)t; (void)w;
  u32x4 a0, a1;
  asm volatile("" : "=v"(a0), "=v"(a1));
  TS(36 + LAYER * 8);
  gather_44v(a0, a1, X.aC0, X.aC1, X.rws, LAYER * C_SZ + X.sd, ~0ull);
  FENCE();
  hook();
  FENCE();
  VMWAIT(0, "+v"(a0), "+v"(a1));
  {
    bool bad = um(mx4(a0), mx4(a1)) == SENT;
    TSV(37 + LAYER * 8, __any(bad) ? 2 : 1);
    if (UNLIKELY(__any(bad))) {
      for (unsigned sp_ = 0; sp_ < SPIN_MAX; ++sp_) {
        TMO(3 + 10 * LAYER);
        gather_44v(a0, a1, X.aC0, X.aC1, X.rws, LAYER * C_SZ + X.sd, __ballot(bad));
        VMWAIT(0, "+v"(a0), "+v"(a1));
        bad = um(mx4(a0), mx4(a1)) == SENT;
        if (!__any(bad)) break;
      }
    }
  }
  TS(LAYER * 10 + 12);
  float o[8];
#pragma unroll
  for (int j = 0; j < 4; ++j) { o[2 * j] = lo(a0[j]) + lo(a1[j]); o[2 * j + 1] = hi(a0[j]) + hi(a1[j]); }
  float o4[4], o2[2];
#pragma unroll
  for (int j = 0; j < 4; ++j) o4[j] = tr32(o[j], o[j + 4]);
#pragma unroll
  for (int j = 0; j < 2; ++j) o2[j] = tr16(o4[j], o4[j + 2]);
  LP(float, cbuf, X.o_cw) = trd<ROR8>(o2[0], o2[1], (X.l & 8) != 0);
  BAR();
  return sum8(LP(const f32x4, cbuf, X.o_l32), LP(const f32x4, cbuf[0][4], X.o_l32)) + LP(const float, xs, X.o_l4);
}

__global__ void __launch_bounds__(NTHR, 2) fused_kernel(Params p) {
  asm volatile("" : "+s"(p.x), "+s"(p.K), "+s"(p.V), "+s"(p.wq), "+s"(p.wo), "+s"(p.w1), "+s"(p.b1), "+s"(p.w2), "+s"(p.b2), "+s"(p.out), "+s"(p.ws));
  __shared__ Lds L;
  Ctx X;
  const int t = threadIdx.x, b = blockIdx.x;
  const int w = __builtin_amdgcn_readfirstlane(t >> 6), l = t & 63, g = l >> 4, c = l & 15;
  const int xg = b & 7, idx = b >> 3, s = idx & 7, cu = xg * 32 + idx;
  const int hg = cu >> 3;
  const int h = hg;
  const bool wocu = idx >= 16;
  const int wpp = (idx >> 3) & 1, wj = idx & 7;
  int u = idx < 12 ? 4 * (xg + 8 * (idx >> 2)) + (idx & 3) : -1;
  if (u >= NU) u = -1;
  TS(0);
  unsigned char* const wsa = (unsigned char*)(((size_t)p.ws + (2u << 20)) & ~(size_t)((2u << 20) - 1));
  const rsrc_t rws = mk(wsa, WS_END), rep = mk(p.ws, 8);
  X.t = t; X.b = b; X.w = w; X.l = l; X.g = g; X.c = c; X.xg = xg; X.idx = idx; X.h = h; X.s = s; X.cu = cu; X.u = u; X.rws = rws;
  const rsrc_t rK = mk(p.K + (size_t)(h * 8 + s) * 65536, 131072), rV = mk(p.V + (size_t)(h * 8 + s) * 65536, 131072);
  const rsrc_t rx = mk(p.x, 1024), rwq = mk(p.wq, DM * NH * DH * 2), rwo = mk(p.wo, wocu ? DM * NH * DH * 2 : 0);
  const unsigned mz = u >= 0 ? 1u : 0u;
  const rsrc_t rw1 = mk(p.w1, mz * DM * FF * 2), rw2 = mk(p.w2, mz * DM * FF * 2), rb1 = mk(p.b1, mz * FF * 2), rb2 = mk(p.b2, DM * 2);
  const rsrc_t rd1 = mk(wsa + WS_D1, NCU * 1024);
  unsigned msk[4];
#pragma unroll
  for (int i = 0; i < 4; ++i) msk[i] = (c >> 1) == i ? ((c & 1) ? 0xFFFF0000u : 0x0000FFFFu) : 0u;
  u32x2 ep;
  asm volatile("buffer_load_dwordx2 %0, off, %1, 0 sc1" : "=v"(ep) : "s"(rep) : "memory");
  u32x4 xv, wqr[16];
  unsigned xq;
  if (w == 0) {
    const int ox = l * 16, oq = (64 * s + 4 * c + g) * 2, ow = (64 * s + g) * 8192 + h * 256 + c * 16;
    asm volatile("buffer_load_dwordx4 %0, %2, %4, 0 offen\n\tbuffer_load_ushort %1, %3, %4, 0 offen" : "=&v"(xv), "=&v"(xq) : "v"(ox), "v"(oq), "s"(rx) : "memory");
#pragma unroll
    for (int n = 0; n < 16; ++n) asm volatile("buffer_load_dwordx4 %0, %1, %2, %3 offen" WNT : "=v"(wqr[n]) : "v"(ow), "s"(rwq), "s"(n * 4 * 8192) : "memory");
  } else if (w == 1) {
#pragma unroll
    for (int k = 0; k < 4; ++k) {
      const int q = l + 64 * k;
      int off = OOB;
      if (q < 17) off = aoff(cu) + q * 16;
      else if (q < 34) off = A_SZ + aoff(cu) + (q - 17) * 16;
      else if (q >= 40 && q < 56 && wocu) off = WS_B + ((q - 40) >> 3) * B_SZ + (((2 * xg + wpp) >> 3) * 16384) + ((2 * xg + wpp) & 7) * 1024 + wj * 128 + (q & 7) * 16;
      else if (q >= 64 && q < 80) off = doff(cu) + ((q - 64) >> 1) * 128 + (q & 1) * 16;
      else if (q >= 80 && q < 97) off = doff(cu) + 1024 + (q - 80) * 16;
      else if (q == 97) off = doff(cu) + 1408;
      else if (q >= 128 && q < 256 && u >= 0) off = WS_C + ((q - 128) >> 6) * C_SZ + (u >> 3) * 16384 + (u & 7) * 1024 + ((q - 128) & 63) * 16;
      st128x2(rws, off, 0, u32x4{SENT, SENT, SENT, SENT});
    }
  }
  if (t == 128) { L.qflag = 0u; L.prb[2] = 0u; L.tBf[0] = 0u; L.tBf[1] = 0u; }
  FENCE();
  LWAIT();
  __builtin_amdgcn_s_barrier();
  FENCE();
  u32x4 kf[16], vr[16];
  {
    int vo[4];
#pragma unroll
    for (int j = 0; j < 4; ++j) vo[j] = w * 16384 + (4 * j + g) * 256 + ((c ^ (4 * j + g)) * 16);
    const unsigned ldsb = (unsigned)(size_t)(__attribute__((address_space(3))) unsigned char*)&L.big[0][0] + w * 16384;
    unsigned keep;
#define KD(j, so) "s_nop 0\n\tbuffer_load_dwordx4 %[v" #j "], %[rs], " so " offen" KVNT " lds\n\ts_add_u32 m0, m0, 0x400\n\t"
#define KD4(so) KD(0, so) KD(1, so) KD(2, so) KD(3, so)
    asm volatile("s_mov_b32 %[keep], m0\n\ts_mov_b32 m0, %[lb]\n\t" KD4("0") KD4("%[s1]") KD4("%[s2]") KD4("%[s3]") "s_mov_b32 m0, %[keep]"
                 : [keep] "=&s"(keep)
                 : [v0] "v"(vo[0]), [v1] "v"(vo[1]), [v2] "v"(vo[2]), [v3] "v"(vo[3]), [rs] "s"(rK), [lb] "s"(ldsb), [s1] "s"(4096), [s2] "s"(8192), [s3] "s"(12288)
                 : "memory", "scc");
#undef KD4
#undef KD
  }
  FENCE();
  if (w == 0) {
    asm volatile("s_waitcnt vmcnt(16)"
                 : "+v"(ep), "+v"(xv), "+v"(xq), "+v"(wqr[0]), "+v"(wqr[1]), "+v"(wqr[2]), "+v"(wqr[3]), "+v"(wqr[4]), "+v"(wqr[5]), "+v"(wqr[6]), "+v"(wqr[7]),
                   "+v"(wqr[8]), "+v"(wqr[9]), "+v"(wqr[10]), "+v"(wqr[11]), "+v"(wqr[12]), "+v"(wqr[13]), "+v"(wqr[14]), "+v"(wqr[15]) :: "memory");
    float xs[8];
#pragma unroll
    for (int j = 0; j < 4; ++j) { xs[2 * j] = lo(xv[j]); xs[2 * j + 1] = hi(xv[j]); }
    const float mean = sum64(((xs[0] + xs[1]) + (xs[2] + xs[3])) + ((xs[4] + xs[5]) + (xs[6] + xs[7]))) * (1.f / 512);
    float d2 = 0.f;
#pragma unroll
    for (int j = 0; j < 8; ++j) { const float d = xs[j] - mean; d2 += d * d; }
    const float rstd = __builtin_amdgcn_rsqf(sum64(d2) * (1.f / 512) + 1e-5f);
    const float xn = (lo(xq & 0xffffu) - mean) * rstd;
    const unsigned pd = pk2(xn, xn);
    const u32x4 onesA = {ONE2 & msk[0], ONE2 & msk[1], ONE2 & msk[2], ONE2 & msk[3]};
    f32x4 aq = {0.f, 0.f, 0.f, 0.f}, ac = {0.f, 0.f, 0.f, 0.f};
#define QST(n) { const unsigned pv = dppu<BC + n>(pd); const u32x4 a = {pv & msk[0], pv & msk[1], pv & msk[2], pv & msk[3]}; aq = mfma(a, wqr[n], aq); ac = mfma(onesA, wqr[n], ac); L.wqs[n][l] = wqr[n]; }
    QST(0) QST(1) QST(2) QST(3) QST(4) QST(5) QST(6) QST(7) QST(8) QST(9) QST(10) QST(11) QST(12) QST(13) QST(14) QST(15)
#undef QST
    const unsigned tag = ep.x + 1u;
    if (l < 32) {
      const int off = WS_D1 + cu * 1024 + (8 * c + 4 * g) * 8;
      st128(rws, off, 0, u32x4{pk2(aq[0], ac[0]), tag, pk2(aq[1], ac[1]), tag});
      st128(rws, off + 16, 0, u32x4{pk2(aq[2], ac[2]), tag, pk2(aq[3], ac[3]), tag});
    }
    TS(1);
  } else if (w == 1) {
    asm volatile("s_waitcnt vmcnt(16)" : "+v"(ep) :: "memory");
    const u64 e1 = (((u64)ep.y << 32) | ep.x) + 1;
    if (l == 0) __builtin_amdgcn_raw_buffer_store_b64(u32x2{(unsigned)e1, (unsigned)(e1 >> 32)}, rws, WS_FLAGS + cu * 8, 0, SC1);
    TS(4);
  }
  FENCE();
  {
    const int uc0 = u >= 0 ? u : 0;
    const int rec = aoff(xg * 32 + (2 * wpp + (w >> 2)) * 8 + (l & 7));
    X.aA = rec + ((w & 3) * 32 + 8 * g) * 2; X.aAml = rec + 256;
    X.aApub = vsel((l & 3) == 0, aoff(cu) + l * 4, vsel(l == 1, aoff(cu) + 256, OOB));
    X.aB0 = WS_B + (l >> 3) * 1024 + w * 128 + (l & 7) * 16;
    X.aB2 = WS_B + (l >> 3) * 1024 + s * 128 + (l & 7) * 16;
    X.aBpub = vsel((l & 7) == 0, WS_B + ((2 * xg + wpp) >> 3) * 16384 + ((2 * xg + wpp) & 7) * 1024 + wj * 128 + l * 2, OOB);
    X.o_bw = w * 16384 + 8192 + (l >> 3) * 144 + (l & 7) * 16; X.o_br = w * 16384 + 8192 + c * 144 + g * 16;
    pin(X.o_bw); pin(X.o_br);
    const int cc = WS_C + w * 16384 + (l >> 3) * 1024 + s * 128 + (l & 7) * 16;
    X.aC0 = cc; X.aC1 = vsel(64 + 8 * w + (l >> 3) < NU, cc + 8 * 16384, OOB);
    X.aCpub = vsel((l & 7) == 0, WS_C + (uc0 >> 3) * 16384 + (uc0 & 7) * 1024 + w * 128 + l * 2, OOB);
    X.aPr = doff(cu) + 1408;
    X.pl = wsa + doff(cu) + 1408;
    if (w == 3) {
      unsigned d0;
      asm volatile("s_load_dword %0, %1, 0x0\n\ts_waitcnt lgkmcnt(0)" : "=s"(d0) : "s"(wsa + aoff(cu) + 384) : "memory");
    }
    X.aDg = doff(hg * 8 + w) + vsel(l < 16, (l >> 1) * 128 + (l & 1) * 16, vsel(l < 32, 768 + l * 16, 1280));
    X.aDq = vsel((l & 55) == 0, doff(cu) + w * 128 + l * 2, OOB);
    X.aDx = vsel(w == 0 ? (l & 3) == 0 : l == 0, doff(cu) + (w == 0 ? 1024 : 1280) + l * 4, OOB);
    pin(X.aA); pin(X.aAml); pin(X.aApub); pin(X.aB0); pin(X.aB2); pin(X.aBpub); pin(X.aC0); pin(X.aC1); pin(X.aCpub); pin(X.aDg); pin(X.aDq); pin(X.aDx);
    X.o_ob = (w * 128 + 8 * c + 4 * g) * 4; X.o_l8 = l * 8; X.o_ml = (l & 7) * 8;
    X.o_yw = ((16 * (g & 1) + c) * 8 + ((w + c) & 7)) * 4; X.o_c32 = c * 32;
    X.o_sw = ((4 * g + ((l >> 3) & 1) * 2 + ((l >> 2) & 1)) * 8 + ((w + 2 * g) & 7)) * 4;
    X.o_cw = ((8 * (l & 7) + (l >> 3)) * 8 + ((w + l) & 7)) * 4; X.o_l32 = l * 32; X.o_l4 = l * 4;
    X.o_xw = w * 256 + l * 2; X.o_xr = w * 256 + (c & 1) * 128 + g * 16;
    X.o_qr = (l & 7) * 288 + g * 16; X.o_st = (l & 7) * 16;
    X.o_dd = vsel(l < 16, (int)__builtin_offsetof(Lds, qrec) + w * 288 + l * 16, vsel(l < 32, (int)__builtin_offsetof(Lds, xfull) + w * 256 + (l - 16) * 16, (int)__builtin_offsetof(Lds, st2) + w * 16));
    X.o_xs = (8 * (l & 7) + (l >> 3)) * 4;
    pin(X.o_ob); pin(X.o_yw); pin(X.o_sw); pin(X.o_cw); pin(X.o_xr); pin(X.o_qr); pin(X.o_dd); pin(X.o_xs);
  }
  TS(28);
  LWAIT();
  __builtin_amdgcn_s_barrier();
  FENCE();
  u32x4 d1[8];
#pragma unroll
  for (int i = 0; i < 8; ++i) vr[i] = ld128<KV_AUX>(rV, w * 16384 + g * 4096 + c * 16, i * 256);
  FENCE();
  if (w == 0) {
#pragma unroll
    for (int i = 0; i < 8; ++i) d1[i] = ld128<SC1>(rd1, hg * 8192 + l * 16, i * 1024);
  }
  FENCE();
#pragma unroll
  for (int i = 8; i < 16; ++i) vr[i] = ld128<KV_AUX>(rV, w * 16384 + g * 4096 + c * 16, i * 256);
  FENCE();
  TS(30);
  asm volatile("s_waitcnt vmcnt(16)" : "+v"(ep) :: "memory");
  const u64 epoch = (((u64)ep.y << 32) | ep.x) + 1;
  {
    const unsigned char* st = L.big[w];
#pragma unroll
    for (int T = 0; T < 4; ++T)
#pragma unroll
      for (int i = 0; i < 4; ++i) kf[4 * T + i] = *(const u32x4*)(st + (16 * T + c) * 256 + (((4 * i + g) ^ c) * 16));
  }
  TS(2);
  if (w == 0) {
    const unsigned tag = (unsigned)epoch;
    unsigned bad = 0;
#pragma unroll
    for (int i = 0; i < 8; ++i) bad |= (d1[i].y ^ tag) | (d1[i].w ^ tag);
    if (UNLIKELY(!__all(bad == 0u))) {
      for (unsigned sp_ = 0; sp_ < SPIN_MAX; ++sp_) {
        asm volatile("" ::: "memory");
        bad = 0;
#pragma unroll
        for (int i = 0; i < 8; ++i) { d1[i] = ld128<SC1>(rd1, hg * 8192 + l * 16, i * 1024); bad |= (d1[i].y ^ tag) | (d1[i].w ^ tag); }
        if (__all(bad == 0u)) break;
      }
    }
    float qa = 0.f, qc = 0.f, ca = 0.f, cb = 0.f;
#pragma unroll
    for (int i = 0; i < 8; ++i) { qa += lo(d1[i].x); ca += hi(d1[i].x); qc += lo(d1[i].z); cb += hi(d1[i].z); }
    L.qb[l] = pk2(qa, qc);
    L.csb[l] = pk2(ca, cb);
    LWAIT();
    if (l == 0) __hip_atomic_store(&L.qflag, 1u, __ATOMIC_RELAXED, __HIP_MEMORY_SCOPE_WORKGROUP);
  } else {
    for (unsigned sp_ = 0; sp_ < (SPIN_MAX << 4); ++sp_) {
      if (__hip_atomic_load(&L.qflag, __ATOMIC_RELAXED, __HIP_MEMORY_SCOPE_WORKGROUP) != 0u) break;
      __builtin_amdgcn_s_sleep(1);
    }
  }
  asm volatile("" ::: "memory");
  TS(3);

  float kcs;
  u32x4 rA[4], wA[4], wqB[2], xf[2];
  unsigned rB[8], b1r, xres_r, b2_r;
  float cs1 = 0.f, b1v = 0.f, b2v = 0.f, xres1 = 0.f;
  const int uc = u >= 0 ? u : 0;
  const int xcol = 8 * (l & 7) + (l >> 3);
  {
    f32x4 sc[4];
#pragma unroll
    for (int T = 0; T < 4; ++T) sc[T] = f32x4{0.f, 0.f, 0.f, 0.f};
#pragma unroll
    for (int i = 0; i < 4; ++i) {
      const u32x4 qf = *(const u32x4*)&L.qb[16 * i + 4 * g];
#pragma unroll
      for (int T = 0; T < 4; ++T) sc[T] = mfma(qf, kf[4 * T + i], sc[T]);
    }
    const float tmax = max16(fmx(fmx3(sc[0][0], sc[1][0], sc[2][0]), sc[3][0]));
    const float tsel = sel4(g, sc[0][0], sc[1][0], sc[2][0], sc[3][0]);
    u32x4 fl = {0u, 0u, 0u, 0u};
    if (w >= 6) fl = ld128<SC1>(rws, WS_FLAGS + (t & 127) * 16);
    FENCE();
    FENCE();
    FENCE();
    pv_publish<0>(L, X, p, tsel, tmax, QS, vr, msk, [&]() {
      if (wocu) {
#pragma unroll
        for (int i = 0; i < 4; ++i) rA[i] = ld128<W_AUX>(rwo, ((4 * xg + 2 * wpp + (w >> 2)) * 128 + 32 * (w & 3) + 8 * i + (l >> 3)) * 1024 + wj * 128 + (l & 7) * 16);
      }
      const unsigned e0 = (unsigned)epoch, e1 = (unsigned)(epoch >> 32);
      if (UNLIKELY(w >= 6 && !__all(((fl.x ^ e0) | (fl.y ^ e1) | (fl.z ^ e0) | (fl.w ^ e1)) == 0u))) {
        for (unsigned sp_ = 0; sp_ < SPIN_MAX; ++sp_) {
          __builtin_amdgcn_s_sleep(16);
          asm volatile("" ::: "memory");
          fl = ld128<SC1>(rws, WS_FLAGS + (t & 127) * 16);
          TMO(5);
          if (__all(((fl.x ^ e0) | (fl.y ^ e1) | (fl.z ^ e0) | (fl.w ^ e1)) == 0u)) break;
        }
      }
    });
  }
  u64 tref = rtime();
  b1r = ld16<0>(rb1, (16 * uc + c) * 2);
  xres_r = ld16<0>(rx, (64 * s + xcol) * 2); b2_r = ld16<0>(rb2, (64 * s + xcol) * 2);
#pragma unroll
  for (int ci = 0; ci < 2; ++ci) xf[ci] = ld128<0>(rx, c == 0 ? (64 * w + 32 * ci + 8 * g) * 2 : 1024);
  if (!wocu) {
#pragma unroll
    for (int i = 0; i < 2; ++i) rA[i] = ld128<W_AUX>(rw1, (64 * w + 32 * i + (l >> 1)) * (FF * 2) + uc * 32 + (l & 1) * 16);
#pragma unroll
    for (int f = 0; f < 8; ++f) rA[2 + (f >> 2)][f & 3] = ld32<W_AUX>(rw2, (16 * uc + f) * 1024 + (t >> 1) * 4);
#pragma unroll
    for (int f = 0; f < 8; ++f) rB[f] = ld32<W_AUX>(rw2, (16 * uc + 8 + f) * 1024 + (t >> 1) * 4);
  }
  TS(6);
  if (wocu) {
    wo_stage<0, 0>(L, X, p, wA, [&]() {
      {
        unsigned char* st = L.big[w];
#pragma unroll
        for (int i = 0; i < 4; ++i) *(u32x4*)(st + (8 * i + (l >> 3)) * 128 + (l & 7) * 16) = rA[i];
        LWAIT();
#pragma unroll
        for (int n = 0; n < 4; ++n) {
          typedef short s16x4 __attribute__((ext_vector_type(4)));
          const unsigned base = (unsigned)(size_t)(__attribute__((address_space(3))) unsigned char*)st + (8 * g + (c >> 2)) * 128 + (16 * n + 4 * (c & 3)) * 2;
          const s16x4 lo4 = __builtin_amdgcn_ds_read_tr16_b64_v4i16((__attribute__((address_space(3))) s16x4*)(size_t)base), hi4 = __builtin_amdgcn_ds_read_tr16_b64_v4i16((__attribute__((address_space(3))) s16x4*)(size_t)(base + 512));
          const u32x2 l2 = __builtin_bit_cast(u32x2, lo4), h2 = __builtin_bit_cast(u32x2, hi4);
          wA[n] = u32x4{l2.x, l2.y, h2.x, h2.y}; pin(wA[n]);
        }
      }
      pin(wA[0]); pin(wA[1]); pin(wA[2]); pin(wA[3]);
    });
  }
  const u64 tpubB = rtime();
  b2v = lo(b2_r); xres1 = lo(xres_r) + b2v; pin(b2v); pin(xres1);
  TS(9);
  if (!wocu) {
    {
      const u32x4 w1r[2] = {rA[0], rA[1]};
      u32x4 fr[2];
      make_frags(L, X, w1r, fr, 2048);
      wA[0] = fr[0]; wA[1] = fr[1];
    }
#pragma unroll
    for (int f = 0; f < 8; ++f) {
      const unsigned r0 = f < 4 ? rA[2 + (f >> 1)][(2 * f) & 3] : rB[2 * f - 8], r1 = f < 4 ? rA[2 + (f >> 1)][(2 * f + 1) & 3] : rB[2 * f - 7];
      wA[2 + (f >> 2)][f & 3] = (t & 1) ? __builtin_amdgcn_perm(r1, r0, 0x07060302u) : __builtin_amdgcn_perm(r1, r0, 0x05040100u);
    }
    pin(wA[0]); pin(wA[1]); pin(wA[2]); pin(wA[3]);
    b1v = lo(b1r); pin(b1v);
    const u32x4 ones = {ONE2, ONE2, ONE2, ONE2};
    f32x4 S = mfma(wA[0], ones, f32x4{0.f, 0.f, 0.f, 0.f});
    S = mfma(wA[1], ones, S);
    if (c == 0) {
#pragma unroll
      for (int i = 0; i < 4; ++i) L.cs1p[4 * g + i][w] = S[i];
    }
  }
  if (w == 6) {
    unsigned pa, pb;
    {
      LWAIT();
      while (__hip_atomic_load(&L.prb[2], __ATOMIC_RELAXED, __HIP_MEMORY_SCOPE_WORKGROUP) == 0u) __builtin_amdgcn_s_sleep(1);
      const unsigned p0 = L.prb[0], p1 = L.prb[1];
      const bool near0 = p0 > 250u && p0 < PCLEAN && p1 > p0 + 100u, near1 = p1 > 250u && p1 < PCLEAN && p0 > p1 + 100u;
      pa = near0 ? 1u : 2u; pb = near0 ? 2u : 1u;
      if (near0 || near1) goto probed;
      pa = p0; pb = p1;
      if (wocu) goto probed;
    }
    for (int tries = 0; tries < 8; ++tries) {
      u64 t0, t1, t2;
      st128x2(rws, l == 0 ? X.aPr : OOB, 0, u32x4{SENT, SENT, SENT, SENT});
      asm volatile("s_waitcnt vmcnt(0)\n\ts_memtime %0\n\ts_waitcnt lgkmcnt(0)" : "=s"(t0) :: "memory");
      const unsigned v0 = ld32<SC1>(rws, X.aPr, 0);
      asm volatile("s_waitcnt vmcnt(0)\n\ts_memtime %0\n\ts_waitcnt lgkmcnt(0)" : "=s"(t1) : "v"(v0) : "memory");
      const unsigned v1 = ld32<SC1>(rws, X.aPr, CPY);
      asm volatile("s_waitcnt vmcnt(0)\n\ts_memtime %0\n\ts_waitcnt lgkmcnt(0)" : "=s"(t2) : "v"(v1) : "memory");
      pa = (unsigned)(t1 - t0); pb = (unsigned)(t2 - t1);
      const unsigned mn = pa < pb ? pa : pb, mxp = pa < pb ? pb : pa;
      if ((mn < 600u && mxp > mn + 90u && mxp < 1000u) || (long long)(rtime() - (tref + PDL)) > 0) break;
    }
  probed:
    if (l == 0) { L.prb[3] = pa < pb ? 0u : (unsigned)CPY; }
    TSV(46, pa * 10000 + pb);
  }
  mlp_stage<0>(L, X, p, tref, tpubB, wocu, xf, wA, cs1, b1v, xres1, []() {});
  TS(11);
  {
    const float x2 = c_gather<0>(L, X, p, [&]() {
      const unsigned char* st = (const unsigned char*)&L.wqs[0][0];
#pragma unroll
      for (int ci = 0; ci < 2; ++ci)
#pragma unroll
        for (int j2 = 0; j2 < 4; ++j2) {
          const int d0 = 32 * ci + 8 * g + 2 * j2;
          const unsigned a = *(const u16*)(st + (d0 >> 2) * 1024 + (d0 & 3) * 256 + (16 * w + c) * 2), bb = *(const u16*)(st + ((d0 + 1) >> 2) * 1024 + ((d0 + 1) & 3) * 256 + (16 * w + c) * 2);
          wqB[ci][j2] = a | (bb << 16);
        }
      pin(wqB[0]); pin(wqB[1]);
    });
    const unsigned hh = pk2(x2, x2);
    const unsigned ll = pk2(x2 - lo(hh), 0.f);
    LP(u16, xhl, X.o_xw) = (u16)hh; LP(u16, xhl[0][1], X.o_xw) = (u16)ll;
    LWAIT();
    const u32x4 a0 = LP(const u32x4, xhl, X.o_xr), a1 = LP(const u32x4, xhl[0][0][32], X.o_xr);
    f32x4 d = mfma(a0, wqB[0], f32x4{0.f, 0.f, 0.f, 0.f});
    d = mfma(a1, wqB[1], d);
    const float q1 = d[0] + d[1];
    const unsigned pk = sanu(pk2(q1, dpp<XOR1>(q1)));
    const u32x4 v = {pk, dppu<SHL + 2>(pk), dppu<SHL + 4>(pk), dppu<SHL + 6>(pk)};
    st128x2(rws, X.aDq, 0, v);
    if (w == 0) {
      const unsigned xu = san(x2);
      const u32x4 vx = {xu, dppu<0x55>(xu), dppu<0xAA>(xu), dppu<0xFF>(xu)};
      st128x2(rws, X.aDx, 0, vx);
    } else if (w == 1) {
      const u32x4 ones = {ONE2, ONE2, ONE2, ONE2};
      const f32x4 z = {0.f, 0.f, 0.f, 0.f};
      f32x4 G = mfma(a0, a0, z), s1 = mfma(a0, ones, z);
      G = mfma(a1, a1, G); s1 = mfma(a1, ones, s1);
      float s2 = G[0] + G[1]; s2 += dpp<XOR1>(s2);
      st128x2(rws, X.aDx, 0, u32x4{san(s1[0] + s1[1]), san(s2), 0u, 0u});
    }
  }
  TS(13);
  {
    u32x4 r4 = {0u, 0u, 0u, 0u};
    pin(r4);
    BAR();
    __builtin_amdgcn_s_sleep(SLP_D);
    TS(38);
    gather_4(r4, X.aDg, rws, X.sd, 0x1FFFFFFFFull);
    FENCE();
    {
      f32x4 sk[4];
#pragma unroll
      for (int T = 0; T < 4; ++T) sk[T] = f32x4{0.f, 0.f, 0.f, 0.f};
#pragma unroll
      for (int i = 0; i < 4; ++i) {
        const u32x4 cf = *(const u32x4*)&L.csb[16 * i + 4 * g];
#pragma unroll
        for (int T = 0; T < 4; ++T) sk[T] = mfma(cf, kf[4 * T + i], sk[T]);
      }
      kcs = sel4(g, sk[0][0], sk[1][0], sk[2][0], sk[3][0]); pin(kcs);
    }
    FENCE();
    VMWAIT(0, "+v"(r4));
    {
      bool bad = mx4(r4) == SENT;
      TSV(39, __any(bad) ? 2 : 1);
      if (UNLIKELY(__any(bad))) {
        for (unsigned sp_ = 0; sp_ < SPIN_MAX; ++sp_) {
          TMO(4);
          gather_4(r4, X.aDg, rws, X.sd, __ballot(bad));
          VMWAIT(0, "+v"(r4));
          bad = mx4(r4) == SENT;
          if (!__any(bad)) break;
        }
      }
    }
    TS(14);
    if (l <= 32) *(u32x4*)((unsigned char*)&L + X.o_dd) = r4;
    BAR();
    u32x4 qf[4];
#pragma unroll
    for (int i = 0; i < 4; ++i) qf[i] = LP(const u32x4, qrec[0][16 * i], X.o_qr);
    const f32x2 st2 = LP(const f32x2, st2, X.o_st);
    f32x4 sc[4];
#pragma unroll
    for (int T = 0; T < 4; ++T) sc[T] = f32x4{0.f, 0.f, 0.f, 0.f};
#pragma unroll
    for (int i = 0; i < 4; ++i)
#pragma unroll
      for (int T = 0; T < 4; ++T) sc[T] = mfma(qf[i], kf[4 * T + i], sc[T]);
    const float mean = sum8l(st2[0]) * (1.f / 512);
    const float ms = sum8l(st2[1]) * (1.f / 512);
    const float scale = __builtin_amdgcn_rsqf(fmx(ms - mean * mean, 0.f) + 1e-5f) * QS;
    const float ta = tr16(sum4(sc[0]), sum4(sc[1])), tb = tr16(sum4(sc[2]), sum4(sc[3]));
    const float tsel = ((g & 2) ? tb : ta) - mean * kcs;
    const float tmax = mx32r(mx16r(max16(tsel)));
    TS(15);
    pv_publish<1>(L, X, p, tsel, tmax, scale, vr, msk, []() {});
  }
  tref = rtime();
  TS(16);
  float xres2 = 0.f;
  auto shadow2 = [&]() {
    xres2 = L.xfull[64 * s + xcol] + b2v; pin(xres2);
    if (!wocu) {
#pragma unroll
      for (int ci = 0; ci < 2; ++ci) {
        const f32x4 p0 = *(const f32x4*)&L.xfull[64 * w + 32 * ci + 8 * g], p1 = *(const f32x4*)&L.xfull[64 * w + 32 * ci + 8 * g + 4];
        const float xv8[8] = {p0[0], p0[1], p0[2], p0[3], p1[0], p1[1], p1[2], p1[3]};
#pragma unroll
        for (int j = 0; j < 4; ++j) {
          const unsigned hh = pk2(xv8[2 * j], xv8[2 * j + 1]);
          const unsigned ll = pk2(xv8[2 * j] - lo(hh), xv8[2 * j + 1] - hi(hh));
          xf[ci][j] = c == 0 ? hh : c == 1 ? ll : 0u;
        }
      }
      pin(xf[0]); pin(xf[1]);
    }
  };
  if (wocu) wo_stage<1, 0>(L, X, p, wA, shadow2); else shadow2();
  const u64 tpubB2 = rtime();
  TS(19);
  const bool fin = (xg == 0 && idx >= 24);
  if (u < 0 && !fin) { TSDUMP(); return; }
  mlp_stage<1>(L, X, p, tref, tpubB2, wocu, xf, wA, cs1, b1v, xres2, []() {});
  TS(21);
  if (!fin) { TSDUMP(); return; }
  const float o = c_gather<1>(L, X, p, []() {});
  if (w == 0) {
    p.out[64 * s + l] = (u16)(pk2(o, o) & 0xffffu);
    if (s == 0 && l == 0) *(u64*)p.ws = epoch;
  }
  TS(23);
  TSDUMP();
}

extern "C" void kernel_launch(void* const* d_in, const int* in_sizes, int n_in, void* d_out, int out_size, void* d_ws, size_t ws_size, hipStream_t stream) {
  Params p;
  p.x = (const u16*)d_in[0]; p.K = (const u16*)d_in[1]; p.V = (const u16*)d_in[2]; p.wq = (const u16*)d_in[3]; p.wo = (const u16*)d_in[4];
  p.w1 = (const u16*)d_in[6]; p.b1 = (const u16*)d_in[7]; p.w2 = (const u16*)d_in[8]; p.b2 = (const u16*)d_in[9];
  p.out = (u16*)d_out; p.ws = (unsigned char*)d_ws;
  hipLaunchKernelGGL(fused_kernel, dim3(NCU), dim3(NTHR), 0, stream, p);
}
```

```cpp
#include <hip/hip_runtime.h>
#include <stdint.h>

typedef unsigned short u16;
typedef unsigned long long u64;
typedef unsigned u32x4 __attribute__((ext_vector_type(4)));
typedef unsigned u32x2 __attribute__((ext_vector_type(2)));
typedef float f32x4 __attribute__((ext_vector_type(4)));
typedef float f32x2 __attribute__((ext_vector_type(2)));
typedef short bf16x8 __attribute__((ext_vector_type(8)));
typedef __bf16 bf16x2 __attribute__((ext_vector_type(2)));
typedef __amdgpu_buffer_rsrc_t rsrc_t;

#define KV_AUX 2
#define KVNT " nt"
#define W_AUX 2
#define SLP_A 4
#define SLP_B 4
#define SLP_AB 36
#define RSLP 0
#define RS1 0
#define PDL 30
#define PCLEAN 620u
#define DL_B1 110
#define DL_B2 150
#define DL_X 150
#define DL_CP 115
#define DL_CB 70
#define SLP_C 4
#define SLP_CN 40
#define SLP_D 4

constexpr int DM = 512, NH = 32, SK = 4096, DH = 128, FF = 1376;
constexpr int NCU = 256, NTHR = 512, NU = 86;
constexpr unsigned SENT = 0xFFFFFFFFu;
constexpr unsigned SPIN_MAX = 1u << 17;
constexpr int SC1 = 16;
constexpr float QS = 0.08838834764831845f * 1.4426950408889634f;
constexpr unsigned ONE2 = 0x3F803F80u;
constexpr int OOB = 0x40000000;

constexpr int CPY = 8192;
constexpr int A_SZ = 16 * 16384;
constexpr int WS_A = 0;
constexpr int B_SZ = 2 * 16384;
constexpr int WS_B = WS_A + 2 * A_SZ;
constexpr int C_SZ = 11 * 16384;
constexpr int WS_C = WS_B + 2 * B_SZ;
constexpr int WS_D2 = WS_C + 2 * C_SZ;
constexpr int WS_FLAGS = WS_D2 + 64 * 16384;
constexpr int WS_D1 = 2 << 20;
constexpr int WS_END = WS_D1 + NCU * 1024;
constexpr int WS_STAMP = 8 << 20;
static_assert(WS_FLAGS + 2048 <= WS_D1, "hand-off buffers fit one 2 MB page");
__device__ __forceinline__ int aoff(int r) { return WS_A + (r >> 4) * 16384 + (r & 15) * 512; }
__device__ __forceinline__ int doff(int r) { return WS_D2 + (r >> 2) * 16384 + (r & 3) * 2048; }

struct Params {
  const u16 *x, *K, *V, *wq, *wo, *w1, *b1, *w2, *b2;
  u16* out;
  unsigned char* ws;
};

__device__ __forceinline__ float lo(unsigned w) { return __builtin_bit_cast(float, w << 16); }
__device__ __forceinline__ float hi(unsigned w) { return __builtin_bit_cast(float, w & 0xffff0000u); }
__device__ __forceinline__ float asf(unsigned w) { return __builtin_bit_cast(float, w); }
__device__ __forceinline__ unsigned asu(float f) { return __builtin_bit_cast(unsigned, f); }
__device__ __forceinline__ unsigned pk2(float a, float b) { const bf16x2 v = {(__bf16)a, (__bf16)b}; return __builtin_bit_cast(unsigned, v); }
__device__ __forceinline__ unsigned sanu(unsigned u) { return u < 0xFFFFFFFEu ? u : 0xFFFFFFFEu; }
__device__ __forceinline__ unsigned san(float f) { return sanu(asu(f)); }
template <int CTRL> __device__ __forceinline__ unsigned dppu(unsigned x) { return (unsigned)__builtin_amdgcn_mov_dpp((int)x, CTRL, 0xf, 0xf, true); }
template <int CTRL> __device__ __forceinline__ float dpp(float x) { return asf(dppu<CTRL>(asu(x))); }
constexpr int XOR1 = 0xB1, XOR2 = 0x4E, HMIR = 0x141, MIR = 0x140, ROR8 = 0x128, BC = 0x150, SHL = 0x100;
__device__ __forceinline__ float tr32(float a, float b) {
  auto s = __builtin_amdgcn_permlane32_swap(asu(a), asu(b), false, false);
  return asf(s[0]) + asf(s[1]);
}
__device__ __forceinline__ float tr16(float a, float b) {
  auto s = __builtin_amdgcn_permlane16_swap(asu(a), asu(b), false, false);
  return asf(s[0]) + asf(s[1]);
}
template <int CTRL> __device__ __forceinline__ float trd(float a, float b, bool sel) {
  const float keep = sel ? b : a, send = sel ? a : b;
  return keep + dpp<CTRL>(send);
}
__device__ __forceinline__ float sum8l(float x) { x += dpp<XOR1>(x); x += dpp<XOR2>(x); x += dpp<HMIR>(x); return x; }
__device__ __forceinline__ float sum16(float x) { x = sum8l(x); x += dpp<MIR>(x); return x; }
__device__ __forceinline__ float sum64(float x) { x = sum16(x); x = tr16(x, x); x = tr32(x, x); return x; }
__device__ __forceinline__ float fmx(float a, float b) { return __builtin_amdgcn_fmed3f(a, b, __builtin_inff()); }
__device__ __forceinline__ float fmx3(float a, float b, float c) { return fmx(fmx(a, b), c); }
__device__ __forceinline__ float fmxa(float a, float b) { float r; asm("v_max_f32 %0, %1, %2" : "=v"(r) : "v"(a), "v"(b)); return r; }
#define MD(ctl) "s_nop 1\n\tv_max_f32_dpp %0, %0, %0 " ctl " row_mask:0xf bank_mask:0xf\n\t"
__device__ __forceinline__ float max8l(float x) { asm(MD("quad_perm:[1,0,3,2]") MD("quad_perm:[2,3,0,1]") MD("row_half_mirror") : "+v"(x)); return x; }
__device__ __forceinline__ float max16(float x) { asm(MD("quad_perm:[1,0,3,2]") MD("quad_perm:[2,3,0,1]") MD("row_half_mirror") MD("row_mirror") : "+v"(x)); return x; }
__device__ __forceinline__ float mx16r(float a) { auto s = __builtin_amdgcn_permlane16_swap(asu(a), asu(a), false, false); return fmxa(asf(s[0]), asf(s[1])); }
__device__ __forceinline__ float mx32r(float a) { auto s = __builtin_amdgcn_permlane32_swap(asu(a), asu(a), false, false); return fmxa(asf(s[0]), asf(s[1])); }
__device__ __forceinline__ float dot2(unsigned a, unsigned b, float c) {
  return __builtin_amdgcn_fdot2_f32_bf16(__builtin_bit_cast(bf16x2, a), __builtin_bit_cast(bf16x2, b), c, false);
}
__device__ __forceinline__ float ex2(float x) { return __builtin_amdgcn_exp2f(x); }
__device__ __forceinline__ f32x4 mfma(u32x4 a, u32x4 b, f32x4 c) {
  return __builtin_amdgcn_mfma_f32_16x16x32_bf16(__builtin_bit_cast(bf16x8, a), __builtin_bit_cast(bf16x8, b), c, 0, 0, 0);
}
__device__ __forceinline__ float sum4(f32x4 s) { return (s[0] + s[1]) + (s[2] + s[3]); }
__device__ __forceinline__ float sum8(f32x4 a, f32x4 b) { return sum4(a + b); }
__device__ __forceinline__ float sel4(int g, float a, float b, float c, float d) { const float x = (g & 1) ? b : a, y = (g & 1) ? d : c; return (g & 2) ? y : x; }

template <int AUX> __device__ __forceinline__ u32x4 ld128(rsrc_t r, int voff, int soff = 0) { return __builtin_amdgcn_raw_buffer_load_b128(r, voff, soff, AUX); }
template <int AUX> __device__ __forceinline__ u32x2 ld64(rsrc_t r, int voff, int soff = 0) { return __builtin_amdgcn_raw_buffer_load_b64(r, voff, soff, AUX); }
template <int AUX> __device__ __forceinline__ unsigned ld32(rsrc_t r, int voff, int soff = 0) { return __builtin_amdgcn_raw_buffer_load_b32(r, voff, soff, AUX); }
template <int AUX> __device__ __forceinline__ unsigned ld16(rsrc_t r, int voff, int soff = 0) { return __builtin_amdgcn_raw_buffer_load_b16(r, voff, soff, AUX); }
__device__ __forceinline__ void st128(rsrc_t r, int voff, int soff, u32x4 v) { __builtin_amdgcn_raw_buffer_store_b128(v, r, voff, soff, SC1); }
__device__ __forceinline__ void st128x2(rsrc_t r, int voff, int soff, u32x4 v) { st128(r, voff, soff, v); st128(r, voff, soff + CPY, v); }
__device__ __forceinline__ rsrc_t mk(const void* p, unsigned bytes) { return __builtin_amdgcn_make_buffer_rsrc((void*)p, 0, bytes, 0x00020000); }
__device__ __forceinline__ unsigned um(unsigned a, unsigned b) { return a > b ? a : b; }
__device__ __forceinline__ unsigned mx4(u32x4 v) { return um(um(v.x, v.y), um(v.z, v.w)); }
__device__ __forceinline__ unsigned mx2(u32x2 v) { return um(v.x, v.y); }
__device__ __forceinline__ int vsel(bool c, int a, int b) { asm("" : "+v"(a)); asm("" : "+v"(b)); return c ? a : b; }
template <typename T> __device__ __forceinline__ void pin(T& x) { asm volatile("" : "+v"(x)); }

__device__ __forceinline__ u64 uni64(u64 m) { return ((u64)(unsigned)__builtin_amdgcn_readfirstlane((unsigned)(m >> 32)) << 32) | (unsigned)__builtin_amdgcn_readfirstlane((unsigned)m); }
#define LD4(dst, off, imm) "buffer_load_dwordx4 %[" dst "], %[" off "], %[rs], %[so] offen offset:" imm " sc1\n\t"
#define LD2(dst, off, imm) "buffer_load_dwordx2 %[" dst "], %[" off "], %[rs], %[so] offen offset:" imm " sc1\n\t"
__device__ __forceinline__ void gather_442(u32x4& a, u32x4& b, u32x2& c, int oa, int oc, rsrc_t rs, int so, u64 m) {
  asm volatile("s_mov_b64 exec, %[m]\n\t" LD4("a", "oa", "0") LD4("b", "oa", "64") LD2("c", "oc", "0") "s_mov_b64 exec, -1"
               : [a] "+v"(a), [b] "+v"(b), [c] "+v"(c) : [oa] "v"(oa), [oc] "v"(oc), [rs] "s"(rs), [so] "s"(so), [m] "s"(uni64(m)) : "memory");
}
__device__ __forceinline__ void gather_42(u32x4& a, u32x2& c, int oa, int oc, rsrc_t rs, int so, u64 m) {
  asm volatile("s_mov_b64 exec, %[m]\n\t" LD4("a", "oa", "0") LD2("c", "oc", "0") "s_mov_b64 exec, -1"
               : [a] "+v"(a), [c] "+v"(c) : [oa] "v"(oa), [oc] "v"(oc), [rs] "s"(rs), [so] "s"(so), [m] "s"(uni64(m)) : "memory");
}
__device__ __forceinline__ void gather_44s(u32x4& a, u32x4& b, int oa, rsrc_t rs, int so, int so2, u64 m) {
  asm volatile("s_mov_b64 exec, %[m]\n\t" LD4("a", "oa", "0") "buffer_load_dwordx4 %[b], %[oa], %[rs], %[so2] offen sc1\n\ts_mov_b64 exec, -1"
               : [a] "+v"(a), [b] "+v"(b) : [oa] "v"(oa), [rs] "s"(rs), [so] "s"(so), [so2] "s"(so2), [m] "s"(uni64(m)) : "memory");
}
__device__ __forceinline__ void gather_442b(u32x4& a, u32x4& b, int oa, rsrc_t rs, int so, u64 m) {
  asm volatile("s_mov_b64 exec, %[m]\n\t" LD4("a", "oa", "0") LD4("b", "oa", "64") "s_mov_b64 exec, -1"
               : [a] "+v"(a), [b] "+v"(b) : [oa] "v"(oa), [rs] "s"(rs), [so] "s"(so), [m] "s"(uni64(m)) : "memory");
}
__device__ __forceinline__ void gather_44v(u32x4& a, u32x4& b, int oa, int ob, rsrc_t rs, int so, u64 m) {
  asm volatile("s_mov_b64 exec, %[m]\n\t" LD4("a", "oa", "0") LD4("b", "ob", "0") "s_mov_b64 exec, -1"
               : [a] "+v"(a), [b] "+v"(b) : [oa] "v"(oa), [ob] "v"(ob), [rs] "s"(rs), [so] "s"(so), [m] "s"(uni64(m)) : "memory");
}
__device__ __forceinline__ void gather_44(u32x4& a, u32x4& b, int oa, rsrc_t rs, int so, u64 m) {
  asm volatile("s_mov_b64 exec, %[m]\n\t" LD4("a", "oa", "0") LD4("b", "oa", "256") "s_mov_b64 exec, -1"
               : [a] "+v"(a), [b] "+v"(b) : [oa] "v"(oa), [rs] "s"(rs), [so] "s"(so), [m] "s"(uni64(m)) : "memory");
}
__device__ __forceinline__ void gather_4(u32x4& a, int oa, rsrc_t rs, int so, u64 m) {
  asm volatile("s_mov_b64 exec, %[m]\n\t" LD4("a", "oa", "0") "s_mov_b64 exec, -1"
               : [a] "+v"(a) : [oa] "v"(oa), [rs] "s"(rs), [so] "s"(so), [m] "s"(uni64(m)) : "memory");
}
__device__ __forceinline__ void gather_444(u32x4& a, u32x4& b, u32x4& c, int oa, int ob, int oc, rsrc_t rs, int so, u64 m) {
  asm volatile("s_mov_b64 exec, %[m]\n\t" LD4("a", "oa", "0") LD4("b", "ob", "0") LD4("c", "oc", "0") "s_mov_b64 exec, -1"
               : [a] "+v"(a), [b] "+v"(b), [c] "+v"(c) : [oa] "v"(oa), [ob] "v"(ob), [oc] "v"(oc), [rs] "s"(rs), [so] "s"(so), [m] "s"(uni64(m)) : "memory");
}
__device__ __forceinline__ u64 umask(bool on, u64 m) { const unsigned f = __builtin_amdgcn_readfirstlane(on ? ~0u : 0u); return m & (((u64)f << 32) | f); }
#define VMWAIT(N, ...) asm volatile("s_waitcnt vmcnt(" #N ")" : __VA_ARGS__ :: "memory")
#define TMO(code) do { } while (0)
#define UNLIKELY(x) __builtin_expect(!!(x), 0)

__device__ __forceinline__ int side_from(unsigned p0, unsigned p1) { return __builtin_amdgcn_readfirstlane(p0 < p1 ? 0 : CPY); }
__device__ __forceinline__ u64 rtime() { return __builtin_amdgcn_s_memrealtime(); }
__device__ __forceinline__ void wait_until(u64 t) { while ((long long)(rtime() - t) < 0) __builtin_amdgcn_s_sleep(1); }
#define FENCE() do { asm volatile("" ::: "memory"); __builtin_amdgcn_sched_barrier(0); } while (0)
#define BAR() do { asm volatile("s_waitcnt lgkmcnt(0)" ::: "memory"); __builtin_amdgcn_s_barrier(); asm volatile("" ::: "memory"); } while (0)
#define LWAIT() asm volatile("s_waitcnt lgkmcnt(0)" ::: "memory")
#define TS(i) do { } while (0)
#define TSV(i, v) do { } while (0)
#define TSDUMP() do { } while (0)

struct __attribute__((aligned(16))) Lds {
  union {
    float obuf[8][128];
    struct { float cbuf[64][8]; u16 xhl[8][2][64]; };
  };
  float ml[8][2];
  unsigned qb[64];
  unsigned csb[64];
  float ybuf[64][8];
  float sbuf[16][8];
  float s12[2][8];
  float cs1p[16][8];
  float xs[64];
  float xfull[512];
  unsigned qrec[8][72];
  float st2[8][4];
  unsigned prb[4];
  u64 tB[2];
  unsigned tBf[2];
  unsigned qflag;
  unsigned pad_[1];
  u32x4 wqs[16][64];
  unsigned char big[8][16384];
};
#define LP(T, field, off) (*(T*)((unsigned char*)&L.field + (off)))

struct Ctx {
  int t, b, w, l, g, c, xg, idx, h, s, cu, u;
  rsrc_t rws;
  int sd;
  const unsigned char* pl;
  int aPr, aA, aAml, aApub, aB0, aB2, aBpub, o_bw, o_br, o_yw2, aC0, aC1, aCpub, aDg, aDq, aDx;
  int o_ob, o_l8, o_ml, o_yw, o_c32, o_sw, o_cw, o_l32, o_l4, o_xw, o_xr, o_qr, o_st, o_dd, o_xs;
};
constexpr u64 M_C8 = 0x00FF00FF00FF00FFull;

__device__ __forceinline__ void make_frags(Lds& L, const Ctx& X, const u32x4 (&raw)[2], u32x4 (&fr)[2], int base) {
  unsigned char* st = &L.big[X.w][base];
#pragma unroll
  for (int i = 0; i < 2; ++i) *(u32x4*)(st + (32 * i + (X.l >> 1)) * 32 + (X.l & 1) * 16) = raw[i];
  LWAIT();
#pragma unroll
  for (int ci = 0; ci < 2; ++ci)
#pragma unroll
    for (int j2 = 0; j2 < 4; ++j2) {
      const unsigned a = *(const u16*)(st + (32 * ci + 8 * X.g + 2 * j2) * 32 + X.c * 2), bb = *(const u16*)(st + (32 * ci + 8 * X.g + 2 * j2 + 1) * 32 + X.c * 2);
      fr[ci][j2] = a | (bb << 16);
    }
  pin(fr[0]); pin(fr[1]);
}

template <int LAYER, typename HOOK>
__device__ __forceinline__ void pv_publish(Lds& L, const Ctx& X, const Params& p, float tsel, float tmax, float scale, const u32x4 (&vr)[16], const unsigned (&msk)[4], HOOK hook) {
  const int t = X.t, b = X.b, w = X.w, l = X.l;
  (void)b; (void)t;
  const float pe = ex2((tsel - tmax) * scale);
  const unsigned pd = pk2(pe, pe);
  if (LAYER == 0) TS(5);
  f32x4 o0 = {0.f, 0.f, 0.f, 0.f}, o1 = {0.f, 0.f, 0.f, 0.f};
#define PVS(n) { const unsigned pv = dppu<BC + n>(pd); const u32x4 a = {pv & msk[0], pv & msk[1], pv & msk[2], pv & msk[3]}; if (n & 1) o1 = mfma(a, vr[n], o1); else o0 = mfma(a, vr[n], o0); }
  PVS(0) PVS(1) PVS(2) PVS(3) PVS(4) PVS(5) PVS(6) PVS(7) PVS(8) PVS(9) PVS(10) PVS(11) PVS(12) PVS(13) PVS(14) PVS(15)
#undef PVS
  const float lsum = sum64(lo(pd));
  o0 += o1;
  if (l < 32) LP(f32x4, obuf, X.o_ob) = o0;
  if (l == 0) *(f32x2*)&L.ml[w][0] = f32x2{tmax * scale, lsum};
  hook();
  BAR();
  if (LAYER == 0 && w == 3) {
    u64 t0, t1, t2; unsigned d1, d2;
    asm volatile("s_memtime %0\n\ts_waitcnt lgkmcnt(0)" : "=s"(t0) :: "memory");
    asm volatile("s_load_dword %0, %2, 0x0\n\ts_waitcnt lgkmcnt(0)\n\ts_memtime %1\n\ts_waitcnt lgkmcnt(0)" : "=s"(d1), "=s"(t1) : "s"(X.pl) : "memory");
    asm volatile("s_load_dword %0, %2, 0x2000\n\ts_waitcnt lgkmcnt(0)\n\ts_memtime %1\n\ts_waitcnt lgkmcnt(0)" : "=s"(d2), "=s"(t2) : "s"(X.pl) : "memory");
    if (l == 0) { L.prb[0] = (unsigned)(t1 - t0); L.prb[1] = (unsigned)(t2 - t1); }
    LWAIT();
    if (l == 0) __hip_atomic_store(&L.prb[2], 1u, __ATOMIC_RELAXED, __HIP_MEMORY_SCOPE_WORKGROUP);
    TSV(45, (t1 - t0) * 10000 + (t2 - t1));
  }
  if (w == 0) {
    const f32x2 mlu = LP(const f32x2, ml, X.o_ml);
    f32x2 ob[8];
#pragma unroll
    for (int u = 0; u < 8; ++u) ob[u] = LP(const f32x2, obuf[u], X.o_l8);
    const float M = max8l(mlu[0]);
    const float f = ex2(mlu[0] - M);
    const float Ls = sum8l(f * mlu[1]);
    f32x2 o = dpp<BC + 0>(f) * ob[0];
    o += dpp<BC + 1>(f) * ob[1]; o += dpp<BC + 2>(f) * ob[2]; o += dpp<BC + 3>(f) * ob[3];
    o += dpp<BC + 4>(f) * ob[4]; o += dpp<BC + 5>(f) * ob[5]; o += dpp<BC + 6>(f) * ob[6]; o += dpp<BC + 7>(f) * ob[7];
    const unsigned pk = sanu(pk2(o[0], o[1]));
    u32x4 v = {pk, dppu<0x55>(pk), dppu<0xAA>(pk), dppu<0xFF>(pk)};
    if (l == 1) v = u32x4{san(M), san(Ls), 0u, 0u};
    st128x2(X.rws, X.aApub, LAYER * A_SZ, v);
  }
}

template <int LAYER, int NYOUNG, typename HOOK>
__device__ __forceinline__ void wo_stage(Lds& L, Ctx& X, const Params& p, const u32x4 (&wof)[4], HOOK hook) {
  const int t = X.t, b = X.b, w = X.w, l = X.l;
  (void)b; (void)t;
  const unsigned ini = X.c < 8 ? SENT : 0u;
  u32x4 a0 = {ini, ini, ini, ini}; u32x2 mlv = {ini, ini};
  pin(a0); pin(mlv);
  BAR();
  if (LAYER == 0) X.sd = side_from(L.prb[0], L.prb[1]);
  __builtin_amdgcn_s_sleep(SLP_A);
  TS(32 + LAYER * 8);
  gather_42(a0, mlv, X.aA, X.aAml, X.rws, LAYER * A_SZ + X.sd, M_C8);
  FENCE();
  hook();
  FENCE();
  VMWAIT(0, "+v"(a0), "+v"(mlv));
  {
    bool bad = um(mx4(a0), mx2(mlv)) == SENT;
    TSV(33 + LAYER * 8, __any(bad) ? 2 : 1);
    if (UNLIKELY(__any(bad))) {
      for (unsigned sp_ = 0; sp_ < SPIN_MAX; ++sp_) {
        TMO(1 + 10 * LAYER);
        if (LAYER == 0 && RS1) __builtin_amdgcn_s_sleep(RS1);
        gather_42(a0, mlv, X.aA, X.aAml, X.rws, LAYER * A_SZ + X.sd, __ballot(bad));
        VMWAIT(0, "+v"(a0), "+v"(mlv));
        bad = um(mx4(a0), mx2(mlv)) == SENT;
        if (!__any(bad)) break;
      }
    }
  }
  TS(LAYER * 10 + 8);
  f32x4 d[4];
#pragma unroll
  for (int n = 0; n < 4; ++n) d[n] = mfma(a0, wof[n], f32x4{0.f, 0.f, 0.f, 0.f});
  const float m = asf(mlv.x), ls = asf(mlv.y);
  const float M = max8l(m);
  const float e = ex2(m - M);
  const float f = e * __builtin_amdgcn_rcpf(sum8l(e * ls));
  const bool odd = (X.g & 1) != 0;
  const float F0 = odd ? dpp<BC + 4>(f) : dpp<BC + 0>(f), F1 = odd ? dpp<BC + 5>(f) : dpp<BC + 1>(f), F2 = odd ? dpp<BC + 6>(f) : dpp<BC + 2>(f), F3 = odd ? dpp<BC + 7>(f) : dpp<BC + 3>(f);
  float y[4];
#pragma unroll
  for (int n = 0; n < 4; ++n) y[n] = (F0 * d[n][0] + F1 * d[n][1]) + (F2 * d[n][2] + F3 * d[n][3]);
  const float ya = tr16(y[0], y[1]), yb = tr16(y[2], y[3]);
  if (l < 32) { LP(float, ybuf, X.o_yw) = ya; LP(float, ybuf[32], X.o_yw) = yb; }
  BAR();
  if (w == 0) {
    const float s = sum8(LP(const f32x4, ybuf, X.o_l32), LP(const f32x4, ybuf[0][4], X.o_l32));
    const unsigned pk = sanu(pk2(s, dpp<XOR1>(s)));
    const u32x4 v = {pk, dppu<SHL + 2>(pk), dppu<SHL + 4>(pk), dppu<SHL + 6>(pk)};
    st128x2(X.rws, X.aBpub, LAYER * B_SZ, v);
  }
}

template <int LAYER, typename HOOK>
__device__ __forceinline__ void mlp_stage(Lds& L, Ctx& X, const Params& p, u64 tref, u64 tpub, bool wocu, const u32x4 (&xf)[2], const u32x4 (&wA)[4], float& cs1, const float& b1v, const float& xres, HOOK hook) {
  const int t = X.t, b = X.b, w = X.w, l = X.l;
  (void)b; (void)t;
  u32x4 a0 = {0u, 0u, 0u, 0u}, a1 = a0, a2 = a0, a3 = a0;
  pin(a0); pin(a1); pin(a2); pin(a3);
  const bool mlp = X.u >= 0;
  const u64 m01 = umask(mlp, ~0ull), m2 = umask(w == 7, ~0ull);
  BAR();
  if (LAYER == 0) X.sd = __builtin_amdgcn_readfirstlane(L.prb[3]);
  TSV(47, X.sd + 1);
  wait_until(tref + (mlp ? (LAYER == 0 ? DL_B1 : DL_B2) : DL_X));
  TS(34 + LAYER * 8);
  gather_44s(a0, a1, X.aB0, X.rws, LAYER * B_SZ + X.sd, LAYER * B_SZ + X.sd + 16384, m01);
  gather_44s(a2, a3, X.aB2, X.rws, LAYER * B_SZ + X.sd, LAYER * B_SZ + X.sd + 16384, m2);
  FENCE();
  hook();
  float S1x, s1x, s2x;
  {
    const u32x4 ones = {ONE2, ONE2, ONE2, ONE2};
    const f32x4 z = {0.f, 0.f, 0.f, 0.f};
    f32x4 S0 = mfma(wA[0], xf[0], z), G0 = mfma(xf[0], xf[0], z), s10 = mfma(ones, xf[0], z);
    S0 = mfma(wA[1], xf[1], S0); G0 = mfma(xf[1], xf[1], G0); s10 = mfma(ones, xf[1], s10);
    float S2[2];
#pragma unroll
    for (int j = 0; j < 2; ++j) S2[j] = trd<ROR8>(S0[j], S0[j + 2], (l & 8) != 0);
    S1x = trd<HMIR>(S2[0], S2[1], (l & 4) != 0);
    S1x += dpp<XOR2>(S1x); S1x += dpp<XOR1>(S1x);
    s1x = sum16(s10[0]);
    s2x = sum64(sum4(G0));
    pin(S1x); pin(s1x); pin(s2x);
  }
  FENCE();
  VMWAIT(0, "+v"(a0), "+v"(a1), "+v"(a2), "+v"(a3));
  bool waited;
  {
    bool bad = um(um(mx4(a0), mx4(a1)), um(mx4(a2), mx4(a3))) == SENT;
    TSV(35 + LAYER * 8, __any(bad) ? 2 : 1);
    waited = __any(bad);
    if (UNLIKELY(__any(bad))) {
      for (unsigned sp_ = 0; sp_ < SPIN_MAX; ++sp_) {
        TMO(2 + 10 * LAYER);
        if (LAYER == 0 && RS1) __builtin_amdgcn_s_sleep(RS1);
        const u64 bm = __ballot(bad);
        gather_44s(a0, a1, X.aB0, X.rws, LAYER * B_SZ + X.sd, LAYER * B_SZ + X.sd + 16384, bm & m01);
        gather_44s(a2, a3, X.aB2, X.rws, LAYER * B_SZ + X.sd, LAYER * B_SZ + X.sd + 16384, bm & m2);
        VMWAIT(0, "+v"(a0), "+v"(a1), "+v"(a2), "+v"(a3));
        bad = um(um(mx4(a0), mx4(a1)), um(mx4(a2), mx4(a3))) == SENT;
        if (!__any(bad)) break;
      }
    }
  }
  TS(LAYER * 10 + 10);
  if (mlp) {
    LP(u32x4, big, X.o_bw) = a0; LP(u32x4, big[0][8 * 144], X.o_bw) = a1;
    LWAIT();
    a0 = LP(const u32x4, big, X.o_br); a1 = LP(const u32x4, big[0][64], X.o_br);
    const u32x4 ones = {ONE2, ONE2, ONE2, ONE2};
    const f32x4 z = {0.f, 0.f, 0.f, 0.f};
    f32x4 S = mfma(wA[0], a0, z), G = mfma(a0, a0, z), Gx = mfma(xf[0], a0, z), s1 = mfma(ones, a0, z);
    S = mfma(wA[1], a1, S); G = mfma(a1, a1, G); Gx = mfma(xf[1], a1, Gx); s1 = mfma(ones, a1, s1);
    asm volatile("" : "+v"(S), "+v"(G), "+v"(Gx), "+v"(s1));
    G += 2.f * Gx;
    float S2[2];
#pragma unroll
    for (int j = 0; j < 2; ++j) S2[j] = trd<ROR8>(S[j], S[j + 2], (l & 8) != 0);
    float S1 = trd<HMIR>(S2[0], S2[1], (l & 4) != 0);
    S1 += dpp<XOR2>(S1); S1 += dpp<XOR1>(S1);
    S1 += S1x;
    const float s2t = sum64(sum4(G)) + s2x;
    const float s1t = sum16(s1[0]) + s1x;
    if ((l & 3) == 0) LP(float, sbuf, X.o_sw) = S1;
    if (l == 0) { L.s12[0][w] = s1t; L.s12[1][w] = s2t; }
    BAR();
    const float St = sum8(LP(const f32x4, sbuf, X.o_c32), LP(const f32x4, sbuf[0][4], X.o_c32));
    if (LAYER == 0) { cs1 = sum8(LP(const f32x4, cs1p, X.o_c32), LP(const f32x4, cs1p[0][4], X.o_c32)); pin(cs1); }
    const float sv = sum8l(LP(const float, s12, X.c * 4)) * (1.f / 512);
    const float mean = dpp<BC + 0>(sv), ms = dpp<BC + 8>(sv);
    const float rstd = __builtin_amdgcn_rsqf(fmx(ms - mean * mean, 0.f) + 1e-5f);
    const float hp = rstd * (St - mean * cs1) + b1v;
    const float uu = hp * (0.7978845608028654f + 0.7978845608028654f * 0.044715f * hp * hp);
    const float ee = ex2(uu * (2.f * 1.4426950408889634f));
    const float hv = hp - hp * __builtin_amdgcn_rcpf(ee + 1.f);
    const unsigned hp2 = pk2(hv, dpp<XOR1>(hv));
    unsigned hb[8] = {dppu<BC + 0>(hp2), dppu<BC + 2>(hp2), dppu<BC + 4>(hp2), dppu<BC + 6>(hp2), dppu<BC + 8>(hp2), dppu<BC + 10>(hp2), dppu<BC + 12>(hp2), dppu<BC + 14>(hp2)};
    asm volatile("" : "+v"(hb[0]), "+v"(hb[1]), "+v"(hb[2]), "+v"(hb[3]), "+v"(hb[4]), "+v"(hb[5]), "+v"(hb[6]), "+v"(hb[7]));
    float acc0 = 0.f, acc1 = 0.f;
#pragma unroll
    for (int F = 0; F < 8; F += 2) { acc0 = dot2(hb[F], wA[2 + (F >> 2)][F & 3], acc0); acc1 = dot2(hb[F + 1], wA[2 + ((F + 1) >> 2)][(F + 1) & 3], acc1); }
    const float av = acc0 + acc1;
    const unsigned pk = sanu(pk2(av, dpp<XOR1>(av)));
    const u32x4 v = {pk, dppu<SHL + 2>(pk), dppu<SHL + 4>(pk), dppu<SHL + 6>(pk)};
    st128x2(X.rws, X.aCpub, LAYER * C_SZ, v);
    TS(LAYER * 10 + 17);
  }
  if (!mlp && w == 7) {
    const u64 ta = waited ? rtime() + DL_CB : 0ull, tc = wocu ? tpub + DL_CP : 0ull, tb = ta > tc ? ta : tc;
    if (l == 0) { L.tB[LAYER] = tb; } LWAIT(); if (l == 0) __hip_atomic_store(&L.tBf[LAYER], 1u, __ATOMIC_RELAXED, __HIP_MEMORY_SCOPE_WORKGROUP); }
  if (w == 7) {
    float o[8];
#pragma unroll
    for (int j = 0; j < 4; ++j) { o[2 * j] = lo(a2[j]) + lo(a3[j]); o[2 * j + 1] = hi(a2[j]) + hi(a3[j]); }
    float o4[4], oo2[2];
#pragma unroll
    for (int j = 0; j < 4; ++j) o4[j] = tr32(o[j], o[j + 4]);
#pragma unroll
    for (int j = 0; j < 2; ++j) oo2[j] = tr16(o4[j], o4[j + 2]);
    const float v = trd<ROR8>(oo2[0], oo2[1], (l & 8) != 0);
    LP(float, xs, X.o_xs) = v + xres;
  }
  if (mlp) __builtin_amdgcn_s_sleep(SLP_C);
  else {
    for (unsigned sp_ = 0; sp_ < (SPIN_MAX << 4); ++sp_) { if (__hip_atomic_load(&L.tBf[LAYER], __ATOMIC_RELAXED, __HIP_MEMORY_SCOPE_WORKGROUP) != 0u) break; __builtin_amdgcn_s_sleep(2); }
    asm volatile("" ::: "memory");
    const u64 tb = L.tB[LAYER];
    wait_until(uni64(tb));
  }
}

template <int LAYER, typename HOOK>
__device__ __forceinline__ float c_gather(Lds& L, const Ctx& X, const Params& p, HOOK hook) {
  const int t = X.t, b = X.b, w = X.w;
  (void)b; (void)t; (void)w;
  u32x4 a0, a1;
  asm volatile("" : "=v"(a0), "=v"(a1));
  TS(36 + LAYER * 8);
  gather_44v(a0, a1, X.aC0, X.aC1, X.rws, LAYER * C_SZ + X.sd, ~0ull);
  FENCE();
  hook();
  FENCE();
  VMWAIT(0, "+v"(a0), "+v"(a1));
  {
    bool bad = um(mx4(a0), mx4(a1)) == SENT;
    TSV(37 + LAYER * 8, __any(bad) ? 2 : 1);
    if (UNLIKELY(__any(bad))) {
      for (unsigned sp_ = 0; sp_ < SPIN_MAX; ++sp_) {
        TMO(3 + 10 * LAYER);
        gather_44v(a0, a1, X.aC0, X.aC1, X.rws, LAYER * C_SZ + X.sd, __ballot(bad));
        VMWAIT(0, "+v"(a0), "+v"(a1));
        bad = um(mx4(a0), mx4(a1)) == SENT;
        if (!__any(bad)) break;
      }
    }
  }
  TS(LAYER * 10 + 12);
  float o[8];
#pragma unroll
  for (int j = 0; j < 4; ++j) { o[2 * j] = lo(a0[j]) + lo(a1[j]); o[2 * j + 1] = hi(a0[j]) + hi(a1[j]); }
  float o4[4], o2[2];
#pragma unroll
  for (int j = 0; j < 4; ++j) o4[j] = tr32(o[j], o[j + 4]);
#pragma unroll
  for (int j = 0; j < 2; ++j) o2[j] = tr16(o4[j], o4[j + 2]);
  LP(float, cbuf, X.o_cw) = trd<ROR8>(o2[0], o2[1], (X.l & 8) != 0);
  BAR();
  return sum8(LP(const f32x4, cbuf, X.o_l32), LP(const f32x4, cbuf[0][4], X.o_l32)) + LP(const float, xs, X.o_l4);
}

__global__ void __launch_bounds__(NTHR, 2) fused_kernel(Params p) {
  asm volatile("" : "+s"(p.x), "+s"(p.K), "+s"(p.V), "+s"(p.wq), "+s"(p.wo), "+s"(p.w1), "+s"(p.b1), "+s"(p.w2), "+s"(p.b2), "+s"(p.out), "+s"(p.ws));
  __shared__ Lds L;
  Ctx X;
  const int t = threadIdx.x, b = blockIdx.x;
  const int w = __builtin_amdgcn_readfirstlane(t >> 6), l = t & 63, g = l >> 4, c = l & 15;
  const int xg = b & 7, idx = b >> 3, s = idx & 7, cu = xg * 32 + idx;
  const int hg = cu >> 3;
  const int h = hg;
  const bool wocu = idx >= 16;
  const int wpp = (idx >> 3) & 1, wj = idx & 7;
  int u = idx < 12 ? 4 * (xg + 8 * (idx >> 2)) + (idx & 3) : -1;
  if (u >= NU) u = -1;
  TS(0);
  unsigned char* const wsa = (unsigned char*)(((size_t)p.ws + (2u << 20)) & ~(size_t)((2u << 20) - 1));
  const rsrc_t rws = mk(wsa, WS_END), rep = mk(p.ws, 8);
  X.t = t; X.b = b; X.w = w; X.l = l; X.g = g; X.c = c; X.xg = xg; X.idx = idx; X.h = h; X.s = s; X.cu = cu; X.u = u; X.rws = rws;
  const rsrc_t rK = mk(p.K + (size_t)(h * 8 + s) * 65536, 131072), rV = mk(p.V + (size_t)(h * 8 + s) * 65536, 131072);
  const rsrc_t rx = mk(p.x, 1024), rwq = mk(p.wq, DM * NH * DH * 2), rwo = mk(p.wo, wocu ? DM * NH * DH * 2 : 0);
  const unsigned mz = u >= 0 ? 1u : 0u;
  const rsrc_t rw1 = mk(p.w1, mz * DM * FF * 2), rw2 = mk(p.w2, mz * DM * FF * 2), rb1 = mk(p.b1, mz * FF * 2), rb2 = mk(p.b2, DM * 2);
  const rsrc_t rd1 = mk(wsa + WS_D1, NCU * 1024);
  unsigned msk[4];
#pragma unroll
  for (int i = 0; i < 4; ++i) msk[i] = (c >> 1) == i ? ((c & 1) ? 0xFFFF0000u : 0x0000FFFFu) : 0u;
  const u32x2 ep = ld64<0>(rep, 0);
  if (w == 0) {
    const u32x4 xv = ld128<0>(rx, l * 16);
    const unsigned xq = ld16<0>(rx, (64 * s + 4 * c + g) * 2);
    u32x4 wqr[16];
#pragma unroll
    for (int n = 0; n < 16; ++n) wqr[n] = ld128<W_AUX>(rwq, (64 * s + g) * 8192 + h * 256 + c * 16, n * 4 * 8192);
    FENCE();
    __builtin_amdgcn_s_barrier();
    FENCE();
    float xs[8];
#pragma unroll
    for (int j = 0; j < 4; ++j) { xs[2 * j] = lo(xv[j]); xs[2 * j + 1] = hi(xv[j]); }
    const float mean = sum64(((xs[0] + xs[1]) + (xs[2] + xs[3])) + ((xs[4] + xs[5]) + (xs[6] + xs[7]))) * (1.f / 512);
    float d2 = 0.f;
#pragma unroll
    for (int j = 0; j < 8; ++j) { const float d = xs[j] - mean; d2 += d * d; }
    const float rstd = __builtin_amdgcn_rsqf(sum64(d2) * (1.f / 512) + 1e-5f);
    const float xn = (lo(xq) - mean) * rstd;
    const unsigned pd = pk2(xn, xn);
    const u32x4 onesA = {ONE2 & msk[0], ONE2 & msk[1], ONE2 & msk[2], ONE2 & msk[3]};
    f32x4 aq = {0.f, 0.f, 0.f, 0.f}, ac = {0.f, 0.f, 0.f, 0.f};
#define QST(n) { const unsigned pv = dppu<BC + n>(pd); const u32x4 a = {pv & msk[0], pv & msk[1], pv & msk[2], pv & msk[3]}; aq = mfma(a, wqr[n], aq); ac = mfma(onesA, wqr[n], ac); L.wqs[n][l] = wqr[n]; }
    QST(0) QST(1) QST(2) QST(3) QST(4) QST(5) QST(6) QST(7) QST(8) QST(9) QST(10) QST(11) QST(12) QST(13) QST(14) QST(15)
#undef QST
    const unsigned tag = ep.x + 1u;
    if (l < 32) {
      const int off = WS_D1 + cu * 1024 + (8 * c + 4 * g) * 8;
      st128(rws, off, 0, u32x4{pk2(aq[0], ac[0]), tag, pk2(aq[1], ac[1]), tag});
      st128(rws, off + 16, 0, u32x4{pk2(aq[2], ac[2]), tag, pk2(aq[3], ac[3]), tag});
    }
    TS(1);
  }
  if (w == 1) {
#pragma unroll
    for (int k = 0; k < 4; ++k) {
      const int q = l + 64 * k;
      int off = OOB;
      if (q < 17) off = aoff(cu) + q * 16;
      else if (q < 34) off = A_SZ + aoff(cu) + (q - 17) * 16;
      else if (q >= 40 && q < 56 && wocu) off = WS_B + ((q - 40) >> 3) * B_SZ + xg * 0 + (((2 * xg + wpp) >> 3) * 16384) + ((2 * xg + wpp) & 7) * 1024 + wj * 128 + (q & 7) * 16;
      else if (q >= 64 && q < 80) off = doff(cu) + ((q - 64) >> 1) * 128 + (q & 1) * 16;
      else if (q >= 80 && q < 97) off = doff(cu) + 1024 + (q - 80) * 16;
      else if (q == 97) off = doff(cu) + 1408;
      else if (q >= 128 && q < 256 && u >= 0) off = WS_C + ((q - 128) >> 6) * C_SZ + (u >> 3) * 16384 + (u & 7) * 1024 + ((q - 128) & 63) * 16;
      st128x2(rws, off, 0, u32x4{SENT, SENT, SENT, SENT});
    }
    FENCE();
    __builtin_amdgcn_s_barrier();
    asm volatile("s_waitcnt vmcnt(0)" ::: "memory");
    const u64 e1 = (((u64)ep.y << 32) | ep.x) + 1;
    if (l == 0) __builtin_amdgcn_raw_buffer_store_b64(u32x2{(unsigned)e1, (unsigned)(e1 >> 32)}, rws, WS_FLAGS + cu * 8, 0, SC1);
    TS(4);
  }
  if (t == 128) { L.qflag = 0u; L.prb[2] = 0u; L.tBf[0] = 0u; L.tBf[1] = 0u; }
  if (w >= 2) { LWAIT(); __builtin_amdgcn_s_barrier(); }
  FENCE();
  u32x4 kf[16], vr[16];
  {
    int vo[4];
#pragma unroll
    for (int j = 0; j < 4; ++j) vo[j] = w * 16384 + (4 * j + g) * 256 + ((c ^ (4 * j + g)) * 16);
    const unsigned ldsb = (unsigned)(size_t)(__attribute__((address_space(3))) unsigned char*)&L.big[0][0] + w * 16384;
    unsigned keep;
#define KD(j, so) "s_nop 0\n\tbuffer_load_dwordx4 %[v" #j "], %[rs], " so " offen" KVNT " lds\n\ts_add_u32 m0, m0, 0x400\n\t"
#define KD4(so) KD(0, so) KD(1, so) KD(2, so) KD(3, so)
    asm volatile("s_mov_b32 %[keep], m0\n\ts_mov_b32 m0, %[lb]\n\t" KD4("0") KD4("%[s1]") KD4("%[s2]") KD4("%[s3]") "s_mov_b32 m0, %[keep]"
                 : [keep] "=&s"(keep)
                 : [v0] "v"(vo[0]), [v1] "v"(vo[1]), [v2] "v"(vo[2]), [v3] "v"(vo[3]), [rs] "s"(rK), [lb] "s"(ldsb), [s1] "s"(4096), [s2] "s"(8192), [s3] "s"(12288)
                 : "memory", "scc");
#undef KD4
#undef KD
  }
  FENCE();
  u32x4 d1[8];
  if (w == 0) {
#pragma unroll
    for (int i = 0; i < 8; ++i) d1[i] = ld128<SC1>(rd1, hg * 8192 + l * 16, i * 1024);
  }
  FENCE();
  {
    const int uc0 = u >= 0 ? u : 0;
    const int rec = aoff(xg * 32 + (2 * wpp + (w >> 2)) * 8 + (l & 7));
    X.aA = rec + ((w & 3) * 32 + 8 * g) * 2; X.aAml = rec + 256;
    X.aApub = vsel((l & 3) == 0, aoff(cu) + l * 4, vsel(l == 1, aoff(cu) + 256, OOB));
    X.aB0 = WS_B + (l >> 3) * 1024 + w * 128 + (l & 7) * 16;
    X.aB2 = WS_B + (l >> 3) * 1024 + s * 128 + (l & 7) * 16;
    X.aBpub = vsel((l & 7) == 0, WS_B + ((2 * xg + wpp) >> 3) * 16384 + ((2 * xg + wpp) & 7) * 1024 + wj * 128 + l * 2, OOB);
    X.o_bw = w * 16384 + 8192 + (l >> 3) * 144 + (l & 7) * 16; X.o_br = w * 16384 + 8192 + c * 144 + g * 16;
    pin(X.o_bw); pin(X.o_br);
    const int cc = WS_C + w * 16384 + (l >> 3) * 1024 + s * 128 + (l & 7) * 16;
    X.aC0 = cc; X.aC1 = vsel(64 + 8 * w + (l >> 3) < NU, cc + 8 * 16384, OOB);
    X.aCpub = vsel((l & 7) == 0, WS_C + (uc0 >> 3) * 16384 + (uc0 & 7) * 1024 + w * 128 + l * 2, OOB);
    X.aPr = doff(cu) + 1408;
    X.pl = wsa + doff(cu) + 1408;
    if (w == 3) {
      unsigned d0;
      asm volatile("s_load_dword %0, %1, 0x0\n\ts_waitcnt lgkmcnt(0)" : "=s"(d0) : "s"(wsa + aoff(cu) + 384) : "memory");
    }
    X.aDg = doff(hg * 8 + w) + vsel(l < 16, (l >> 1) * 128 + (l & 1) * 16, vsel(l < 32, 768 + l * 16, 1280));
    X.aDq = vsel((l & 55) == 0, doff(cu) + w * 128 + l * 2, OOB);
    X.aDx = vsel(w == 0 ? (l & 3) == 0 : l == 0, doff(cu) + (w == 0 ? 1024 : 1280) + l * 4, OOB);
    pin(X.aA); pin(X.aAml); pin(X.aApub); pin(X.aB0); pin(X.aB2); pin(X.aBpub); pin(X.aC0); pin(X.aC1); pin(X.aCpub); pin(X.aDg); pin(X.aDq); pin(X.aDx);
    X.o_ob = (w * 128 + 8 * c + 4 * g) * 4; X.o_l8 = l * 8; X.o_ml = (l & 7) * 8;
    X.o_yw = ((16 * (g & 1) + c) * 8 + ((w + c) & 7)) * 4; X.o_c32 = c * 32;
    X.o_sw = ((4 * g + ((l >> 3) & 1) * 2 + ((l >> 2) & 1)) * 8 + ((w + 2 * g) & 7)) * 4;
    X.o_cw = ((8 * (l & 7) + (l >> 3)) * 8 + ((w + l) & 7)) * 4; X.o_l32 = l * 32; X.o_l4 = l * 4;
    X.o_xw = w * 256 + l * 2; X.o_xr = w * 256 + (c & 1) * 128 + g * 16;
    X.o_qr = (l & 7) * 288 + g * 16; X.o_st = (l & 7) * 16;
    X.o_dd = vsel(l < 16, (int)__builtin_offsetof(Lds, qrec) + w * 288 + l * 16, vsel(l < 32, (int)__builtin_offsetof(Lds, xfull) + w * 256 + (l - 16) * 16, (int)__builtin_offsetof(Lds, st2) + w * 16));
    X.o_xs = (8 * (l & 7) + (l >> 3)) * 4;
    pin(X.o_ob); pin(X.o_yw); pin(X.o_sw); pin(X.o_cw); pin(X.o_xr); pin(X.o_qr); pin(X.o_dd); pin(X.o_xs);
  }
  TS(28);
  LWAIT();
  __builtin_amdgcn_s_barrier();
  FENCE();
#pragma unroll
  for (int i = 0; i < 16; ++i) vr[i] = ld128<KV_AUX>(rV, w * 16384 + g * 4096 + c * 16, i * 256);
  FENCE();
  TS(30);
  const u64 epoch = (((u64)ep.y << 32) | ep.x) + 1;
  asm volatile("s_waitcnt vmcnt(16)" ::: "memory");
  {
    const unsigned char* st = L.big[w];
#pragma unroll
    for (int T = 0; T < 4; ++T)
#pragma unroll
      for (int i = 0; i < 4; ++i) kf[4 * T + i] = *(const u32x4*)(st + (16 * T + c) * 256 + (((4 * i + g) ^ c) * 16));
  }
  TS(2);
  if (w == 0) {
    const unsigned tag = (unsigned)epoch;
    unsigned bad = 0;
#pragma unroll
    for (int i = 0; i < 8; ++i) bad |= (d1[i].y ^ tag) | (d1[i].w ^ tag);
    if (UNLIKELY(!__all(bad == 0u))) {
      for (unsigned sp_ = 0; sp_ < SPIN_MAX; ++sp_) {
        asm volatile("" ::: "memory");
        bad = 0;
#pragma unroll
        for (int i = 0; i < 8; ++i) { d1[i] = ld128<SC1>(rd1, hg * 8192 + l * 16, i * 1024); bad |= (d1[i].y ^ tag) | (d1[i].w ^ tag); }
        if (__all(bad == 0u)) break;
      }
    }
    float qa = 0.f, qc = 0.f, ca = 0.f, cb = 0.f;
#pragma unroll
    for (int i = 0; i < 8; ++i) { qa += lo(d1[i].x); ca += hi(d1[i].x); qc += lo(d1[i].z); cb += hi(d1[i].z); }
    L.qb[l] = pk2(qa, qc);
    L.csb[l] = pk2(ca, cb);
    LWAIT();
    if (l == 0) __hip_atomic_store(&L.qflag, 1u, __ATOMIC_RELAXED, __HIP_MEMORY_SCOPE_WORKGROUP);
  } else {
    for (unsigned sp_ = 0; sp_ < (SPIN_MAX << 4); ++sp_) {
      if (__hip_atomic_load(&L.qflag, __ATOMIC_RELAXED, __HIP_MEMORY_SCOPE_WORKGROUP) != 0u) break;
      __builtin_amdgcn_s_sleep(1);
    }
  }
  asm volatile("" ::: "memory");
  TS(3);

  float kcs;
  u32x4 rA[4], wA[4], wqB[2], xf[2];
  unsigned rB[8], b1r, xres_r, b2_r;
  float cs1 = 0.f, b1v = 0.f, b2v = 0.f, xres1 = 0.f;
  const int uc = u >= 0 ? u : 0;
  const int xcol = 8 * (l & 7) + (l >> 3);
  {
    f32x4 sc[4];
#pragma unroll
    for (int T = 0; T < 4; ++T) sc[T] = f32x4{0.f, 0.f, 0.f, 0.f};
#pragma unroll
    for (int i = 0; i < 4; ++i) {
      const u32x4 qf = *(const u32x4*)&L.qb[16 * i + 4 * g];
#pragma unroll
      for (int T = 0; T < 4; ++T) sc[T] = mfma(qf, kf[4 * T + i], sc[T]);
    }
    const float tmax = max16(fmx(fmx3(sc[0][0], sc[1][0], sc[2][0]), sc[3][0]));
    const float tsel = sel4(g, sc[0][0], sc[1][0], sc[2][0], sc[3][0]);
    u32x4 fl = {0u, 0u, 0u, 0u};
    if (w >= 6) fl = ld128<SC1>(rws, WS_FLAGS + (t & 127) * 16);
    FENCE();
    FENCE();
    FENCE();
    pv_publish<0>(L, X, p, tsel, tmax, QS, vr, msk, [&]() {
      if (wocu) {
#pragma unroll
        for (int i = 0; i < 4; ++i) rA[i] = ld128<W_AUX>(rwo, ((4 * xg + 2 * wpp + (w >> 2)) * 128 + 32 * (w & 3) + 8 * i + (l >> 3)) * 1024 + wj * 128 + (l & 7) * 16);
      }
      const unsigned e0 = (unsigned)epoch, e1 = (unsigned)(epoch >> 32);
      if (UNLIKELY(w >= 6 && !__all(((fl.x ^ e0) | (fl.y ^ e1) | (fl.z ^ e0) | (fl.w ^ e1)) == 0u))) {
        for (unsigned sp_ = 0; sp_ < SPIN_MAX; ++sp_) {
          __builtin_amdgcn_s_sleep(16);
          asm volatile("" ::: "memory");
          fl = ld128<SC1>(rws, WS_FLAGS + (t & 127) * 16);
          TMO(5);
          if (__all(((fl.x ^ e0) | (fl.y ^ e1) | (fl.z ^ e0) | (fl.w ^ e1)) == 0u)) break;
        }
      }
    });
  }
  u64 tref = rtime();
  b1r = ld16<0>(rb1, (16 * uc + c) * 2);
  xres_r = ld16<0>(rx, (64 * s + xcol) * 2); b2_r = ld16<0>(rb2, (64 * s + xcol) * 2);
#pragma unroll
  for (int ci = 0; ci < 2; ++ci) xf[ci] = ld128<0>(rx, c == 0 ? (64 * w + 32 * ci + 8 * g) * 2 : 1024);
  if (!wocu) {
#pragma unroll
    for (int i = 0; i < 2; ++i) rA[i] = ld128<W_AUX>(rw1, (64 * w + 32 * i + (l >> 1)) * (FF * 2) + uc * 32 + (l & 1) * 16);
#pragma unroll
    for (int f = 0; f < 8; ++f) rA[2 + (f >> 2)][f & 3] = ld32<W_AUX>(rw2, (16 * uc + f) * 1024 + (t >> 1) * 4);
#pragma unroll
    for (int f = 0; f < 8; ++f) rB[f] = ld32<W_AUX>(rw2, (16 * uc + 8 + f) * 1024 + (t >> 1) * 4);
  }
  TS(6);
  if (wocu) {
    wo_stage<0, 0>(L, X, p, wA, [&]() {
      {
        unsigned char* st = L.big[w];
#pragma unroll
        for (int i = 0; i < 4; ++i) *(u32x4*)(st + (8 * i + (l >> 3)) * 128 + (l & 7) * 16) = rA[i];
        LWAIT();
#pragma unroll
        for (int n = 0; n < 4; ++n) {
          typedef short s16x4 __attribute__((ext_vector_type(4)));
          const unsigned base = (unsigned)(size_t)(__attribute__((address_space(3))) unsigned char*)st + (8 * g + (c >> 2)) * 128 + (16 * n + 4 * (c & 3)) * 2;
          const s16x4 lo4 = __builtin_amdgcn_ds_read_tr16_b64_v4i16((__attribute__((address_space(3))) s16x4*)(size_t)base), hi4 = __builtin_amdgcn_ds_read_tr16_b64_v4i16((__attribute__((address_space(3))) s16x4*)(size_t)(base + 512));
          const u32x2 l2 = __builtin_bit_cast(u32x2, lo4), h2 = __builtin_bit_cast(u32x2, hi4);
          wA[n] = u32x4{l2.x, l2.y, h2.x, h2.y}; pin(wA[n]);
        }
      }
      pin(wA[0]); pin(wA[1]); pin(wA[2]); pin(wA[3]);
    });
  }
  const u64 tpubB = rtime();
  b2v = lo(b2_r); xres1 = lo(xres_r) + b2v; pin(b2v); pin(xres1);
  TS(9);
  if (!wocu) {
    {
      const u32x4 w1r[2] = {rA[0], rA[1]};
      u32x4 fr[2];
      make_frags(L, X, w1r, fr, 2048);
      wA[0] = fr[0]; wA[1] = fr[1];
    }
#pragma unroll
    for (int f = 0; f < 8; ++f) {
      const unsigned r0 = f < 4 ? rA[2 + (f >> 1)][(2 * f) & 3] : rB[2 * f - 8], r1 = f < 4 ? rA[2 + (f >> 1)][(2 * f + 1) & 3] : rB[2 * f - 7];
      wA[2 + (f >> 2)][f & 3] = (t & 1) ? __builtin_amdgcn_perm(r1, r0, 0x07060302u) : __builtin_amdgcn_perm(r1, r0, 0x05040100u);
    }
    pin(wA[0]); pin(wA[1]); pin(wA[2]); pin(wA[3]);
    b1v = lo(b1r); pin(b1v);
    const u32x4 ones = {ONE2, ONE2, ONE2, ONE2};
    f32x4 S = mfma(wA[0], ones, f32x4{0.f, 0.f, 0.f, 0.f});
    S = mfma(wA[1], ones, S);
    if (c == 0) {
#pragma unroll
      for (int i = 0; i < 4; ++i) L.cs1p[4 * g + i][w] = S[i];
    }
  }
  if (w == 6) {
    unsigned pa, pb;
    {
      LWAIT();
      while (__hip_atomic_load(&L.prb[2], __ATOMIC_RELAXED, __HIP_MEMORY_SCOPE_WORKGROUP) == 0u) __builtin_amdgcn_s_sleep(1);
      const unsigned p0 = L.prb[0], p1 = L.prb[1];
      const bool near0 = p0 > 250u && p0 < PCLEAN && p1 > p0 + 100u, near1 = p1 > 250u && p1 < PCLEAN && p0 > p1 + 100u;
      pa = near0 ? 1u : 2u; pb = near0 ? 2u : 1u;
      if (near0 || near1) goto probed;
      pa = p0; pb = p1;
      if (wocu) goto probed;
    }
    for (int tries = 0; tries < 8; ++tries) {
      u64 t0, t1, t2;
      st128x2(rws, l == 0 ? X.aPr : OOB, 0, u32x4{SENT, SENT, SENT, SENT});
      asm volatile("s_waitcnt vmcnt(0)\n\ts_memtime %0\n\ts_waitcnt lgkmcnt(0)" : "=s"(t0) :: "memory");
      const unsigned v0 = ld32<SC1>(rws, X.aPr, 0);
      asm volatile("s_waitcnt vmcnt(0)\n\ts_memtime %0\n\ts_waitcnt lgkmcnt(0)" : "=s"(t1) : "v"(v0) : "memory");
      const unsigned v1 = ld32<SC1>(rws, X.aPr, CPY);
      asm volatile("s_waitcnt vmcnt(0)\n\ts_memtime %0\n\ts_waitcnt lgkmcnt(0)" : "=s"(t2) : "v"(v1) : "memory");
      pa = (unsigned)(t1 - t0); pb = (unsigned)(t2 - t1);
      const unsigned mn = pa < pb ? pa : pb, mxp = pa < pb ? pb : pa;
      if ((mn < 600u && mxp > mn + 90u && mxp < 1000u) || (long long)(rtime() - (tref + PDL)) > 0) break;
    }
  probed:
    if (l == 0) { L.prb[3] = pa < pb ? 0u : (unsigned)CPY; }
    TSV(46, pa * 10000 + pb);
  }
  mlp_stage<0>(L, X, p, tref, tpubB, wocu, xf, wA, cs1, b1v, xres1, []() {});
  TS(11);
  {
    const float x2 = c_gather<0>(L, X, p, [&]() {
      const unsigned char* st = (const unsigned char*)&L.wqs[0][0];
#pragma unroll
      for (int ci = 0; ci < 2; ++ci)
#pragma unroll
        for (int j2 = 0; j2 < 4; ++j2) {
          const int d0 = 32 * ci + 8 * g + 2 * j2;
          const unsigned a = *(const u16*)(st + (d0 >> 2) * 1024 + (d0 & 3) * 256 + (16 * w + c) * 2), bb = *(const u16*)(st + ((d0 + 1) >> 2) * 1024 + ((d0 + 1) & 3) * 256 + (16 * w + c) * 2);
          wqB[ci][j2] = a | (bb << 16);
        }
      pin(wqB[0]); pin(wqB[1]);
    });
    const unsigned hh = pk2(x2, x2);
    const unsigned ll = pk2(x2 - lo(hh), 0.f);
    LP(u16, xhl, X.o_xw) = (u16)hh; LP(u16, xhl[0][1], X.o_xw) = (u16)ll;
    LWAIT();
    const u32x4 a0 = LP(const u32x4, xhl, X.o_xr), a1 = LP(const u32x4, xhl[0][0][32], X.o_xr);
    f32x4 d = mfma(a0, wqB[0], f32x4{0.f, 0.f, 0.f, 0.f});
    d = mfma(a1, wqB[1], d);
    const float q1 = d[0] + d[1];
    const unsigned pk = sanu(pk2(q1, dpp<XOR1>(q1)));
    const u32x4 v = {pk, dppu<SHL + 2>(pk), dppu<SHL + 4>(pk), dppu<SHL + 6>(pk)};
    st128x2(rws, X.aDq, 0, v);
    if (w == 0) {
      const unsigned xu = san(x2);
      const u32x4 vx = {xu, dppu<0x55>(xu), dppu<0xAA>(xu), dppu<0xFF>(xu)};
      st128x2(rws, X.aDx, 0, vx);
    } else if (w == 1) {
      const u32x4 ones = {ONE2, ONE2, ONE2, ONE2};
      const f32x4 z = {0.f, 0.f, 0.f, 0.f};
      f32x4 G = mfma(a0, a0, z), s1 = mfma(a0, ones, z);
      G = mfma(a1, a1, G); s1 = mfma(a1, ones, s1);
      float s2 = G[0] + G[1]; s2 += dpp<XOR1>(s2);
      st128x2(rws, X.aDx, 0, u32x4{san(s1[0] + s1[1]), san(s2), 0u, 0u});
    }
  }
  TS(13);
  {
    u32x4 r4 = {0u, 0u, 0u, 0u};
    pin(r4);
    BAR();
    __builtin_amdgcn_s_sleep(SLP_D);
    TS(38);
    gather_4(r4, X.aDg, rws, X.sd, 0x1FFFFFFFFull);
    FENCE();
    {
      f32x4 sk[4];
#pragma unroll
      for (int T = 0; T < 4; ++T) sk[T] = f32x4{0.f, 0.f, 0.f, 0.f};
#pragma unroll
      for (int i = 0; i < 4; ++i) {
        const u32x4 cf = *(const u32x4*)&L.csb[16 * i + 4 * g];
#pragma unroll
        for (int T = 0; T < 4; ++T) sk[T] = mfma(cf, kf[4 * T + i], sk[T]);
      }
      kcs = sel4(g, sk[0][0], sk[1][0], sk[2][0], sk[3][0]); pin(kcs);
    }
    FENCE();
    VMWAIT(0, "+v"(r4));
    {
      bool bad = mx4(r4) == SENT;
      TSV(39, __any(bad) ? 2 : 1);
      if (UNLIKELY(__any(bad))) {
        for (unsigned sp_ = 0; sp_ < SPIN_MAX; ++sp_) {
          TMO(4);
          gather_4(r4, X.aDg, rws, X.sd, __ballot(bad));
          VMWAIT(0, "+v"(r4));
          bad = mx4(r4) == SENT;
          if (!__any(bad)) break;
        }
      }
    }
    TS(14);
    if (l <= 32) *(u32x4*)((unsigned char*)&L + X.o_dd) = r4;
    BAR();
    u32x4 qf[4];
#pragma unroll
    for (int i = 0; i < 4; ++i) qf[i] = LP(const u32x4, qrec[0][16 * i], X.o_qr);
    const f32x2 st2 = LP(const f32x2, st2, X.o_st);
    f32x4 sc[4];
#pragma unroll
    for (int T = 0; T < 4; ++T) sc[T] = f32x4{0.f, 0.f, 0.f, 0.f};
#pragma unroll
    for (int i = 0; i < 4; ++i)
#pragma unroll
      for (int T = 0; T < 4; ++T) sc[T] = mfma(qf[i], kf[4 * T + i], sc[T]);
    const float mean = sum8l(st2[0]) * (1.f / 512);
    const float ms = sum8l(st2[1]) * (1.f / 512);
    const float scale = __builtin_amdgcn_rsqf(fmx(ms - mean * mean, 0.f) + 1e-5f) * QS;
    const float ta = tr16(sum4(sc[0]), sum4(sc[1])), tb = tr16(sum4(sc[2]), sum4(sc[3]));
    const float tsel = ((g & 2) ? tb : ta) - mean * kcs;
    const float tmax = mx32r(mx16r(max16(tsel)));
    TS(15);
    pv_publish<1>(L, X, p, tsel, tmax, scale, vr, msk, []() {});
  }
  tref = rtime();
  TS(16);
  float xres2 = 0.f;
  auto shadow2 = [&]() {
    xres2 = L.xfull[64 * s + xcol] + b2v; pin(xres2);
    if (!wocu) {
#pragma unroll
      for (int ci = 0; ci < 2; ++ci) {
        const f32x4 p0 = *(const f32x4*)&L.xfull[64 * w + 32 * ci + 8 * g], p1 = *(const f32x4*)&L.xfull[64 * w + 32 * ci + 8 * g + 4];
        const float xv8[8] = {p0[0], p0[1], p0[2], p0[3], p1[0], p1[1], p1[2], p1[3]};
#pragma unroll
        for (int j = 0; j < 4; ++j) {
          const unsigned hh = pk2(xv8[2 * j], xv8[2 * j + 1]);
          const unsigned ll = pk2(xv8[2 * j] - lo(hh), xv8[2 * j + 1] - hi(hh));
          xf[ci][j] = c == 0 ? hh : c == 1 ? ll : 0u;
        }
      }
      pin(xf[0]); pin(xf[1]);
    }
  };
  if (wocu) wo_stage<1, 0>(L, X, p, wA, shadow2); else shadow2();
  const u64 tpubB2 = rtime();
  TS(19);
  const bool fin = (xg == 0 && idx >= 24);
  if (u < 0 && !fin) { TSDUMP(); return; }
  mlp_stage<1>(L, X, p, tref, tpubB2, wocu, xf, wA, cs1, b1v, xres2, []() {});
  TS(21);
  if (!fin) { TSDUMP(); return; }
  const float o = c_gather<1>(L, X, p, []() {});
  if (w == 0) {
    p.out[64 * s + l] = (u16)(pk2(o, o) & 0xffffu);
    if (s == 0 && l == 0) *(u64*)p.ws = epoch;
  }
  TS(23);
  TSDUMP();
}

extern "C" void kernel_launch(void* const* d_in, const int* in_sizes, int n_in, void* d_out, int out_size, void* d_ws, size_t ws_size, hipStream_t stream) {
  Params p;
  p.x = (const u16*)d_in[0]; p.K = (const u16*)d_in[1]; p.V = (const u16*)d_in[2]; p.wq = (const u16*)d_in[3]; p.wo = (const u16*)d_in[4];
  p.w1 = (const u16*)d_in[6]; p.b1 = (const u16*)d_in[7]; p.w2 = (const u16*)d_in[8]; p.b2 = (const u16*)d_in[9];
  p.out = (u16*)d_out; p.ws = (unsigned char*)d_ws;
  hipLaunchKernelGGL(fused_kernel, dim3(NCU), dim3(NTHR), 0, stream, p);
}
```

```cpp
#define AMPX 5
#include <hip/hip_runtime.h>
#include <stdint.h>

typedef unsigned short u16;
typedef unsigned long long u64;
typedef unsigned u32x4 __attribute__((ext_vector_type(4)));
typedef unsigned u32x2 __attribute__((ext_vector_type(2)));
typedef float f32x4 __attribute__((ext_vector_type(4)));
typedef float f32x2 __attribute__((ext_vector_type(2)));
typedef short bf16x8 __attribute__((ext_vector_type(8)));
typedef __bf16 bf16x2 __attribute__((ext_vector_type(2)));
typedef __amdgpu_buffer_rsrc_t rsrc_t;

#define KV_AUX 2
#define KVNT " nt"
#define W_AUX 2
#define SLP_A 4
#define SLP_B 4
#define SLP_AB 36
#define RSLP 0
#define RS1 0
#define PDL 30
#define PCLEAN 620u
#define DL_B1 110
#define DL_B2 150
#define DL_X 150
#define DL_CP 115
#define DL_CB 70
#define SLP_C 4
#define SLP_CN 40
#define SLP_D 4

constexpr int DM = 512, NH = 32, SK = 4096, DH = 128, FF = 1376;
constexpr int NCU = 256, NTHR = 512, NU = 86;
constexpr unsigned SENT = 0xFFFFFFFFu;
constexpr unsigned SPIN_MAX = 1u << 17;
constexpr int SC1 = 16;
constexpr float QS = 0.08838834764831845f * 1.4426950408889634f;
constexpr unsigned ONE2 = 0x3F803F80u;
constexpr int OOB = 0x40000000;

constexpr int CPY = 8192;
constexpr int A_SZ = 16 * 16384;
constexpr int WS_A = 0;
constexpr int B_SZ = 2 * 16384;
constexpr int WS_B = WS_A + 2 * A_SZ;
constexpr int C_SZ = 11 * 16384;
constexpr int WS_C = WS_B + 2 * B_SZ;
constexpr int WS_D2 = WS_C + 2 * C_SZ;
constexpr int WS_FLAGS = WS_D2 + 64 * 16384;
constexpr int WS_D1 = 2 << 20;
constexpr int WS_END = WS_D1 + NCU * 1024;
constexpr int WS_STAMP = 8 << 20;
static_assert(WS_FLAGS + 2048 <= WS_D1, "hand-off buffers fit one 2 MB page");
__device__ __forceinline__ int aoff(int r) { return WS_A + (r >> 4) * 16384 + (r & 15) * 512; }
__device__ __forceinline__ int doff(int r) { return WS_D2 + (r >> 2) * 16384 + (r & 3) * 2048; }

struct Params {
  const u16 *x, *K, *V, *wq, *wo, *w1, *b1, *w2, *b2;
  u16* out;
  unsigned char* ws;
};

__device__ __forceinline__ float lo(unsigned w) { return __builtin_bit_cast(float, w << 16); }
__device__ __forceinline__ float hi(unsigned w) { return __builtin_bit_cast(float, w & 0xffff0000u); }
__device__ __forceinline__ float asf(unsigned w) { return __builtin_bit_cast(float, w); }
__device__ __forceinline__ unsigned asu(float f) { return __builtin_bit_cast(unsigned, f); }
__device__ __forceinline__ unsigned pk2(float a, float b) { const bf16x2 v = {(__bf16)a, (__bf16)b}; return __builtin_bit_cast(unsigned, v); }
__device__ __forceinline__ unsigned sanu(unsigned u) { return u < 0xFFFFFFFEu ? u : 0xFFFFFFFEu; }
__device__ __forceinline__ unsigned san(float f) { return sanu(asu(f)); }
template <int CTRL> __device__ __forceinline__ unsigned dppu(unsigned x) { return (unsigned)__builtin_amdgcn_mov_dpp((int)x, CTRL, 0xf, 0xf, true); }
template <int CTRL> __device__ __forceinline__ float dpp(float x) { return asf(dppu<CTRL>(asu(x))); }
constexpr int XOR1 = 0xB1, XOR2 = 0x4E, HMIR = 0x141, MIR = 0x140, ROR8 = 0x128, BC = 0x150, SHL = 0x100;
__device__ __forceinline__ float tr32(float a, float b) {
  auto s = __builtin_amdgcn_permlane32_swap(asu(a), asu(b), false, false);
  return asf(s[0]) + asf(s[1]);
}
__device__ __forceinline__ float tr16(float a, float b) {
  auto s = __builtin_amdgcn_permlane16_swap(asu(a), asu(b), false, false);
  return asf(s[0]) + asf(s[1]);
}
template <int CTRL> __device__ __forceinline__ float trd(float a, float b, bool sel) {
  const float keep = sel ? b : a, send = sel ? a : b;
  return keep + dpp<CTRL>(send);
}
__device__ __forceinline__ float sum8l(float x) { x += dpp<XOR1>(x); x += dpp<XOR2>(x); x += dpp<HMIR>(x); return x; }
__device__ __forceinline__ float sum16(float x) { x = sum8l(x); x += dpp<MIR>(x); return x; }
__device__ __forceinline__ float sum64(float x) { x = sum16(x); x = tr16(x, x); x = tr32(x, x); return x; }
__device__ __forceinline__ float fmx(float a, float b) { return __builtin_amdgcn_fmed3f(a, b, __builtin_inff()); }
__device__ __forceinline__ float fmx3(float a, float b, float c) { return fmx(fmx(a, b), c); }
__device__ __forceinline__ float fmxa(float a, float b) { float r; asm("v_max_f32 %0, %1, %2" : "=v"(r) : "v"(a), "v"(b)); return r; }
#define MD(ctl) "s_nop 1\n\tv_max_f32_dpp %0, %0, %0 " ctl " row_mask:0xf bank_mask:0xf\n\t"
__device__ __forceinline__ float max8l(float x) { asm(MD("quad_perm:[1,0,3,2]") MD("quad_perm:[2,3,0,1]") MD("row_half_mirror") : "+v"(x)); return x; }
__device__ __forceinline__ float max16(float x) { asm(MD("quad_perm:[1,0,3,2]") MD("quad_perm:[2,3,0,1]") MD("row_half_mirror") MD("row_mirror") : "+v"(x)); return x; }
__device__ __forceinline__ float mx16r(float a) { auto s = __builtin_amdgcn_permlane16_swap(asu(a), asu(a), false, false); return fmxa(asf(s[0]), asf(s[1])); }
__device__ __forceinline__ float mx32r(float a) { auto s = __builtin_amdgcn_permlane32_swap(asu(a), asu(a), false, false); return fmxa(asf(s[0]), asf(s[1])); }
__device__ __forceinline__ float dot2(unsigned a, unsigned b, float c) {
  return __builtin_amdgcn_fdot2_f32_bf16(__builtin_bit_cast(bf16x2, a), __builtin_bit_cast(bf16x2, b), c, false);
}
__device__ __forceinline__ float ex2(float x) { return __builtin_amdgcn_exp2f(x); }
__device__ __forceinline__ f32x4 mfma(u32x4 a, u32x4 b, f32x4 c) {
  return __builtin_amdgcn_mfma_f32_16x16x32_bf16(__builtin_bit_cast(bf16x8, a), __builtin_bit_cast(bf16x8, b), c, 0, 0, 0);
}
__device__ __forceinline__ float sum4(f32x4 s) { return (s[0] + s[1]) + (s[2] + s[3]); }
__device__ __forceinline__ float sum8(f32x4 a, f32x4 b) { return sum4(a + b); }
__device__ __forceinline__ float sel4(int g, float a, float b, float c, float d) { const float x = (g & 1) ? b : a, y = (g & 1) ? d : c; return (g & 2) ? y : x; }

template <int AUX> __device__ __forceinline__ u32x4 ld128(rsrc_t r, int voff, int soff = 0) { return __builtin_amdgcn_raw_buffer_load_b128(r, voff, soff, AUX); }
template <int AUX> __device__ __forceinline__ u32x2 ld64(rsrc_t r, int voff, int soff = 0) { return __builtin_amdgcn_raw_buffer_load_b64(r, voff, soff, AUX); }
template <int AUX> __device__ __forceinline__ unsigned ld32(rsrc_t r, int voff, int soff = 0) { return __builtin_amdgcn_raw_buffer_load_b32(r, voff, soff, AUX); }
template <int AUX> __device__ __forceinline__ unsigned ld16(rsrc_t r, int voff, int soff = 0) { return __builtin_amdgcn_raw_buffer_load_b16(r, voff, soff, AUX); }
__device__ __forceinline__ void st128(rsrc_t r, int voff, int soff, u32x4 v) { __builtin_amdgcn_raw_buffer_store_b128(v, r, voff, soff, SC1); }
__device__ __forceinline__ void st128x2(rsrc_t r, int voff, int soff, u32x4 v) { st128(r, voff, soff, v); st128(r, voff, soff + CPY, v); }
__device__ __forceinline__ rsrc_t mk(const void* p, unsigned bytes) { return __builtin_amdgcn_make_buffer_rsrc((void*)p, 0, bytes, 0x00020000); }
__device__ __forceinline__ unsigned um(unsigned a, unsigned b) { return a > b ? a : b; }
__device__ __forceinline__ unsigned mx4(u32x4 v) { return um(um(v.x, v.y), um(v.z, v.w)); }
__device__ __forceinline__ unsigned mx2(u32x2 v) { return um(v.x, v.y); }
__device__ __forceinline__ int vsel(bool c, int a, int b) { asm("" : "+v"(a)); asm("" : "+v"(b)); return c ? a : b; }
template <typename T> __device__ __forceinline__ void pin(T& x) { asm volatile("" : "+v"(x)); }

__device__ __forceinline__ u64 uni64(u64 m) { return ((u64)(unsigned)__builtin_amdgcn_readfirstlane((unsigned)(m >> 32)) << 32) | (unsigned)__builtin_amdgcn_readfirstlane((unsigned)m); }
#define LD4(dst, off, imm) "buffer_load_dwordx4 %[" dst "], %[" off "], %[rs], %[so] offen offset:" imm " sc1\n\t"
#define LD2(dst, off, imm) "buffer_load_dwordx2 %[" dst "], %[" off "], %[rs], %[so] offen offset:" imm " sc1\n\t"
__device__ __forceinline__ void gather_442(u32x4& a, u32x4& b, u32x2& c, int oa, int oc, rsrc_t rs, int so, u64 m) {
  asm volatile("s_mov_b64 exec, %[m]\n\t" LD4("a", "oa", "0") LD4("b", "oa", "64") LD2("c", "oc", "0") "s_mov_b64 exec, -1"
               : [a] "+v"(a), [b] "+v"(b), [c] "+v"(c) : [oa] "v"(oa), [oc] "v"(oc), [rs] "s"(rs), [so] "s"(so), [m] "s"(uni64(m)) : "memory");
}
__device__ __forceinline__ void gather_42(u32x4& a, u32x2& c, int oa, int oc, rsrc_t rs, int so, u64 m) {
  asm volatile("s_mov_b64 exec, %[m]\n\t" LD4("a", "oa", "0") LD2("c", "oc", "0") "s_mov_b64 exec, -1"
               : [a] "+v"(a), [c] "+v"(c) : [oa] "v"(oa), [oc] "v"(oc), [rs] "s"(rs), [so] "s"(so), [m] "s"(uni64(m)) : "memory");
}
__device__ __forceinline__ void gather_44s(u32x4& a, u32x4& b, int oa, rsrc_t rs, int so, int so2, u64 m) {
  asm volatile("s_mov_b64 exec, %[m]\n\t" LD4("a", "oa", "0") "buffer_load_dwordx4 %[b], %[oa], %[rs], %[so2] offen sc1\n\ts_mov_b64 exec, -1"
               : [a] "+v"(a), [b] "+v"(b) : [oa] "v"(oa), [rs] "s"(rs), [so] "s"(so), [so2] "s"(so2), [m] "s"(uni64(m)) : "memory");
}
__device__ __forceinline__ void gather_442b(u32x4& a, u32x4& b, int oa, rsrc_t rs, int so, u64 m) {
  asm volatile("s_mov_b64 exec, %[m]\n\t" LD4("a", "oa", "0") LD4("b", "oa", "64") "s_mov_b64 exec, -1"
               : [a] "+v"(a), [b] "+v"(b) : [oa] "v"(oa), [rs] "s"(rs), [so] "s"(so), [m] "s"(uni64(m)) : "memory");
}
__device__ __forceinline__ void gather_44v(u32x4& a, u32x4& b, int oa, int ob, rsrc_t rs, int so, u64 m) {
  asm volatile("s_mov_b64 exec, %[m]\n\t" LD4("a", "oa", "0") LD4("b", "ob", "0") "s_mov_b64 exec, -1"
               : [a] "+v"(a), [b] "+v"(b) : [oa] "v"(oa), [ob] "v"(ob), [rs] "s"(rs), [so] "s"(so), [m] "s"(uni64(m)) : "memory");
}
__device__ __forceinline__ void gather_44(u32x4& a, u32x4& b, int oa, rsrc_t rs, int so, u64 m) {
  asm volatile("s_mov_b64 exec, %[m]\n\t" LD4("a", "oa", "0") LD4("b", "oa", "256") "s_mov_b64 exec, -1"
               : [a] "+v"(a), [b] "+v"(b) : [oa] "v"(oa), [rs] "s"(rs), [so] "s"(so), [m] "s"(uni64(m)) : "memory");
}
__device__ __forceinline__ void gather_4(u32x4& a, int oa, rsrc_t rs, int so, u64 m) {
  asm volatile("s_mov_b64 exec, %[m]\n\t" LD4("a", "oa", "0") "s_mov_b64 exec, -1"
               : [a] "+v"(a) : [oa] "v"(oa), [rs] "s"(rs), [so] "s"(so), [m] "s"(uni64(m)) : "memory");
}
__device__ __forceinline__ void gather_444(u32x4& a, u32x4& b, u32x4& c, int oa, int ob, int oc, rsrc_t rs, int so, u64 m) {
  asm volatile("s_mov_b64 exec, %[m]\n\t" LD4("a", "oa", "0") LD4("b", "ob", "0") LD4("c", "oc", "0") "s_mov_b64 exec, -1"
               : [a] "+v"(a), [b] "+v"(b), [c] "+v"(c) : [oa] "v"(oa), [ob] "v"(ob), [oc] "v"(oc), [rs] "s"(rs), [so] "s"(so), [m] "s"(uni64(m)) : "memory");
}
__device__ __forceinline__ u64 umask(bool on, u64 m) { const unsigned f = __builtin_amdgcn_readfirstlane(on ? ~0u : 0u); return m & (((u64)f << 32) | f); }
#define VMWAIT(N, ...) asm volatile("s_waitcnt vmcnt(" #N ")" : __VA_ARGS__ :: "memory")
#define TMO(code) do { } while (0)
#define UNLIKELY(x) __builtin_expect(!!(x), 0)

__device__ __forceinline__ int side_from(unsigned p0, unsigned p1) { return __builtin_amdgcn_readfirstlane(p0 < p1 ? 0 : CPY); }
__device__ __forceinline__ u64 rtime() { return __builtin_amdgcn_s_memrealtime(); }
__device__ __forceinline__ void wait_until(u64 t) { while ((long long)(rtime() - t) < 0) __builtin_amdgcn_s_sleep(1); }
#define FENCE() do { asm volatile("" ::: "memory"); __builtin_amdgcn_sched_barrier(0); } while (0)
#define BAR() do { asm volatile("s_waitcnt lgkmcnt(0)" ::: "memory"); __builtin_amdgcn_s_barrier(); asm volatile("" ::: "memory"); } while (0)
#define LWAIT() asm volatile("s_waitcnt lgkmcnt(0)" ::: "memory")
#define TS(i) do { if ((i) == AMPX && (threadIdx.x & 63) == 0) __builtin_amdgcn_raw_buffer_store_b64(u32x2{(unsigned)(__builtin_amdgcn_s_memrealtime() - X.tk0), X.tag32}, X.rdbg, (blockIdx.x * 8 + (threadIdx.x >> 6)) * 8, 0, SC1); } while (0)
#define TSV(i, v) do { } while (0)
#define TSDUMP() do { } while (0)

struct __attribute__((aligned(16))) Lds {
  union {
    float obuf[8][128];
    struct { float cbuf[64][8]; u16 xhl[8][2][64]; };
  };
  float ml[8][2];
  unsigned qb[64];
  unsigned csb[64];
  float ybuf[64][8];
  float sbuf[16][8];
  float s12[2][8];
  float cs1p[16][8];
  float xs[64];
  float xfull[512];
  unsigned qrec[8][72];
  float st2[8][4];
  unsigned prb[4];
  u64 tB[2];
  unsigned tBf[2];
  unsigned qflag;
  unsigned pad_[1];
  u32x4 wqs[16][64];
  unsigned char big[8][16384];
};
#define LP(T, field, off) (*(T*)((unsigned char*)&L.field + (off)))

struct Ctx {
  int t, b, w, l, g, c, xg, idx, h, s, cu, u;
  rsrc_t rws;
  u64 tk0; unsigned tag32; rsrc_t rdbg;
  int sd;
  const unsigned char* pl;
  int aPr, aA, aAml, aApub, aB0, aB2, aBpub, o_bw, o_br, o_yw2, aC0, aC1, aCpub, aDg, aDq, aDx;
  int o_ob, o_l8, o_ml, o_yw, o_c32, o_sw, o_cw, o_l32, o_l4, o_xw, o_xr, o_qr, o_st, o_dd, o_xs;
};
constexpr u64 M_C8 = 0x00FF00FF00FF00FFull;

__device__ __forceinline__ void make_frags(Lds& L, const Ctx& X, const u32x4 (&raw)[2], u32x4 (&fr)[2], int base) {
  unsigned char* st = &L.big[X.w][base];
#pragma unroll
  for (int i = 0; i < 2; ++i) *(u32x4*)(st + (32 * i + (X.l >> 1)) * 32 + (X.l & 1) * 16) = raw[i];
  LWAIT();
#pragma unroll
  for (int ci = 0; ci < 2; ++ci)
#pragma unroll
    for (int j2 = 0; j2 < 4; ++j2) {
      const unsigned a = *(const u16*)(st + (32 * ci + 8 * X.g + 2 * j2) * 32 + X.c * 2), bb = *(const u16*)(st + (32 * ci + 8 * X.g + 2 * j2 + 1) * 32 + X.c * 2);
      fr[ci][j2] = a | (bb << 16);
    }
  pin(fr[0]); pin(fr[1]);
}

template <int LAYER, typename HOOK>
__device__ __forceinline__ void pv_publish(Lds& L, const Ctx& X, const Params& p, float tsel, float tmax, float scale, const u32x4 (&vr)[16], const unsigned (&msk)[4], HOOK hook) {
  const int t = X.t, b = X.b, w = X.w, l = X.l;
  (void)b; (void)t;
  const float pe = ex2((tsel - tmax) * scale);
  const unsigned pd = pk2(pe, pe);
  if (LAYER == 0) TS(5);
  f32x4 o0 = {0.f, 0.f, 0.f, 0.f}, o1 = {0.f, 0.f, 0.f, 0.f};
#define PVS(n) { const unsigned pv = dppu<BC + n>(pd); const u32x4 a = {pv & msk[0], pv & msk[1], pv & msk[2], pv & msk[3]}; if (n & 1) o1 = mfma(a, vr[n], o1); else o0 = mfma(a, vr[n], o0); }
  PVS(0) PVS(1) PVS(2) PVS(3) PVS(4) PVS(5) PVS(6) PVS(7) PVS(8) PVS(9) PVS(10) PVS(11) PVS(12) PVS(13) PVS(14) PVS(15)
#undef PVS
  const float lsum = sum64(lo(pd));
  o0 += o1;
  if (l < 32) LP(f32x4, obuf, X.o_ob) = o0;
  if (l == 0) *(f32x2*)&L.ml[w][0] = f32x2{tmax * scale, lsum};
  hook();
  BAR();
  if (LAYER == 0 && w == 3) {
    u64 t0, t1, t2; unsigned d1, d2;
    asm volatile("s_memtime %0\n\ts_waitcnt lgkmcnt(0)" : "=s"(t0) :: "memory");
    asm volatile("s_load_dword %0, %2, 0x0\n\ts_waitcnt lgkmcnt(0)\n\ts_memtime %1\n\ts_waitcnt lgkmcnt(0)" : "=s"(d1), "=s"(t1) : "s"(X.pl) : "memory");
    asm volatile("s_load_dword %0, %2, 0x2000\n\ts_waitcnt lgkmcnt(0)\n\ts_memtime %1\n\ts_waitcnt lgkmcnt(0)" : "=s"(d2), "=s"(t2) : "s"(X.pl) : "memory");
    if (l == 0) { L.prb[0] = (unsigned)(t1 - t0); L.prb[1] = (unsigned)(t2 - t1); }
    LWAIT();
    if (l == 0) __hip_atomic_store(&L.prb[2], 1u, __ATOMIC_RELAXED, __HIP_MEMORY_SCOPE_WORKGROUP);
    TSV(45, (t1 - t0) * 10000 + (t2 - t1));
  }
  if (w == 0) {
    const f32x2 mlu = LP(const f32x2, ml, X.o_ml);
    f32x2 ob[8];
#pragma unroll
    for (int u = 0; u < 8; ++u) ob[u] = LP(const f32x2, obuf[u], X.o_l8);
    const float M = max8l(mlu[0]);
    const float f = ex2(mlu[0] - M);
    const float Ls = sum8l(f * mlu[1]);
    f32x2 o = dpp<BC + 0>(f) * ob[0];
    o += dpp<BC + 1>(f) * ob[1]; o += dpp<BC + 2>(f) * ob[2]; o += dpp<BC + 3>(f) * ob[3];
    o += dpp<BC + 4>(f) * ob[4]; o += dpp<BC + 5>(f) * ob[5]; o += dpp<BC + 6>(f) * ob[6]; o += dpp<BC + 7>(f) * ob[7];
    const unsigned pk = sanu(pk2(o[0], o[1]));
    u32x4 v = {pk, dppu<0x55>(pk), dppu<0xAA>(pk), dppu<0xFF>(pk)};
    if (l == 1) v = u32x4{san(M), san(Ls), 0u, 0u};
    st128x2(X.rws, X.aApub, LAYER * A_SZ, v);
  }
}

template <int LAYER, int NYOUNG, typename HOOK>
__device__ __forceinline__ void wo_stage(Lds& L, Ctx& X, const Params& p, const u32x4 (&wof)[4], HOOK hook) {
  const int t = X.t, b = X.b, w = X.w, l = X.l;
  (void)b; (void)t;
  const unsigned ini = X.c < 8 ? SENT : 0u;
  u32x4 a0 = {ini, ini, ini, ini}; u32x2 mlv = {ini, ini};
  pin(a0); pin(mlv);
  BAR();
  if (LAYER == 0) X.sd = side_from(L.prb[0], L.prb[1]);
  __builtin_amdgcn_s_sleep(SLP_A);
  TS(32 + LAYER * 8);
  gather_42(a0, mlv, X.aA, X.aAml, X.rws, LAYER * A_SZ + X.sd, M_C8);
  FENCE();
  hook();
  FENCE();
  VMWAIT(0, "+v"(a0), "+v"(mlv));
  {
    bool bad = um(mx4(a0), mx2(mlv)) == SENT;
    TSV(33 + LAYER * 8, __any(bad) ? 2 : 1);
    if (UNLIKELY(__any(bad))) {
      for (unsigned sp_ = 0; sp_ < SPIN_MAX; ++sp_) {
        TMO(1 + 10 * LAYER);
        if (LAYER == 0 && RS1) __builtin_amdgcn_s_sleep(RS1);
        gather_42(a0, mlv, X.aA, X.aAml, X.rws, LAYER * A_SZ + X.sd, __ballot(bad));
        VMWAIT(0, "+v"(a0), "+v"(mlv));
        bad = um(mx4(a0), mx2(mlv)) == SENT;
        if (!__any(bad)) break;
      }
    }
  }
  TS(LAYER * 10 + 8);
  f32x4 d[4];
#pragma unroll
  for (int n = 0; n < 4; ++n) d[n] = mfma(a0, wof[n], f32x4{0.f, 0.f, 0.f, 0.f});
  const float m = asf(mlv.x), ls = asf(mlv.y);
  const float M = max8l(m);
  const float e = ex2(m - M);
  const float f = e * __builtin_amdgcn_rcpf(sum8l(e * ls));
  const bool odd = (X.g & 1) != 0;
  const float F0 = odd ? dpp<BC + 4>(f) : dpp<BC + 0>(f), F1 = odd ? dpp<BC + 5>(f) : dpp<BC + 1>(f), F2 = odd ? dpp<BC + 6>(f) : dpp<BC + 2>(f), F3 = odd ? dpp<BC + 7>(f) : dpp<BC + 3>(f);
  float y[4];
#pragma unroll
  for (int n = 0; n < 4; ++n) y[n] = (F0 * d[n][0] + F1 * d[n][1]) + (F2 * d[n][2] + F3 * d[n][3]);
  const float ya = tr16(y[0], y[1]), yb = tr16(y[2], y[3]);
  if (l < 32) { LP(float, ybuf, X.o_yw) = ya; LP(float, ybuf[32], X.o_yw) = yb; }
  BAR();
  if (w == 0) {
    const float s = sum8(LP(const f32x4, ybuf, X.o_l32), LP(const f32x4, ybuf[0][4], X.o_l32));
    const unsigned pk = sanu(pk2(s, dpp<XOR1>(s)));
    const u32x4 v = {pk, dppu<SHL + 2>(pk), dppu<SHL + 4>(pk), dppu<SHL + 6>(pk)};
    st128x2(X.rws, X.aBpub, LAYER * B_SZ, v);
  }
}

template <int LAYER, typename HOOK>
__device__ __forceinline__ void mlp_stage(Lds& L, Ctx& X, const Params& p, u64 tref, u64 tpub, bool wocu, const u32x4 (&xf)[2], const u32x4 (&wA)[4], float& cs1, const float& b1v, const float& xres, HOOK hook) {
  const int t = X.t, b = X.b, w = X.w, l = X.l;
  (void)b; (void)t;
  u32x4 a0 = {0u, 0u, 0u, 0u}, a1 = a0, a2 = a0, a3 = a0;
  pin(a0); pin(a1); pin(a2); pin(a3);
  const bool mlp = X.u >= 0;
  const u64 m01 = umask(mlp, ~0ull), m2 = umask(w == 7, ~0ull);
  BAR();
  if (LAYER == 0) X.sd = __builtin_amdgcn_readfirstlane(L.prb[3]);
  TSV(47, X.sd + 1);
  wait_until(tref + (mlp ? (LAYER == 0 ? DL_B1 : DL_B2) : DL_X));
  TS(34 + LAYER * 8);
  gather_44s(a0, a1, X.aB0, X.rws, LAYER * B_SZ + X.sd, LAYER * B_SZ + X.sd + 16384, m01);
  gather_44s(a2, a3, X.aB2, X.rws, LAYER * B_SZ + X.sd, LAYER * B_SZ + X.sd + 16384, m2);
  FENCE();
  hook();
  float S1x, s1x, s2x;
  {
    const u32x4 ones = {ONE2, ONE2, ONE2, ONE2};
    const f32x4 z = {0.f, 0.f, 0.f, 0.f};
    f32x4 S0 = mfma(wA[0], xf[0], z), G0 = mfma(xf[0], xf[0], z), s10 = mfma(ones, xf[0], z);
    S0 = mfma(wA[1], xf[1], S0); G0 = mfma(xf[1], xf[1], G0); s10 = mfma(ones, xf[1], s10);
    float S2[2];
#pragma unroll
    for (int j = 0; j < 2; ++j) S2[j] = trd<ROR8>(S0[j], S0[j + 2], (l & 8) != 0);
    S1x = trd<HMIR>(S2[0], S2[1], (l & 4) != 0);
    S1x += dpp<XOR2>(S1x); S1x += dpp<XOR1>(S1x);
    s1x = sum16(s10[0]);
    s2x = sum64(sum4(G0));
    pin(S1x); pin(s1x); pin(s2x);
  }
  FENCE();
  VMWAIT(0, "+v"(a0), "+v"(a1), "+v"(a2), "+v"(a3));
  bool waited;
  {
    bool bad = um(um(mx4(a0), mx4(a1)), um(mx4(a2), mx4(a3))) == SENT;
    TSV(35 + LAYER * 8, __any(bad) ? 2 : 1);
    waited = __any(bad);
    if (UNLIKELY(__any(bad))) {
      for (unsigned sp_ = 0; sp_ < SPIN_MAX; ++sp_) {
        TMO(2 + 10 * LAYER);
        if (LAYER == 0 && RS1) __builtin_amdgcn_s_sleep(RS1);
        const u64 bm = __ballot(bad);
        gather_44s(a0, a1, X.aB0, X.rws, LAYER * B_SZ + X.sd, LAYER * B_SZ + X.sd + 16384, bm & m01);
        gather_44s(a2, a3, X.aB2, X.rws, LAYER * B_SZ + X.sd, LAYER * B_SZ + X.sd + 16384, bm & m2);
        VMWAIT(0, "+v"(a0), "+v"(a1), "+v"(a2), "+v"(a3));
        bad = um(um(mx4(a0), mx4(a1)), um(mx4(a2), mx4(a3))) == SENT;
        if (!__any(bad)) break;
      }
    }
  }
  TS(LAYER * 10 + 10);
  if (mlp) {
    LP(u32x4, big, X.o_bw) = a0; LP(u32x4, big[0][8 * 144], X.o_bw) = a1;
    LWAIT();
    a0 = LP(const u32x4, big, X.o_br); a1 = LP(const u32x4, big[0][64], X.o_br);
    const u32x4 ones = {ONE2, ONE2, ONE2, ONE2};
    const f32x4 z = {0.f, 0.f, 0.f, 0.f};
    f32x4 S = mfma(wA[0], a0, z), G = mfma(a0, a0, z), Gx = mfma(xf[0], a0, z), s1 = mfma(ones, a0, z);
    S = mfma(wA[1], a1, S); G = mfma(a1, a1, G); Gx = mfma(xf[1], a1, Gx); s1 = mfma(ones, a1, s1);
    asm volatile("" : "+v"(S), "+v"(G), "+v"(Gx), "+v"(s1));
    G += 2.f * Gx;
    float S2[2];
#pragma unroll
    for (int j = 0; j < 2; ++j) S2[j] = trd<ROR8>(S[j], S[j + 2], (l & 8) != 0);
    float S1 = trd<HMIR>(S2[0], S2[1], (l & 4) != 0);
    S1 += dpp<XOR2>(S1); S1 += dpp<XOR1>(S1);
    S1 += S1x;
    const float s2t = sum64(sum4(G)) + s2x;
    const float s1t = sum16(s1[0]) + s1x;
    if ((l & 3) == 0) LP(float, sbuf, X.o_sw) = S1;
    if (l == 0) { L.s12[0][w] = s1t; L.s12[1][w] = s2t; }
    BAR();
    const float St = sum8(LP(const f32x4, sbuf, X.o_c32), LP(const f32x4, sbuf[0][4], X.o_c32));
    if (LAYER == 0) { cs1 = sum8(LP(const f32x4, cs1p, X.o_c32), LP(const f32x4, cs1p[0][4], X.o_c32)); pin(cs1); }
    const float sv = sum8l(LP(const float, s12, X.c * 4)) * (1.f / 512);
    const float mean = dpp<BC + 0>(sv), ms = dpp<BC + 8>(sv);
    const float rstd = __builtin_amdgcn_rsqf(fmx(ms - mean * mean, 0.f) + 1e-5f);
    const float hp = rstd * (St - mean * cs1) + b1v;
    const float uu = hp * (0.7978845608028654f + 0.7978845608028654f * 0.044715f * hp * hp);
    const float ee = ex2(uu * (2.f * 1.4426950408889634f));
    const float hv = hp - hp * __builtin_amdgcn_rcpf(ee + 1.f);
    const unsigned hp2 = pk2(hv, dpp<XOR1>(hv));
    unsigned hb[8] = {dppu<BC + 0>(hp2), dppu<BC + 2>(hp2), dppu<BC + 4>(hp2), dppu<BC + 6>(hp2), dppu<BC + 8>(hp2), dppu<BC + 10>(hp2), dppu<BC + 12>(hp2), dppu<BC + 14>(hp2)};
    asm volatile("" : "+v"(hb[0]), "+v"(hb[1]), "+v"(hb[2]), "+v"(hb[3]), "+v"(hb[4]), "+v"(hb[5]), "+v"(hb[6]), "+v"(hb[7]));
    float acc0 = 0.f, acc1 = 0.f;
#pragma unroll
    for (int F = 0; F < 8; F += 2) { acc0 = dot2(hb[F], wA[2 + (F >> 2)][F & 3], acc0); acc1 = dot2(hb[F + 1], wA[2 + ((F + 1) >> 2)][(F + 1) & 3], acc1); }
    const float av = acc0 + acc1;
    const unsigned pk = sanu(pk2(av, dpp<XOR1>(av)));
    const u32x4 v = {pk, dppu<SHL + 2>(pk), dppu<SHL + 4>(pk), dppu<SHL + 6>(pk)};
    st128x2(X.rws, X.aCpub, LAYER * C_SZ, v);
    TS(LAYER * 10 + 17);
  }
  if (!mlp && w == 7) {
    const u64 ta = waited ? rtime() + DL_CB : 0ull, tc = wocu ? tpub + DL_CP : 0ull, tb = ta > tc ? ta : tc;
    if (l == 0) { L.tB[LAYER] = tb; } LWAIT(); if (l == 0) __hip_atomic_store(&L.tBf[LAYER], 1u, __ATOMIC_RELAXED, __HIP_MEMORY_SCOPE_WORKGROUP); }
  if (w == 7) {
    float o[8];
#pragma unroll
    for (int j = 0; j < 4; ++j) { o[2 * j] = lo(a2[j]) + lo(a3[j]); o[2 * j + 1] = hi(a2[j]) + hi(a3[j]); }
    float o4[4], oo2[2];
#pragma unroll
    for (int j = 0; j < 4; ++j) o4[j] = tr32(o[j], o[j + 4]);
#pragma unroll
    for (int j = 0; j < 2; ++j) oo2[j] = tr16(o4[j], o4[j + 2]);
    const float v = trd<ROR8>(oo2[0], oo2[1], (l & 8) != 0);
    LP(float, xs, X.o_xs) = v + xres;
  }
  if (mlp) __builtin_amdgcn_s_sleep(SLP_C);
  else {
    for (unsigned sp_ = 0; sp_ < (SPIN_MAX << 4); ++sp_) { if (__hip_atomic_load(&L.tBf[LAYER], __ATOMIC_RELAXED, __HIP_MEMORY_SCOPE_WORKGROUP) != 0u) break; __builtin_amdgcn_s_sleep(2); }
    asm volatile("" ::: "memory");
    const u64 tb = L.tB[LAYER];
    wait_until(uni64(tb));
  }
}

template <int LAYER, typename HOOK>
__device__ __forceinline__ float c_gather(Lds& L, const Ctx& X, const Params& p, HOOK hook) {
  const int t = X.t, b = X.b, w = X.w;
  (void)b; (void)t; (void)w;
  u32x4 a0, a1;
  asm volatile("" : "=v"(a0), "=v"(a1));
  TS(36 + LAYER * 8);
  gather_44v(a0, a1, X.aC0, X.aC1, X.rws, LAYER * C_SZ + X.sd, ~0ull);
  FENCE();
  hook();
  FENCE();
  VMWAIT(0, "+v"(a0), "+v"(a1));
  {
    bool bad = um(mx4(a0), mx4(a1)) == SENT;
    TSV(37 + LAYER * 8, __any(bad) ? 2 : 1);
    if (UNLIKELY(__any(bad))) {
      for (unsigned sp_ = 0; sp_ < SPIN_MAX; ++sp_) {
        TMO(3 + 10 * LAYER);
        gather_44v(a0, a1, X.aC0, X.aC1, X.rws, LAYER * C_SZ + X.sd, __ballot(bad));
        VMWAIT(0, "+v"(a0), "+v"(a1));
        bad = um(mx4(a0), mx4(a1)) == SENT;
        if (!__any(bad)) break;
      }
    }
  }
  TS(LAYER * 10 + 12);
  float o[8];
#pragma unroll
  for (int j = 0; j < 4; ++j) { o[2 * j] = lo(a0[j]) + lo(a1[j]); o[2 * j + 1] = hi(a0[j]) + hi(a1[j]); }
  float o4[4], o2[2];
#pragma unroll
  for (int j = 0; j < 4; ++j) o4[j] = tr32(o[j], o[j + 4]);
#pragma unroll
  for (int j = 0; j < 2; ++j) o2[j] = tr16(o4[j], o4[j + 2]);
  LP(float, cbuf, X.o_cw) = trd<ROR8>(o2[0], o2[1], (X.l & 8) != 0);
  BAR();
  return sum8(LP(const f32x4, cbuf, X.o_l32), LP(const f32x4, cbuf[0][4], X.o_l32)) + LP(const float, xs, X.o_l4);
}

__global__ void __launch_bounds__(NTHR, 2) fused_kernel(Params p) {
  asm volatile("" : "+s"(p.x), "+s"(p.K), "+s"(p.V), "+s"(p.wq), "+s"(p.wo), "+s"(p.w1), "+s"(p.b1), "+s"(p.w2), "+s"(p.b2), "+s"(p.out), "+s"(p.ws));
  __shared__ Lds L;
  Ctx X;
  X.tk0 = __builtin_amdgcn_s_memrealtime(); X.tag32 = 0; X.rdbg = mk(p.ws + (16 << 20), 16384);
  const int t = threadIdx.x, b = blockIdx.x;
  const int w = __builtin_amdgcn_readfirstlane(t >> 6), l = t & 63, g = l >> 4, c = l & 15;
  const int xg = b & 7, idx = b >> 3, s = idx & 7, cu = xg * 32 + idx;
  const int hg = cu >> 3;
  const int h = hg;
  const bool wocu = idx >= 16;
  const int wpp = (idx >> 3) & 1, wj = idx & 7;
  int u = idx < 12 ? 4 * (xg + 8 * (idx >> 2)) + (idx & 3) : -1;
  if (u >= NU) u = -1;
  TS(0);
  unsigned char* const wsa = (unsigned char*)(((size_t)p.ws + (2u << 20)) & ~(size_t)((2u << 20) - 1));
  const rsrc_t rws = mk(wsa, WS_END), rep = mk(p.ws, 8);
  X.t = t; X.b = b; X.w = w; X.l = l; X.g = g; X.c = c; X.xg = xg; X.idx = idx; X.h = h; X.s = s; X.cu = cu; X.u = u; X.rws = rws;
  const rsrc_t rK = mk(p.K + (size_t)(h * 8 + s) * 65536, 131072), rV = mk(p.V + (size_t)(h * 8 + s) * 65536, 131072);
  const rsrc_t rx = mk(p.x, 1024), rwq = mk(p.wq, DM * NH * DH * 2), rwo = mk(p.wo, wocu ? DM * NH * DH * 2 : 0);
  const unsigned mz = u >= 0 ? 1u : 0u;
  const rsrc_t rw1 = mk(p.w1, mz * DM * FF * 2), rw2 = mk(p.w2, mz * DM * FF * 2), rb1 = mk(p.b1, mz * FF * 2), rb2 = mk(p.b2, DM * 2);
  const rsrc_t rd1 = mk(wsa + WS_D1, NCU * 1024);
  unsigned msk[4];
#pragma unroll
  for (int i = 0; i < 4; ++i) msk[i] = (c >> 1) == i ? ((c & 1) ? 0xFFFF0000u : 0x0000FFFFu) : 0u;
  const u32x2 ep = ld64<0>(rep, 0);
  if (w == 0) {
    const u32x4 xv = ld128<0>(rx, l * 16);
    const unsigned xq = ld16<0>(rx, (64 * s + 4 * c + g) * 2);
    u32x4 wqr[16];
#pragma unroll
    for (int n = 0; n < 16; ++n) wqr[n] = ld128<W_AUX>(rwq, (64 * s + g) * 8192 + h * 256 + c * 16, n * 4 * 8192);
    FENCE();
    __builtin_amdgcn_s_barrier();
    FENCE();
    float xs[8];
#pragma unroll
    for (int j = 0; j < 4; ++j) { xs[2 * j] = lo(xv[j]); xs[2 * j + 1] = hi(xv[j]); }
    const float mean = sum64(((xs[0] + xs[1]) + (xs[2] + xs[3])) + ((xs[4] + xs[5]) + (xs[6] + xs[7]))) * (1.f / 512);
    float d2 = 0.f;
#pragma unroll
    for (int j = 0; j < 8; ++j) { const float d = xs[j] - mean; d2 += d * d; }
    const float rstd = __builtin_amdgcn_rsqf(sum64(d2) * (1.f / 512) + 1e-5f);
    const float xn = (lo(xq) - mean) * rstd;
    const unsigned pd = pk2(xn, xn);
    const u32x4 onesA = {ONE2 & msk[0], ONE2 & msk[1], ONE2 & msk[2], ONE2 & msk[3]};
    f32x4 aq = {0.f, 0.f, 0.f, 0.f}, ac = {0.f, 0.f, 0.f, 0.f};
#define QST(n) { const unsigned pv = dppu<BC + n>(pd); const u32x4 a = {pv & msk[0], pv & msk[1], pv & msk[2], pv & msk[3]}; aq = mfma(a, wqr[n], aq); ac = mfma(onesA, wqr[n], ac); L.wqs[n][l] = wqr[n]; }
    QST(0) QST(1) QST(2) QST(3) QST(4) QST(5) QST(6) QST(7) QST(8) QST(9) QST(10) QST(11) QST(12) QST(13) QST(14) QST(15)
#undef QST
    const unsigned tag = ep.x + 1u;
    if (l < 32) {
      const int off = WS_D1 + cu * 1024 + (8 * c + 4 * g) * 8;
      st128(rws, off, 0, u32x4{pk2(aq[0], ac[0]), tag, pk2(aq[1], ac[1]), tag});
      st128(rws, off + 16, 0, u32x4{pk2(aq[2], ac[2]), tag, pk2(aq[3], ac[3]), tag});
    }
    TS(1);
  }
  if (w == 1) {
#pragma unroll
    for (int k = 0; k < 4; ++k) {
      const int q = l + 64 * k;
      int off = OOB;
      if (q < 17) off = aoff(cu) + q * 16;
      else if (q < 34) off = A_SZ + aoff(cu) + (q - 17) * 16;
      else if (q >= 40 && q < 56 && wocu) off = WS_B + ((q - 40) >> 3) * B_SZ + xg * 0 + (((2 * xg + wpp) >> 3) * 16384) + ((2 * xg + wpp) & 7) * 1024 + wj * 128 + (q & 7) * 16;
      else if (q >= 64 && q < 80) off = doff(cu) + ((q - 64) >> 1) * 128 + (q & 1) * 16;
      else if (q >= 80 && q < 97) off = doff(cu) + 1024 + (q - 80) * 16;
      else if (q == 97) off = doff(cu) + 1408;
      else if (q >= 128 && q < 256 && u >= 0) off = WS_C + ((q - 128) >> 6) * C_SZ + (u >> 3) * 16384 + (u & 7) * 1024 + ((q - 128) & 63) * 16;
      st128x2(rws, off, 0, u32x4{SENT, SENT, SENT, SENT});
    }
    FENCE();
    __builtin_amdgcn_s_barrier();
    asm volatile("s_waitcnt vmcnt(0)" ::: "memory");
    const u64 e1 = (((u64)ep.y << 32) | ep.x) + 1;
    if (l == 0) __builtin_amdgcn_raw_buffer_store_b64(u32x2{(unsigned)e1, (unsigned)(e1 >> 32)}, rws, WS_FLAGS + cu * 8, 0, SC1);
    TS(4);
  }
  if (t == 128) { L.qflag = 0u; L.prb[2] = 0u; L.tBf[0] = 0u; L.tBf[1] = 0u; }
  if (w >= 2) { LWAIT(); __builtin_amdgcn_s_barrier(); }
  FENCE();
  u32x4 kf[16], vr[16];
  {
    int vo[4];
#pragma unroll
    for (int j = 0; j < 4; ++j) vo[j] = w * 16384 + (4 * j + g) * 256 + ((c ^ (4 * j + g)) * 16);
    const unsigned ldsb = (unsigned)(size_t)(__attribute__((address_space(3))) unsigned char*)&L.big[0][0] + w * 16384;
    unsigned keep;
#define KD(j, so) "s_nop 0\n\tbuffer_load_dwordx4 %[v" #j "], %[rs], " so " offen" KVNT " lds\n\ts_add_u32 m0, m0, 0x400\n\t"
#define KD4(so) KD(0, so) KD(1, so) KD(2, so) KD(3, so)
    asm volatile("s_mov_b32 %[keep], m0\n\ts_mov_b32 m0, %[lb]\n\t" KD4("0") KD4("%[s1]") KD4("%[s2]") KD4("%[s3]") "s_mov_b32 m0, %[keep]"
                 : [keep] "=&s"(keep)
                 : [v0] "v"(vo[0]), [v1] "v"(vo[1]), [v2] "v"(vo[2]), [v3] "v"(vo[3]), [rs] "s"(rK), [lb] "s"(ldsb), [s1] "s"(4096), [s2] "s"(8192), [s3] "s"(12288)
                 : "memory", "scc");
#undef KD4
#undef KD
  }
  FENCE();
  u32x4 d1[8];
  if (w == 0) {
#pragma unroll
    for (int i = 0; i < 8; ++i) d1[i] = ld128<SC1>(rd1, hg * 8192 + l * 16, i * 1024);
  }
  FENCE();
  {
    const int uc0 = u >= 0 ? u : 0;
    const int rec = aoff(xg * 32 + (2 * wpp + (w >> 2)) * 8 + (l & 7));
    X.aA = rec + ((w & 3) * 32 + 8 * g) * 2; X.aAml = rec + 256;
    X.aApub = vsel((l & 3) == 0, aoff(cu) + l * 4, vsel(l == 1, aoff(cu) + 256, OOB));
    X.aB0 = WS_B + (l >> 3) * 1024 + w * 128 + (l & 7) * 16;
    X.aB2 = WS_B + (l >> 3) * 1024 + s * 128 + (l & 7) * 16;
    X.aBpub = vsel((l & 7) == 0, WS_B + ((2 * xg + wpp) >> 3) * 16384 + ((2 * xg + wpp) & 7) * 1024 + wj * 128 + l * 2, OOB);
    X.o_bw = w * 16384 + 8192 + (l >> 3) * 144 + (l & 7) * 16; X.o_br = w * 16384 + 8192 + c * 144 + g * 16;
    pin(X.o_bw); pin(X.o_br);
    const int cc = WS_C + w * 16384 + (l >> 3) * 1024 + s * 128 + (l & 7) * 16;
    X.aC0 = cc; X.aC1 = vsel(64 + 8 * w + (l >> 3) < NU, cc + 8 * 16384, OOB);
    X.aCpub = vsel((l & 7) == 0, WS_C + (uc0 >> 3) * 16384 + (uc0 & 7) * 1024 + w * 128 + l * 2, OOB);
    X.aPr = doff(cu) + 1408;
    X.pl = wsa + doff(cu) + 1408;
    if (w == 3) {
      unsigned d0;
      asm volatile("s_load_dword %0, %1, 0x0\n\ts_waitcnt lgkmcnt(0)" : "=s"(d0) : "s"(wsa + aoff(cu) + 384) : "memory");
    }
    X.aDg = doff(hg * 8 + w) + vsel(l < 16, (l >> 1) * 128 + (l & 1) * 16, vsel(l < 32, 768 + l * 16, 1280));
    X.aDq = vsel((l & 55) == 0, doff(cu) + w * 128 + l * 2, OOB);
    X.aDx = vsel(w == 0 ? (l & 3) == 0 : l == 0, doff(cu) + (w == 0 ? 1024 : 1280) + l * 4, OOB);
    pin(X.aA); pin(X.aAml); pin(X.aApub); pin(X.aB0); pin(X.aB2); pin(X.aBpub); pin(X.aC0); pin(X.aC1); pin(X.aCpub); pin(X.aDg); pin(X.aDq); pin(X.aDx);
    X.o_ob = (w * 128 + 8 * c + 4 * g) * 4; X.o_l8 = l * 8; X.o_ml = (l & 7) * 8;
    X.o_yw = ((16 * (g & 1) + c) * 8 + ((w + c) & 7)) * 4; X.o_c32 = c * 32;
    X.o_sw = ((4 * g + ((l >> 3) & 1) * 2 + ((l >> 2) & 1)) * 8 + ((w + 2 * g) & 7)) * 4;
    X.o_cw = ((8 * (l & 7) + (l >> 3)) * 8 + ((w + l) & 7)) * 4; X.o_l32 = l * 32; X.o_l4 = l * 4;
    X.o_xw = w * 256 + l * 2; X.o_xr = w * 256 + (c & 1) * 128 + g * 16;
    X.o_qr = (l & 7) * 288 + g * 16; X.o_st = (l & 7) * 16;
    X.o_dd = vsel(l < 16, (int)__builtin_offsetof(Lds, qrec) + w * 288 + l * 16, vsel(l < 32, (int)__builtin_offsetof(Lds, xfull) + w * 256 + (l - 16) * 16, (int)__builtin_offsetof(Lds, st2) + w * 16));
    X.o_xs = (8 * (l & 7) + (l >> 3)) * 4;
    pin(X.o_ob); pin(X.o_yw); pin(X.o_sw); pin(X.o_cw); pin(X.o_xr); pin(X.o_qr); pin(X.o_dd); pin(X.o_xs);
  }
  TS(28);
  LWAIT();
  __builtin_amdgcn_s_barrier();
  FENCE();
#pragma unroll
  for (int i = 0; i < 16; ++i) vr[i] = ld128<KV_AUX>(rV, w * 16384 + g * 4096 + c * 16, i * 256);
  FENCE();
  TS(30);
  const u64 epoch = (((u64)ep.y << 32) | ep.x) + 1;
  X.tag32 = (unsigned)epoch;
  asm volatile("s_waitcnt vmcnt(16)" ::: "memory");
  {
    const unsigned char* st = L.big[w];
#pragma unroll
    for (int T = 0; T < 4; ++T)
#pragma unroll
      for (int i = 0; i < 4; ++i) kf[4 * T + i] = *(const u32x4*)(st + (16 * T + c) * 256 + (((4 * i + g) ^ c) * 16));
  }
  TS(2);
  if (w == 0) {
    const unsigned tag = (unsigned)epoch;
    unsigned bad = 0;
#pragma unroll
    for (int i = 0; i < 8; ++i) bad |= (d1[i].y ^ tag) | (d1[i].w ^ tag);
    if (UNLIKELY(!__all(bad == 0u))) {
      for (unsigned sp_ = 0; sp_ < SPIN_MAX; ++sp_) {
        asm volatile("" ::: "memory");
        bad = 0;
#pragma unroll
        for (int i = 0; i < 8; ++i) { d1[i] = ld128<SC1>(rd1, hg * 8192 + l * 16, i * 1024); bad |= (d1[i].y ^ tag) | (d1[i].w ^ tag); }
        if (__all(bad == 0u)) break;
      }
    }
    float qa = 0.f, qc = 0.f, ca = 0.f, cb = 0.f;
#pragma unroll
    for (int i = 0; i < 8; ++i) { qa += lo(d1[i].x); ca += hi(d1[i].x); qc += lo(d1[i].z); cb += hi(d1[i].z); }
    L.qb[l] = pk2(qa, qc);
    L.csb[l] = pk2(ca, cb);
    LWAIT();
    if (l == 0) __hip_atomic_store(&L.qflag, 1u, __ATOMIC_RELAXED, __HIP_MEMORY_SCOPE_WORKGROUP);
  } else {
    for (unsigned sp_ = 0; sp_ < (SPIN_MAX << 4); ++sp_) {
      if (__hip_atomic_load(&L.qflag, __ATOMIC_RELAXED, __HIP_MEMORY_SCOPE_WORKGROUP) != 0u) break;
      __builtin_amdgcn_s_sleep(1);
    }
  }
  asm volatile("" ::: "memory");
  TS(3);

  float kcs;
  u32x4 rA[4], wA[4], wqB[2], xf[2];
  unsigned rB[8], b1r, xres_r, b2_r;
  float cs1 = 0.f, b1v = 0.f, b2v = 0.f, xres1 = 0.f;
  const int uc = u >= 0 ? u : 0;
  const int xcol = 8 * (l & 7) + (l >> 3);
  {
    f32x4 sc[4];
#pragma unroll
    for (int T = 0; T < 4; ++T) sc[T] = f32x4{0.f, 0.f, 0.f, 0.f};
#pragma unroll
    for (int i = 0; i < 4; ++i) {
      const u32x4 qf = *(const u32x4*)&L.qb[16 * i + 4 * g];
#pragma unroll
      for (int T = 0; T < 4; ++T) sc[T] = mfma(qf, kf[4 * T + i], sc[T]);
    }
    const float tmax = max16(fmx(fmx3(sc[0][0], sc[1][0], sc[2][0]), sc[3][0]));
    const float tsel = sel4(g, sc[0][0], sc[1][0], sc[2][0], sc[3][0]);
    u32x4 fl = {0u, 0u, 0u, 0u};
    if (w >= 6) fl = ld128<SC1>(rws, WS_FLAGS + (t & 127) * 16);
    FENCE();
    FENCE();
    FENCE();
    pv_publish<0>(L, X, p, tsel, tmax, QS, vr, msk, [&]() {
      if (wocu) {
#pragma unroll
        for (int i = 0; i < 4; ++i) rA[i] = ld128<W_AUX>(rwo, ((4 * xg + 2 * wpp + (w >> 2)) * 128 + 32 * (w & 3) + 8 * i + (l >> 3)) * 1024 + wj * 128 + (l & 7) * 16);
      }
      const unsigned e0 = (unsigned)epoch, e1 = (unsigned)(epoch >> 32);
      if (UNLIKELY(w >= 6 && !__all(((fl.x ^ e0) | (fl.y ^ e1) | (fl.z ^ e0) | (fl.w ^ e1)) == 0u))) {
        for (unsigned sp_ = 0; sp_ < SPIN_MAX; ++sp_) {
          __builtin_amdgcn_s_sleep(16);
          asm volatile("" ::: "memory");
          fl = ld128<SC1>(rws, WS_FLAGS + (t & 127) * 16);
          TMO(5);
          if (__all(((fl.x ^ e0) | (fl.y ^ e1) | (fl.z ^ e0) | (fl.w ^ e1)) == 0u)) break;
        }
      }
    });
  }
  u64 tref = rtime();
  b1r = ld16<0>(rb1, (16 * uc + c) * 2);
  xres_r = ld16<0>(rx, (64 * s + xcol) * 2); b2_r = ld16<0>(rb2, (64 * s + xcol) * 2);
#pragma unroll
  for (int ci = 0; ci < 2; ++ci) xf[ci] = ld128<0>(rx, c == 0 ? (64 * w + 32 * ci + 8 * g) * 2 : 1024);
  if (!wocu) {
#pragma unroll
    for (int i = 0; i < 2; ++i) rA[i] = ld128<W_AUX>(rw1, (64 * w + 32 * i + (l >> 1)) * (FF * 2) + uc * 32 + (l & 1) * 16);
#pragma unroll
    for (int f = 0; f < 8; ++f) rA[2 + (f >> 2)][f & 3] = ld32<W_AUX>(rw2, (16 * uc + f) * 1024 + (t >> 1) * 4);
#pragma unroll
    for (int f = 0; f < 8; ++f) rB[f] = ld32<W_AUX>(rw2, (16 * uc + 8 + f) * 1024 + (t >> 1) * 4);
  }
  TS(6);
  if (wocu) {
    wo_stage<0, 0>(L, X, p, wA, [&]() {
      {
        unsigned char* st = L.big[w];
#pragma unroll
        for (int i = 0; i < 4; ++i) *(u32x4*)(st + (8 * i + (l >> 3)) * 128 + (l & 7) * 16) = rA[i];
        LWAIT();
#pragma unroll
        for (int n = 0; n < 4; ++n) {
          typedef short s16x4 __attribute__((ext_vector_type(4)));
          const unsigned base = (unsigned)(size_t)(__attribute__((address_space(3))) unsigned char*)st + (8 * g + (c >> 2)) * 128 + (16 * n + 4 * (c & 3)) * 2;
          const s16x4 lo4 = __builtin_amdgcn_ds_read_tr16_b64_v4i16((__attribute__((address_space(3))) s16x4*)(size_t)base), hi4 = __builtin_amdgcn_ds_read_tr16_b64_v4i16((__attribute__((address_space(3))) s16x4*)(size_t)(base + 512));
          const u32x2 l2 = __builtin_bit_cast(u32x2, lo4), h2 = __builtin_bit_cast(u32x2, hi4);
          wA[n] = u32x4{l2.x, l2.y, h2.x, h2.y}; pin(wA[n]);
        }
      }
      pin(wA[0]); pin(wA[1]); pin(wA[2]); pin(wA[3]);
    });
  }
  const u64 tpubB = rtime();
  b2v = lo(b2_r); xres1 = lo(xres_r) + b2v; pin(b2v); pin(xres1);
  TS(9);
  if (!wocu) {
    {
      const u32x4 w1r[2] = {rA[0], rA[1]};
      u32x4 fr[2];
      make_frags(L, X, w1r, fr, 2048);
      wA[0] = fr[0]; wA[1] = fr[1];
    }
#pragma unroll
    for (int f = 0; f < 8; ++f) {
      const unsigned r0 = f < 4 ? rA[2 + (f >> 1)][(2 * f) & 3] : rB[2 * f - 8], r1 = f < 4 ? rA[2 + (f >> 1)][(2 * f + 1) & 3] : rB[2 * f - 7];
      wA[2 + (f >> 2)][f & 3] = (t & 1) ? __builtin_amdgcn_perm(r1, r0, 0x07060302u) : __builtin_amdgcn_perm(r1, r0, 0x05040100u);
    }
    pin(wA[0]); pin(wA[1]); pin(wA[2]); pin(wA[3]);
    b1v = lo(b1r); pin(b1v);
    const u32x4 ones = {ONE2, ONE2, ONE2, ONE2};
    f32x4 S = mfma(wA[0], ones, f32x4{0.f, 0.f, 0.f, 0.f});
    S = mfma(wA[1], ones, S);
    if (c == 0) {
#pragma unroll
      for (int i = 0; i < 4; ++i) L.cs1p[4 * g + i][w] = S[i];
    }
  }
  if (w == 6) {
    unsigned pa, pb;
    {
      LWAIT();
      while (__hip_atomic_load(&L.prb[2], __ATOMIC_RELAXED, __HIP_MEMORY_SCOPE_WORKGROUP) == 0u) __builtin_amdgcn_s_sleep(1);
      const unsigned p0 = L.prb[0], p1 = L.prb[1];
      const bool near0 = p0 > 250u && p0 < PCLEAN && p1 > p0 + 100u, near1 = p1 > 250u && p1 < PCLEAN && p0 > p1 + 100u;
      pa = near0 ? 1u : 2u; pb = near0 ? 2u : 1u;
      if (near0 || near1) goto probed;
      pa = p0; pb = p1;
      if (wocu) goto probed;
    }
    for (int tries = 0; tries < 8; ++tries) {
      u64 t0, t1, t2;
      st128x2(rws, l == 0 ? X.aPr : OOB, 0, u32x4{SENT, SENT, SENT, SENT});
      asm volatile("s_waitcnt vmcnt(0)\n\ts_memtime %0\n\ts_waitcnt lgkmcnt(0)" : "=s"(t0) :: "memory");
      const unsigned v0 = ld32<SC1>(rws, X.aPr, 0);
      asm volatile("s_waitcnt vmcnt(0)\n\ts_memtime %0\n\ts_waitcnt lgkmcnt(0)" : "=s"(t1) : "v"(v0) : "memory");
      const unsigned v1 = ld32<SC1>(rws, X.aPr, CPY);
      asm volatile("s_waitcnt vmcnt(0)\n\ts_memtime %0\n\ts_waitcnt lgkmcnt(0)" : "=s"(t2) : "v"(v1) : "memory");
      pa = (unsigned)(t1 - t0); pb = (unsigned)(t2 - t1);
      const unsigned mn = pa < pb ? pa : pb, mxp = pa < pb ? pb : pa;
      if ((mn < 600u && mxp > mn + 90u && mxp < 1000u) || (long long)(rtime() - (tref + PDL)) > 0) break;
    }
  probed:
    if (l == 0) { L.prb[3] = pa < pb ? 0u : (unsigned)CPY; }
    TSV(46, pa * 10000 + pb);
  }
  mlp_stage<0>(L, X, p, tref, tpubB, wocu, xf, wA, cs1, b1v, xres1, []() {});
  TS(11);
  {
    const float x2 = c_gather<0>(L, X, p, [&]() {
      const unsigned char* st = (const unsigned char*)&L.wqs[0][0];
#pragma unroll
      for (int ci = 0; ci < 2; ++ci)
#pragma unroll
        for (int j2 = 0; j2 < 4; ++j2) {
          const int d0 = 32 * ci + 8 * g + 2 * j2;
          const unsigned a = *(const u16*)(st + (d0 >> 2) * 1024 + (d0 & 3) * 256 + (16 * w + c) * 2), bb = *(const u16*)(st + ((d0 + 1) >> 2) * 1024 + ((d0 + 1) & 3) * 256 + (16 * w + c) * 2);
          wqB[ci][j2] = a | (bb << 16);
        }
      pin(wqB[0]); pin(wqB[1]);
    });
    const unsigned hh = pk2(x2, x2);
    const unsigned ll = pk2(x2 - lo(hh), 0.f);
    LP(u16, xhl, X.o_xw) = (u16)hh; LP(u16, xhl[0][1], X.o_xw) = (u16)ll;
    LWAIT();
    const u32x4 a0 = LP(const u32x4, xhl, X.o_xr), a1 = LP(const u32x4, xhl[0][0][32], X.o_xr);
    f32x4 d = mfma(a0, wqB[0], f32x4{0.f, 0.f, 0.f, 0.f});
    d = mfma(a1, wqB[1], d);
    const float q1 = d[0] + d[1];
    const unsigned pk = sanu(pk2(q1, dpp<XOR1>(q1)));
    const u32x4 v = {pk, dppu<SHL + 2>(pk), dppu<SHL + 4>(pk), dppu<SHL + 6>(pk)};
    st128x2(rws, X.aDq, 0, v);
    if (w == 0) {
      const unsigned xu = san(x2);
      const u32x4 vx = {xu, dppu<0x55>(xu), dppu<0xAA>(xu), dppu<0xFF>(xu)};
      st128x2(rws, X.aDx, 0, vx);
    } else if (w == 1) {
      const u32x4 ones = {ONE2, ONE2, ONE2, ONE2};
      const f32x4 z = {0.f, 0.f, 0.f, 0.f};
      f32x4 G = mfma(a0, a0, z), s1 = mfma(a0, ones, z);
      G = mfma(a1, a1, G); s1 = mfma(a1, ones, s1);
      float s2 = G[0] + G[1]; s2 += dpp<XOR1>(s2);
      st128x2(rws, X.aDx, 0, u32x4{san(s1[0] + s1[1]), san(s2), 0u, 0u});
    }
  }
  TS(13);
  {
    u32x4 r4 = {0u, 0u, 0u, 0u};
    pin(r4);
    BAR();
    __builtin_amdgcn_s_sleep(SLP_D);
    TS(38);
    gather_4(r4, X.aDg, rws, X.sd, 0x1FFFFFFFFull);
    FENCE();
    {
      f32x4 sk[4];
#pragma unroll
      for (int T = 0; T < 4; ++T) sk[T] = f32x4{0.f, 0.f, 0.f, 0.f};
#pragma unroll
      for (int i = 0; i < 4; ++i) {
        const u32x4 cf = *(const u32x4*)&L.csb[16 * i + 4 * g];
#pragma unroll
        for (int T = 0; T < 4; ++T) sk[T] = mfma(cf, kf[4 * T + i], sk[T]);
      }
      kcs = sel4(g, sk[0][0], sk[1][0], sk[2][0], sk[3][0]); pin(kcs);
    }
    FENCE();
    VMWAIT(0, "+v"(r4));
    {
      bool bad = mx4(r4) == SENT;
      TSV(39, __any(bad) ? 2 : 1);
      if (UNLIKELY(__any(bad))) {
        for (unsigned sp_ = 0; sp_ < SPIN_MAX; ++sp_) {
          TMO(4);
          gather_4(r4, X.aDg, rws, X.sd, __ballot(bad));
          VMWAIT(0, "+v"(r4));
          bad = mx4(r4) == SENT;
          if (!__any(bad)) break;
        }
      }
    }
    TS(14);
    if (l <= 32) *(u32x4*)((unsigned char*)&L + X.o_dd) = r4;
    BAR();
    u32x4 qf[4];
#pragma unroll
    for (int i = 0; i < 4; ++i) qf[i] = LP(const u32x4, qrec[0][16 * i], X.o_qr);
    const f32x2 st2 = LP(const f32x2, st2, X.o_st);
    f32x4 sc[4];
#pragma unroll
    for (int T = 0; T < 4; ++T) sc[T] = f32x4{0.f, 0.f, 0.f, 0.f};
#pragma unroll
    for (int i = 0; i < 4; ++i)
#pragma unroll
      for (int T = 0; T < 4; ++T) sc[T] = mfma(qf[i], kf[4 * T + i], sc[T]);
    const float mean = sum8l(st2[0]) * (1.f / 512);
    const float ms = sum8l(st2[1]) * (1.f / 512);
    const float scale = __builtin_amdgcn_rsqf(fmx(ms - mean * mean, 0.f) + 1e-5f) * QS;
    const float ta = tr16(sum4(sc[0]), sum4(sc[1])), tb = tr16(sum4(sc[2]), sum4(sc[3]));
    const float tsel = ((g & 2) ? tb : ta) - mean * kcs;
    const float tmax = mx32r(mx16r(max16(tsel)));
    TS(15);
    pv_publish<1>(L, X, p, tsel, tmax, scale, vr, msk, []() {});
  }
  tref = rtime();
  TS(16);
  float xres2 = 0.f;
  auto shadow2 = [&]() {
    xres2 = L.xfull[64 * s + xcol] + b2v; pin(xres2);
    if (!wocu) {
#pragma unroll
      for (int ci = 0; ci < 2; ++ci) {
        const f32x4 p0 = *(const f32x4*)&L.xfull[64 * w + 32 * ci + 8 * g], p1 = *(const f32x4*)&L.xfull[64 * w + 32 * ci + 8 * g + 4];
        const float xv8[8] = {p0[0], p0[1], p0[2], p0[3], p1[0], p1[1], p1[2], p1[3]};
#pragma unroll
        for (int j = 0; j < 4; ++j) {
          const unsigned hh = pk2(xv8[2 * j], xv8[2 * j + 1]);
          const unsigned ll = pk2(xv8[2 * j] - lo(hh), xv8[2 * j + 1] - hi(hh));
          xf[ci][j] = c == 0 ? hh : c == 1 ? ll : 0u;
        }
      }
      pin(xf[0]); pin(xf[1]);
    }
  };
  if (wocu) wo_stage<1, 0>(L, X, p, wA, shadow2); else shadow2();
  const u64 tpubB2 = rtime();
  TS(19);
  const bool fin = (xg == 0 && idx >= 24);
  if (u < 0 && !fin) { TSDUMP(); return; }
  mlp_stage<1>(L, X, p, tref, tpubB2, wocu, xf, wA, cs1, b1v, xres2, []() {});
  TS(21);
  if (!fin) { TSDUMP(); return; }
  const float o = c_gather<1>(L, X, p, []() {});
  {
    unsigned mx = 0;
    for (int i = 0; i < 32; ++i) { const u32x2 g2 = ld64<SC1>(X.rdbg, (l + 64 * i) * 8); if (g2.y == (unsigned)epoch) mx = um(mx, g2.x); }
    for (int o_ = 32; o_ > 0; o_ >>= 1) mx = um(mx, (unsigned)__shfl_xor((int)mx, o_));
    const u64 tend = rtime() + 10ull * mx;
    while ((long long)(rtime() - tend) < 0) __builtin_amdgcn_s_sleep(2);
  }
  if (w == 0) {
    p.out[64 * s + l] = (u16)(pk2(o, o) & 0xffffu);
    if (s == 0 && l == 0) *(u64*)p.ws = epoch;
  }
  TS(23);
  TSDUMP();
}

extern "C" void kernel_launch(void* const* d_in, const int* in_sizes, int n_in, void* d_out, int out_size, void* d_ws, size_t ws_size, hipStream_t stream) {
  Params p;
  p.x = (const u16*)d_in[0]; p.K = (const u16*)d_in[1]; p.V = (const u16*)d_in[2]; p.wq = (const u16*)d_in[3]; p.wo = (const u16*)d_in[4];
  p.w1 = (const u16*)d_in[6]; p.b1 = (const u16*)d_in[7]; p.w2 = (const u16*)d_in[8]; p.b2 = (const u16*)d_in[9];
  p.out = (u16*)d_out; p.ws = (unsigned char*)d_ws;
  hipLaunchKernelGGL(fused_kernel, dim3(NCU), dim3(NTHR), 0, stream, p);
}
```

```cpp
#include <hip/hip_runtime.h>
#include <stdint.h>

typedef unsigned short u16;
typedef unsigned long long u64;
typedef unsigned u32x4 __attribute__((ext_vector_type(4)));
typedef unsigned u32x2 __attribute__((ext_vector_type(2)));
typedef float f32x4 __attribute__((ext_vector_type(4)));
typedef float f32x2 __attribute__((ext_vector_type(2)));
typedef short bf16x8 __attribute__((ext_vector_type(8)));
typedef __bf16 bf16x2 __attribute__((ext_vector_type(2)));
typedef __amdgpu_buffer_rsrc_t rsrc_t;

constexpr int KV_AUX = 2, W_AUX = 2;
#define KVNT " nt"
constexpr int SLP_A = 4, SLP_C = 4, SLP_D = 4;
constexpr int DL_B1 = 110, DL_B2 = 150, DL_X = 150;
constexpr int DL_CP = 115, DL_CB = 70;
constexpr int PDL = 30;
constexpr unsigned PCLEAN = 620u;

constexpr int DM = 512, NH = 32, DH = 128, FF = 1376;
constexpr int NCU = 256, NTHR = 512, NU = 86;
constexpr unsigned SENT = 0xFFFFFFFFu;
constexpr unsigned SPIN_MAX = 1u << 17;
constexpr int SC1 = 16;
constexpr float QS = 0.08838834764831845f * 1.4426950408889634f;
constexpr unsigned ONE2 = 0x3F803F80u;
constexpr int OOB = 0x40000000;

constexpr int CPY = 8192;
constexpr int A_SZ = 16 * 16384;
constexpr int WS_A = 0;
constexpr int B_SZ = 2 * 16384;
constexpr int WS_B = WS_A + 2 * A_SZ;
constexpr int C_SZ = 11 * 16384;
constexpr int WS_C = WS_B + 2 * B_SZ;
constexpr int WS_D2 = WS_C + 2 * C_SZ;
constexpr int WS_FLAGS = WS_D2 + 64 * 16384;
constexpr int WS_D1 = 2 << 20;
constexpr int WS_END = WS_D1 + NCU * 1024;
__device__ __forceinline__ int aoff(int r) { return WS_A + (r >> 4) * 16384 + (r & 15) * 512; }
__device__ __forceinline__ int doff(int r) { return WS_D2 + (r >> 2) * 16384 + (r & 3) * 2048; }

struct Params {
  const u16 *x, *K, *V, *wq, *wo, *w1, *b1, *w2, *b2;
  u16* out;
  unsigned char* ws;
};

__device__ __forceinline__ float lo(unsigned w) { return __builtin_bit_cast(float, w << 16); }
__device__ __forceinline__ float hi(unsigned w) { return __builtin_bit_cast(float, w & 0xffff0000u); }
__device__ __forceinline__ float asf(unsigned w) { return __builtin_bit_cast(float, w); }
__device__ __forceinline__ unsigned asu(float f) { return __builtin_bit_cast(unsigned, f); }
__device__ __forceinline__ unsigned pk2(float a, float b) { const bf16x2 v = {(__bf16)a, (__bf16)b}; return __builtin_bit_cast(unsigned, v); }
__device__ __forceinline__ unsigned sanu(unsigned u) { return u < 0xFFFFFFFEu ? u : 0xFFFFFFFEu; }
__device__ __forceinline__ unsigned san(float f) { return sanu(asu(f)); }
template <int CTRL> __device__ __forceinline__ unsigned dppu(unsigned x) { return (unsigned)__builtin_amdgcn_mov_dpp((int)x, CTRL, 0xf, 0xf, true); }
template <int CTRL> __device__ __forceinline__ float dpp(float x) { return asf(dppu<CTRL>(asu(x))); }
constexpr int XOR1 = 0xB1, XOR2 = 0x4E, HMIR = 0x141, MIR = 0x140, ROR8 = 0x128, BC = 0x150, SHL = 0x100;
__device__ __forceinline__ float tr32(float a, float b) {
  auto s = __builtin_amdgcn_permlane32_swap(asu(a), asu(b), false, false);
  return asf(s[0]) + asf(s[1]);
}
__device__ __forceinline__ float tr16(float a, float b) {
  auto s = __builtin_amdgcn_permlane16_swap(asu(a), asu(b), false, false);
  return asf(s[0]) + asf(s[1]);
}
template <int CTRL> __device__ __forceinline__ float trd(float a, float b, bool sel) {
  const float keep = sel ? b : a, send = sel ? a : b;
  return keep + dpp<CTRL>(send);
}
__device__ __forceinline__ float sum8l(float x) { x += dpp<XOR1>(x); x += dpp<XOR2>(x); x += dpp<HMIR>(x); return x; }
__device__ __forceinline__ float sum16(float x) { x = sum8l(x); x += dpp<MIR>(x); return x; }
__device__ __forceinline__ float sum64(float x) { x = sum16(x); x = tr16(x, x); x = tr32(x, x); return x; }
__device__ __forceinline__ float fmx(float a, float b) { return __builtin_amdgcn_fmed3f(a, b, __builtin_inff()); }
__device__ __forceinline__ float fmx3(float a, float b, float c) { return fmx(fmx(a, b), c); }
__device__ __forceinline__ float fmxa(float a, float b) { float r; asm("v_max_f32 %0, %1, %2" : "=v"(r) : "v"(a), "v"(b)); return r; }
#define MD(ctl) "s_nop 1\n\tv_max_f32_dpp %0, %0, %0 " ctl " row_mask:0xf bank_mask:0xf\n\t"
__device__ __forceinline__ float max8l(float x) { asm(MD("quad_perm:[1,0,3,2]") MD("quad_perm:[2,3,0,1]") MD("row_half_mirror") : "+v"(x)); return x; }
__device__ __forceinline__ float max16(float x) { asm(MD("quad_perm:[1,0,3,2]") MD("quad_perm:[2,3,0,1]") MD("row_half_mirror") MD("row_mirror") : "+v"(x)); return x; }
__device__ __forceinline__ float mx16r(float a) { auto s = __builtin_amdgcn_permlane16_swap(asu(a), asu(a), false, false); return fmxa(asf(s[0]), asf(s[1])); }
__device__ __forceinline__ float mx32r(float a) { auto s = __builtin_amdgcn_permlane32_swap(asu(a), asu(a), false, false); return fmxa(asf(s[0]), asf(s[1])); }
__device__ __forceinline__ float dot2(unsigned a, unsigned b, float c) {
  return __builtin_amdgcn_fdot2_f32_bf16(__builtin_bit_cast(bf16x2, a), __builtin_bit_cast(bf16x2, b), c, false);
}
__device__ __forceinline__ float ex2(float x) { return __builtin_amdgcn_exp2f(x); }
__device__ __forceinline__ f32x4 mfma(u32x4 a, u32x4 b, f32x4 c) {
  return __builtin_amdgcn_mfma_f32_16x16x32_bf16(__builtin_bit_cast(bf16x8, a), __builtin_bit_cast(bf16x8, b), c, 0, 0, 0);
}
__device__ __forceinline__ float sum4(f32x4 s) { return (s[0] + s[1]) + (s[2] + s[3]); }
__device__ __forceinline__ float sum8(f32x4 a, f32x4 b) { return sum4(a + b); }
__device__ __forceinline__ float sel4(int g, float a, float b, float c, float d) { const float x = (g & 1) ? b : a, y = (g & 1) ? d : c; return (g & 2) ? y : x; }

template <int AUX> __device__ __forceinline__ u32x4 ld128(rsrc_t r, int voff, int soff = 0) { return __builtin_amdgcn_raw_buffer_load_b128(r, voff, soff, AUX); }
template <int AUX> __device__ __forceinline__ u32x2 ld64(rsrc_t r, int voff, int soff = 0) { return __builtin_amdgcn_raw_buffer_load_b64(r, voff, soff, AUX); }
template <int AUX> __device__ __forceinline__ unsigned ld32(rsrc_t r, int voff, int soff = 0) { return __builtin_amdgcn_raw_buffer_load_b32(r, voff, soff, AUX); }
template <int AUX> __device__ __forceinline__ unsigned ld16(rsrc_t r, int voff, int soff = 0) { return __builtin_amdgcn_raw_buffer_load_b16(r, voff, soff, AUX); }
__device__ __forceinline__ void st128(rsrc_t r, int voff, int soff, u32x4 v) { __builtin_amdgcn_raw_buffer_store_b128(v, r, voff, soff, SC1); }
__device__ __forceinline__ void st128x2(rsrc_t r, int voff, int soff, u32x4 v) { st128(r, voff, soff, v); st128(r, voff, soff + CPY, v); }
__device__ __forceinline__ rsrc_t mk(const void* p, unsigned bytes) { return __builtin_amdgcn_make_buffer_rsrc((void*)p, 0, bytes, 0x00020000); }
__device__ __forceinline__ unsigned um(unsigned a, unsigned b) { return a > b ? a : b; }
__device__ __forceinline__ unsigned mx4(u32x4 v) { return um(um(v.x, v.y), um(v.z, v.w)); }
__device__ __forceinline__ unsigned mx2(u32x2 v) { return um(v.x, v.y); }
__device__ __forceinline__ int vsel(bool c, int a, int b) { asm("" : "+v"(a)); asm("" : "+v"(b)); return c ? a : b; }
template <typename T> __device__ __forceinline__ void pin(T& x) { asm volatile("" : "+v"(x)); }

__device__ __forceinline__ u64 uni64(u64 m) { return ((u64)(unsigned)__builtin_amdgcn_readfirstlane((unsigned)(m >> 32)) << 32) | (unsigned)__builtin_amdgcn_readfirstlane((unsigned)m); }
#define LD4(dst, off, imm) "buffer_load_dwordx4 %[" dst "], %[" off "], %[rs], %[so] offen offset:" imm " sc1\n\t"
#define LD2(dst, off, imm) "buffer_load_dwordx2 %[" dst "], %[" off "], %[rs], %[so] offen offset:" imm " sc1\n\t"
__device__ __forceinline__ void gather_42(u32x4& a, u32x2& c, int oa, int oc, rsrc_t rs, int so, u64 m) {
  asm volatile("s_mov_b64 exec, %[m]\n\t" LD4("a", "oa", "0") LD2("c", "oc", "0") "s_mov_b64 exec, -1"
               : [a] "+v"(a), [c] "+v"(c) : [oa] "v"(oa), [oc] "v"(oc), [rs] "s"(rs), [so] "s"(so), [m] "s"(uni64(m)) : "memory");
}
__device__ __forceinline__ void gather_44s(u32x4& a, u32x4& b, int oa, rsrc_t rs, int so, int so2, u64 m) {
  asm volatile("s_mov_b64 exec, %[m]\n\t" LD4("a", "oa", "0") "buffer_load_dwordx4 %[b], %[oa], %[rs], %[so2] offen sc1\n\ts_mov_b64 exec, -1"
               : [a] "+v"(a), [b] "+v"(b) : [oa] "v"(oa), [rs] "s"(rs), [so] "s"(so), [so2] "s"(so2), [m] "s"(uni64(m)) : "memory");
}
__device__ __forceinline__ void gather_44v(u32x4& a, u32x4& b, int oa, int ob, rsrc_t rs, int so, u64 m) {
  asm volatile("s_mov_b64 exec, %[m]\n\t" LD4("a", "oa", "0") LD4("b", "ob", "0") "s_mov_b64 exec, -1"
               : [a] "+v"(a), [b] "+v"(b) : [oa] "v"(oa), [ob] "v"(ob), [rs] "s"(rs), [so] "s"(so), [m] "s"(uni64(m)) : "memory");
}
__device__ __forceinline__ void gather_4(u32x4& a, int oa, rsrc_t rs, int so, u64 m) {
  asm volatile("s_mov_b64 exec, %[m]\n\t" LD4("a", "oa", "0") "s_mov_b64 exec, -1"
               : [a] "+v"(a) : [oa] "v"(oa), [rs] "s"(rs), [so] "s"(so), [m] "s"(uni64(m)) : "memory");
}
__device__ __forceinline__ u64 umask(bool on, u64 m) { const unsigned f = __builtin_amdgcn_readfirstlane(on ? ~0u : 0u); return m & (((u64)f << 32) | f); }
#define VMWAIT(N, ...) asm volatile("s_waitcnt vmcnt(" #N ")" : __VA_ARGS__ :: "memory")
#define UNLIKELY(x) __builtin_expect(!!(x), 0)

__device__ __forceinline__ int side_from(unsigned p0, unsigned p1) { return __builtin_amdgcn_readfirstlane(p0 < p1 ? 0 : CPY); }
__device__ __forceinline__ u64 rtime() { return __builtin_amdgcn_s_memrealtime(); }
__device__ __forceinline__ void wait_until(u64 t) { while ((long long)(rtime() - t) < 0) __builtin_amdgcn_s_sleep(1); }
#define FENCE() do { asm volatile("" ::: "memory"); __builtin_amdgcn_sched_barrier(0); } while (0)
#define BAR() do { asm volatile("s_waitcnt lgkmcnt(0)" ::: "memory"); __builtin_amdgcn_s_barrier(); asm volatile("" ::: "memory"); } while (0)
#define LWAIT() asm volatile("s_waitcnt lgkmcnt(0)" ::: "memory")

struct __attribute__((aligned(16))) Lds {
  union {
    float obuf[8][128];
    struct { float cbuf[64][8]; u16 xhl[8][2][64]; };
  };
  float ml[8][2];
  unsigned qb[64];
  unsigned csb[64];
  float ybuf[64][8];
  float sbuf[16][8];
  float s12[2][8];
  float cs1p[16][8];
  float xs[64];
  float xfull[512];
  unsigned qrec[8][72];
  float st2[8][4];
  unsigned prb[4];
  u64 tB[2];
  unsigned tBf[2];
  unsigned qflag;
  unsigned pad_[1];
  u32x4 wqs[16][64];
  unsigned char big[8][16384];
};
#define LP(T, field, off) (*(T*)((unsigned char*)&L.field + (off)))

struct Ctx {
  int w, l, g, c, u;
  rsrc_t rws;
  int sd;
  const unsigned char* pl;
  int aPr, aA, aAml, aApub, aB0, aB2, aBpub, o_bw, o_br, aC0, aC1, aCpub, aDg, aDq, aDx;
  int o_ob, o_l8, o_ml, o_yw, o_c32, o_sw, o_cw, o_l32, o_l4, o_xw, o_xr, o_qr, o_st, o_dd, o_xs;
};
constexpr u64 M_C8 = 0x00FF00FF00FF00FFull;

__device__ __forceinline__ void make_frags(Lds& L, const Ctx& X, const u32x4 (&raw)[2], u32x4 (&fr)[2], int base) {
  unsigned char* st = &L.big[X.w][base];
#pragma unroll
  for (int i = 0; i < 2; ++i) *(u32x4*)(st + (32 * i + (X.l >> 1)) * 32 + (X.l & 1) * 16) = raw[i];
  LWAIT();
#pragma unroll
  for (int ci = 0; ci < 2; ++ci)
#pragma unroll
    for (int j2 = 0; j2 < 4; ++j2) {
      const unsigned a = *(const u16*)(st + (32 * ci + 8 * X.g + 2 * j2) * 32 + X.c * 2), bb = *(const u16*)(st + (32 * ci + 8 * X.g + 2 * j2 + 1) * 32 + X.c * 2);
      fr[ci][j2] = a | (bb << 16);
    }
  pin(fr[0]); pin(fr[1]);
}

template <int LAYER, typename HOOK>
__device__ __forceinline__ void pv_publish(Lds& L, const Ctx& X, float tsel, float tmax, float scale, const u32x4 (&vr)[16], const unsigned (&msk)[4], HOOK hook) {
  const int w = X.w, l = X.l;
  const float pe = ex2((tsel - tmax) * scale);
  const unsigned pd = pk2(pe, pe);
  f32x4 o0 = {0.f, 0.f, 0.f, 0.f}, o1 = {0.f, 0.f, 0.f, 0.f};
#define PVS(n) { unsigned pv = dppu<BC + n>(pd); asm("" : "+v"(pv)); const u32x4 a = {pv & msk[0], pv & msk[1], pv & msk[2], pv & msk[3]}; if (n & 1) o1 = mfma(a, vr[n], o1); else o0 = mfma(a, vr[n], o0); }
  PVS(0) PVS(1) PVS(2) PVS(3) PVS(4) PVS(5) PVS(6) PVS(7) PVS(8) PVS(9) PVS(10) PVS(11) PVS(12) PVS(13) PVS(14) PVS(15)
#undef PVS
  const float lsum = sum64(lo(pd));
  o0 += o1;
  if (l < 32) LP(f32x4, obuf, X.o_ob) = o0;
  if (l == 0) *(f32x2*)&L.ml[w][0] = f32x2{tmax * scale, lsum};
  hook();
  BAR();
  if (LAYER == 0 && w == 3) {
    u64 t0, t1, t2; unsigned d1, d2;
    asm volatile("s_memtime %0\n\ts_waitcnt lgkmcnt(0)" : "=s"(t0) :: "memory");
    asm volatile("s_load_dword %0, %2, 0x0\n\ts_waitcnt lgkmcnt(0)\n\ts_memtime %1\n\ts_waitcnt lgkmcnt(0)" : "=s"(d1), "=s"(t1) : "s"(X.pl) : "memory");
    asm volatile("s_load_dword %0, %2, 0x2000\n\ts_waitcnt lgkmcnt(0)\n\ts_memtime %1\n\ts_waitcnt lgkmcnt(0)" : "=s"(d2), "=s"(t2) : "s"(X.pl) : "memory");
    if (l == 0) { L.prb[0] = (unsigned)(t1 - t0); L.prb[1] = (unsigned)(t2 - t1); }
    LWAIT();
    if (l == 0) __hip_atomic_store(&L.prb[2], 1u, __ATOMIC_RELAXED, __HIP_MEMORY_SCOPE_WORKGROUP);
  }
  if (w == 0) {
    const f32x2 mlu = LP(const f32x2, ml, X.o_ml);
    f32x2 ob[8];
#pragma unroll
    for (int u = 0; u < 8; ++u) ob[u] = LP(const f32x2, obuf[u], X.o_l8);
    const float M = max8l(mlu[0]);
    const float f = ex2(mlu[0] - M);
    const float Ls = sum8l(f * mlu[1]);
    f32x2 o = dpp<BC + 0>(f) * ob[0];
    o += dpp<BC + 1>(f) * ob[1]; o += dpp<BC + 2>(f) * ob[2]; o += dpp<BC + 3>(f) * ob[3];
    o += dpp<BC + 4>(f) * ob[4]; o += dpp<BC + 5>(f) * ob[5]; o += dpp<BC + 6>(f) * ob[6]; o += dpp<BC + 7>(f) * ob[7];
    const unsigned pk = sanu(pk2(o[0], o[1]));
    u32x4 v = {pk, dppu<0x55>(pk), dppu<0xAA>(pk), dppu<0xFF>(pk)};
    if (l == 1) v = u32x4{san(M), san(Ls), 0u, 0u};
    st128x2(X.rws, X.aApub, LAYER * A_SZ, v);
  }
}

template <int LAYER, typename HOOK>
__device__ __forceinline__ void wo_stage(Lds& L, Ctx& X, const u32x4 (&wof)[4], HOOK hook) {
  const int w = X.w, l = X.l;
  const unsigned ini = X.c < 8 ? SENT : 0u;
  u32x4 a0 = {ini, ini, ini, ini}; u32x2 mlv = {ini, ini};
  pin(a0); pin(mlv);
  BAR();
  if (LAYER == 0) X.sd = side_from(L.prb[0], L.prb[1]);
  __builtin_amdgcn_s_sleep(SLP_A);
  gather_42(a0, mlv, X.aA, X.aAml, X.rws, LAYER * A_SZ + X.sd, M_C8);
  FENCE();
  hook();
  FENCE();
  VMWAIT(0, "+v"(a0), "+v"(mlv));
  {
    bool bad = um(mx4(a0), mx2(mlv)) == SENT;
    if (UNLIKELY(__any(bad))) {
      for (unsigned sp_ = 0; sp_ < SPIN_MAX; ++sp_) {
        gather_42(a0, mlv, X.aA, X.aAml, X.rws, LAYER * A_SZ + X.sd, __ballot(bad));
        VMWAIT(0, "+v"(a0), "+v"(mlv));
        bad = um(mx4(a0), mx2(mlv)) == SENT;
        if (!__any(bad)) break;
      }
    }
  }
  f32x4 d[4];
#pragma unroll
  for (int n = 0; n < 4; ++n) d[n] = mfma(a0, wof[n], f32x4{0.f, 0.f, 0.f, 0.f});
  const float m = asf(mlv.x), ls = asf(mlv.y);
  const float M = max8l(m);
  const float e = ex2(m - M);
  const float f = e * __builtin_amdgcn_rcpf(sum8l(e * ls));
  const bool odd = (X.g & 1) != 0;
  const float F0 = odd ? dpp<BC + 4>(f) : dpp<BC + 0>(f), F1 = odd ? dpp<BC + 5>(f) : dpp<BC + 1>(f), F2 = odd ? dpp<BC + 6>(f) : dpp<BC + 2>(f), F3 = odd ? dpp<BC + 7>(f) : dpp<BC + 3>(f);
  float y[4];
#pragma unroll
  for (int n = 0; n < 4; ++n) y[n] = (F0 * d[n][0] + F1 * d[n][1]) + (F2 * d[n][2] + F3 * d[n][3]);
  const float ya = tr16(y[0], y[1]), yb = tr16(y[2], y[3]);
  if (l < 32) { LP(float, ybuf, X.o_yw) = ya; LP(float, ybuf[32], X.o_yw) = yb; }
  BAR();
  if (w == 0) {
    const float s = sum8(LP(const f32x4, ybuf, X.o_l32), LP(const f32x4, ybuf[0][4], X.o_l32));
    const unsigned pk = sanu(pk2(s, dpp<XOR1>(s)));
    const u32x4 v = {pk, dppu<SHL + 2>(pk), dppu<SHL + 4>(pk), dppu<SHL + 6>(pk)};
    st128x2(X.rws, X.aBpub, LAYER * B_SZ, v);
  }
}

template <int LAYER>
__device__ __forceinline__ void mlp_stage(Lds& L, Ctx& X, u64 tref, u64 tpub, bool wocu, const u32x4 (&xf)[2], const u32x4 (&wA)[4], float& cs1, const float& b1v, const float& xres) {
  const int w = X.w, l = X.l;
  u32x4 a0 = {0u, 0u, 0u, 0u}, a1 = a0, a2 = a0, a3 = a0;
  pin(a0); pin(a1); pin(a2); pin(a3);
  const bool mlp = X.u >= 0;
  const u64 m01 = umask(mlp, ~0ull), m2 = umask(w == 7, ~0ull);
  BAR();
  if (LAYER == 0) X.sd = __builtin_amdgcn_readfirstlane(L.prb[3]);
  wait_until(tref + (mlp ? (LAYER == 0 ? DL_B1 : DL_B2) : DL_X));
  gather_44s(a0, a1, X.aB0, X.rws, LAYER * B_SZ + X.sd, LAYER * B_SZ + X.sd + 16384, m01);
  gather_44s(a2, a3, X.aB2, X.rws, LAYER * B_SZ + X.sd, LAYER * B_SZ + X.sd + 16384, m2);
  FENCE();
  float S1x, s1x, s2x;
  {
    const u32x4 ones = {ONE2, ONE2, ONE2, ONE2};
    const f32x4 z = {0.f, 0.f, 0.f, 0.f};
    f32x4 S0 = mfma(wA[0], xf[0], z), G0 = mfma(xf[0], xf[0], z), s10 = mfma(ones, xf[0], z);
    S0 = mfma(wA[1], xf[1], S0); G0 = mfma(xf[1], xf[1], G0); s10 = mfma(ones, xf[1], s10);
    float S2[2];
#pragma unroll
    for (int j = 0; j < 2; ++j) S2[j] = trd<ROR8>(S0[j], S0[j + 2], (l & 8) != 0);
    S1x = trd<HMIR>(S2[0], S2[1], (l & 4) != 0);
    S1x += dpp<XOR2>(S1x); S1x += dpp<XOR1>(S1x);
    s1x = sum16(s10[0]);
    s2x = sum64(sum4(G0));
    pin(S1x); pin(s1x); pin(s2x);
  }
  FENCE();
  VMWAIT(0, "+v"(a0), "+v"(a1), "+v"(a2), "+v"(a3));
  bool waited;
  {
    bool bad = um(um(mx4(a0), mx4(a1)), um(mx4(a2), mx4(a3))) == SENT;
    waited = __any(bad);
    if (UNLIKELY(__any(bad))) {
      for (unsigned sp_ = 0; sp_ < SPIN_MAX; ++sp_) {
        const u64 bm = __ballot(bad);
        gather_44s(a0, a1, X.aB0, X.rws, LAYER * B_SZ + X.sd, LAYER * B_SZ + X.sd + 16384, bm & m01);
        gather_44s(a2, a3, X.aB2, X.rws, LAYER * B_SZ + X.sd, LAYER * B_SZ + X.sd + 16384, bm & m2);
        VMWAIT(0, "+v"(a0), "+v"(a1), "+v"(a2), "+v"(a3));
        bad = um(um(mx4(a0), mx4(a1)), um(mx4(a2), mx4(a3))) == SENT;
        if (!__any(bad)) break;
      }
    }
  }
  if (mlp) {
    LP(u32x4, big, X.o_bw) = a0; LP(u32x4, big[0][8 * 144], X.o_bw) = a1;
    LWAIT();
    a0 = LP(const u32x4, big, X.o_br); a1 = LP(const u32x4, big[0][64], X.o_br);
    const u32x4 ones = {ONE2, ONE2, ONE2, ONE2};
    const f32x4 z = {0.f, 0.f, 0.f, 0.f};
    f32x4 S = mfma(wA[0], a0, z), G = mfma(a0, a0, z), Gx = mfma(xf[0], a0, z), s1 = mfma(ones, a0, z);
    S = mfma(wA[1], a1, S); G = mfma(a1, a1, G); Gx = mfma(xf[1], a1, Gx); s1 = mfma(ones, a1, s1);
    asm volatile("" : "+v"(S), "+v"(G), "+v"(Gx), "+v"(s1));
    G += 2.f * Gx;
    float S2[2];
#pragma unroll
    for (int j = 0; j < 2; ++j) S2[j] = trd<ROR8>(S[j], S[j + 2], (l & 8) != 0);
    float S1 = trd<HMIR>(S2[0], S2[1], (l & 4) != 0);
    S1 += dpp<XOR2>(S1); S1 += dpp<XOR1>(S1);
    S1 += S1x;
    const float s2t = sum64(sum4(G)) + s2x;
    const float s1t = sum16(s1[0]) + s1x;
    if ((l & 3) == 0) LP(float, sbuf, X.o_sw) = S1;
    if (l == 0) { L.s12[0][w] = s1t; L.s12[1][w] = s2t; }
    BAR();
    const float St = sum8(LP(const f32x4, sbuf, X.o_c32), LP(const f32x4, sbuf[0][4], X.o_c32));
    if (LAYER == 0) { cs1 = sum8(LP(const f32x4, cs1p, X.o_c32), LP(const f32x4, cs1p[0][4], X.o_c32)); pin(cs1); }
    const float sv = sum8l(LP(const float, s12, X.c * 4)) * (1.f / 512);
    const float mean = dpp<BC + 0>(sv), ms = dpp<BC + 8>(sv);
    const float rstd = __builtin_amdgcn_rsqf(fmx(ms - mean * mean, 0.f) + 1e-5f);
    const float hp = rstd * (St - mean * cs1) + b1v;
    const float uu = hp * (0.7978845608028654f + 0.7978845608028654f * 0.044715f * hp * hp);
    const float ee = ex2(uu * (2.f * 1.4426950408889634f));
    const float hv = hp - hp * __builtin_amdgcn_rcpf(ee + 1.f);
    const unsigned hp2 = pk2(hv, dpp<XOR1>(hv));
    unsigned hb[8] = {dppu<BC + 0>(hp2), dppu<BC + 2>(hp2), dppu<BC + 4>(hp2), dppu<BC + 6>(hp2), dppu<BC + 8>(hp2), dppu<BC + 10>(hp2), dppu<BC + 12>(hp2), dppu<BC + 14>(hp2)};
    asm volatile("" : "+v"(hb[0]), "+v"(hb[1]), "+v"(hb[2]), "+v"(hb[3]), "+v"(hb[4]), "+v"(hb[5]), "+v"(hb[6]), "+v"(hb[7]));
    float acc0 = 0.f, acc1 = 0.f;
#pragma unroll
    for (int F = 0; F < 8; F += 2) { acc0 = dot2(hb[F], wA[2 + (F >> 2)][F & 3], acc0); acc1 = dot2(hb[F + 1], wA[2 + ((F + 1) >> 2)][(F + 1) & 3], acc1); }
    const float av = acc0 + acc1;
    const unsigned pk = sanu(pk2(av, dpp<XOR1>(av)));
    const u32x4 v = {pk, dppu<SHL + 2>(pk), dppu<SHL + 4>(pk), dppu<SHL + 6>(pk)};
    st128x2(X.rws, X.aCpub, LAYER * C_SZ, v);
  }
  if (!mlp && w == 7) {
    const u64 ta = waited ? rtime() + DL_CB : 0ull, tc = wocu ? tpub + DL_CP : 0ull, tb = ta > tc ? ta : tc;
    if (l == 0) L.tB[LAYER] = tb;
    LWAIT();
    if (l == 0) __hip_atomic_store(&L.tBf[LAYER], 1u, __ATOMIC_RELAXED, __HIP_MEMORY_SCOPE_WORKGROUP);
  }
  if (w == 7) {
    float o[8];
#pragma unroll
    for (int j = 0; j < 4; ++j) { o[2 * j] = lo(a2[j]) + lo(a3[j]); o[2 * j + 1] = hi(a2[j]) + hi(a3[j]); }
    float o4[4], oo2[2];
#pragma unroll
    for (int j = 0; j < 4; ++j) o4[j] = tr32(o[j], o[j + 4]);
#pragma unroll
    for (int j = 0; j < 2; ++j) oo2[j] = tr16(o4[j], o4[j + 2]);
    const float v = trd<ROR8>(oo2[0], oo2[1], (l & 8) != 0);
    LP(float, xs, X.o_xs) = v + xres;
  }
  if (mlp) __builtin_amdgcn_s_sleep(SLP_C);
  else {
    for (unsigned sp_ = 0; sp_ < (SPIN_MAX << 4); ++sp_) { if (__hip_atomic_load(&L.tBf[LAYER], __ATOMIC_RELAXED, __HIP_MEMORY_SCOPE_WORKGROUP) != 0u) break; __builtin_amdgcn_s_sleep(2); }
    asm volatile("" ::: "memory");
    const u64 tb = L.tB[LAYER];
    wait_until(uni64(tb));
  }
}

template <int LAYER, typename HOOK>
__device__ __forceinline__ float c_gather(Lds& L, const Ctx& X, HOOK hook) {
  u32x4 a0, a1;
  asm volatile("" : "=v"(a0), "=v"(a1));
  gather_44v(a0, a1, X.aC0, X.aC1, X.rws, LAYER * C_SZ + X.sd, ~0ull);
  FENCE();
  hook();
  FENCE();
  VMWAIT(0, "+v"(a0), "+v"(a1));
  {
    bool bad = um(mx4(a0), mx4(a1)) == SENT;
    if (UNLIKELY(__any(bad))) {
      for (unsigned sp_ = 0; sp_ < SPIN_MAX; ++sp_) {
        gather_44v(a0, a1, X.aC0, X.aC1, X.rws, LAYER * C_SZ + X.sd, __ballot(bad));
        VMWAIT(0, "+v"(a0), "+v"(a1));
        bad = um(mx4(a0), mx4(a1)) == SENT;
        if (!__any(bad)) break;
      }
    }
  }
  float o[8];
#pragma unroll
  for (int j = 0; j < 4; ++j) { o[2 * j] = lo(a0[j]) + lo(a1[j]); o[2 * j + 1] = hi(a0[j]) + hi(a1[j]); }
  float o4[4], o2[2];
#pragma unroll
  for (int j = 0; j < 4; ++j) o4[j] = tr32(o[j], o[j + 4]);
#pragma unroll
  for (int j = 0; j < 2; ++j) o2[j] = tr16(o4[j], o4[j + 2]);
  LP(float, cbuf, X.o_cw) = trd<ROR8>(o2[0], o2[1], (X.l & 8) != 0);
  BAR();
  return sum8(LP(const f32x4, cbuf, X.o_l32), LP(const f32x4, cbuf[0][4], X.o_l32)) + LP(const float, xs, X.o_l4);
}

__global__ void __launch_bounds__(NTHR, 2) fused_kernel(Params p) {
  asm volatile("" : "+s"(p.x), "+s"(p.K), "+s"(p.V), "+s"(p.wq), "+s"(p.wo), "+s"(p.w1), "+s"(p.b1), "+s"(p.w2), "+s"(p.b2), "+s"(p.out), "+s"(p.ws));
  __shared__ Lds L;
  Ctx X;
  const int t = threadIdx.x, b = blockIdx.x;
  const int w = __builtin_amdgcn_readfirstlane(t >> 6), l = t & 63, g = l >> 4, c = l & 15;
  const int xg = b & 7, idx = b >> 3, s = idx & 7, cu = xg * 32 + idx;
  const int h = cu >> 3, hg = h;
  const bool wocu = idx >= 16;
  const int wpp = (idx >> 3) & 1, wj = idx & 7;
  int u = idx < 12 ? 4 * (xg + 8 * (idx >> 2)) + (idx & 3) : -1;
  if (u >= NU) u = -1;
  unsigned char* const wsa = (unsigned char*)(((size_t)p.ws + (2u << 20)) & ~(size_t)((2u << 20) - 1));
  const rsrc_t rws = mk(wsa, WS_END), rep = mk(p.ws, 8);
  X.w = w; X.l = l; X.g = g; X.c = c; X.u = u; X.rws = rws;
  const rsrc_t rK = mk(p.K + (size_t)(h * 8 + s) * 65536, 131072), rV = mk(p.V + (size_t)(h * 8 + s) * 65536, 131072);
  const rsrc_t rx = mk(p.x, 1024), rwq = mk(p.wq, DM * NH * DH * 2), rwo = mk(p.wo, wocu ? DM * NH * DH * 2 : 0);
  const unsigned mz = u >= 0 ? 1u : 0u;
  const rsrc_t rw1 = mk(p.w1, mz * DM * FF * 2), rw2 = mk(p.w2, mz * DM * FF * 2), rb1 = mk(p.b1, mz * FF * 2), rb2 = mk(p.b2, DM * 2);
  const rsrc_t rd1 = mk(wsa + WS_D1, NCU * 1024);
  unsigned msk[4];
#pragma unroll
  for (int i = 0; i < 4; ++i) msk[i] = (c >> 1) == i ? ((c & 1) ? 0xFFFF0000u : 0x0000FFFFu) : 0u;
  const u32x2 ep = ld64<SC1>(rep, 0);
  if (w == 0) {
    const u32x4 xv = ld128<0>(rx, l * 16);
    const unsigned xq = ld16<0>(rx, (64 * s + 4 * c + g) * 2);
    u32x4 wqr[16];
#pragma unroll
    for (int n = 0; n < 16; ++n) wqr[n] = ld128<W_AUX>(rwq, (64 * s + g) * 8192 + h * 256 + c * 16, n * 4 * 8192);
    FENCE();
    __builtin_amdgcn_s_barrier();
    FENCE();
    float xs[8];
#pragma unroll
    for (int j = 0; j < 4; ++j) { xs[2 * j] = lo(xv[j]); xs[2 * j + 1] = hi(xv[j]); }
    const float mean = sum64(((xs[0] + xs[1]) + (xs[2] + xs[3])) + ((xs[4] + xs[5]) + (xs[6] + xs[7]))) * (1.f / 512);
    float d2 = 0.f;
#pragma unroll
    for (int j = 0; j < 8; ++j) { const float d = xs[j] - mean; d2 += d * d; }
    const float rstd = __builtin_amdgcn_rsqf(sum64(d2) * (1.f / 512) + 1e-5f);
    const float xn = (lo(xq) - mean) * rstd;
    const unsigned pd = pk2(xn, xn);
    const u32x4 onesA = {ONE2 & msk[0], ONE2 & msk[1], ONE2 & msk[2], ONE2 & msk[3]};
    f32x4 aq = {0.f, 0.f, 0.f, 0.f}, ac = {0.f, 0.f, 0.f, 0.f};
#define QST(n) { const unsigned pv = dppu<BC + n>(pd); const u32x4 a = {pv & msk[0], pv & msk[1], pv & msk[2], pv & msk[3]}; aq = mfma(a, wqr[n], aq); ac = mfma(onesA, wqr[n], ac); L.wqs[n][l] = wqr[n]; }
    QST(0) QST(1) QST(2) QST(3) QST(4) QST(5) QST(6) QST(7) QST(8) QST(9) QST(10) QST(11) QST(12) QST(13) QST(14) QST(15)
#undef QST
    const unsigned tag = ep.x + 1u;
    if (l < 32) {
      const int off = WS_D1 + cu * 1024 + (8 * c + 4 * g) * 8;
      st128(rws, off, 0, u32x4{pk2(aq[0], ac[0]), tag, pk2(aq[1], ac[1]), tag});
      st128(rws, off + 16, 0, u32x4{pk2(aq[2], ac[2]), tag, pk2(aq[3], ac[3]), tag});
    }
  }
  if (w == 1) {
#pragma unroll
    for (int k = 0; k < 4; ++k) {
      const int q = l + 64 * k;
      int off = OOB;
      if (q < 17) off = aoff(cu) + q * 16;
      else if (q < 34) off = A_SZ + aoff(cu) + (q - 17) * 16;
      else if (q >= 40 && q < 56 && wocu) off = WS_B + ((q - 40) >> 3) * B_SZ + ((2 * xg + wpp) >> 3) * 16384 + ((2 * xg + wpp) & 7) * 1024 + wj * 128 + (q & 7) * 16;
      else if (q >= 64 && q < 80) off = doff(cu) + ((q - 64) >> 1) * 128 + (q & 1) * 16;
      else if (q >= 80 && q < 97) off = doff(cu) + 1024 + (q - 80) * 16;
      else if (q == 97) off = doff(cu) + 1408;
      else if (q >= 128 && q < 256 && u >= 0) off = WS_C + ((q - 128) >> 6) * C_SZ + (u >> 3) * 16384 + (u & 7) * 1024 + ((q - 128) & 63) * 16;
      st128x2(rws, off, 0, u32x4{SENT, SENT, SENT, SENT});
    }
    FENCE();
    __builtin_amdgcn_s_barrier();
    asm volatile("s_waitcnt vmcnt(0)" ::: "memory");
    const u64 e1 = (((u64)ep.y << 32) | ep.x) + 1;
    if (l == 0) __builtin_amdgcn_raw_buffer_store_b64(u32x2{(unsigned)e1, (unsigned)(e1 >> 32)}, rws, WS_FLAGS + cu * 8, 0, SC1);
  }
  if (t == 128) { L.qflag = 0u; L.prb[2] = 0u; L.tBf[0] = 0u; L.tBf[1] = 0u; }
  if (w >= 2) { LWAIT(); __builtin_amdgcn_s_barrier(); }
  FENCE();
  u32x4 kf[16], vr[16];
  {
    int vo[4];
#pragma unroll
    for (int j = 0; j < 4; ++j) vo[j] = w * 16384 + (4 * j + g) * 256 + ((c ^ (4 * j + g)) * 16);
    const unsigned ldsb = (unsigned)(size_t)(__attribute__((address_space(3))) unsigned char*)&L.big[0][0] + w * 16384;
    unsigned keep;
#define KD(j, so) "s_nop 0\n\tbuffer_load_dwordx4 %[v" #j "], %[rs], " so " offen" KVNT " lds\n\ts_add_u32 m0, m0, 0x400\n\t"
#define KD4(so) KD(0, so) KD(1, so) KD(2, so) KD(3, so)
    asm volatile("s_mov_b32 %[keep], m0\n\ts_mov_b32 m0, %[lb]\n\t" KD4("0") KD4("%[s1]") KD4("%[s2]") KD4("%[s3]") "s_mov_b32 m0, %[keep]"
                 : [keep] "=&s"(keep)
                 : [v0] "v"(vo[0]), [v1] "v"(vo[1]), [v2] "v"(vo[2]), [v3] "v"(vo[3]), [rs] "s"(rK), [lb] "s"(ldsb), [s1] "s"(4096), [s2] "s"(8192), [s3] "s"(12288)
                 : "memory", "scc");
#undef KD4
#undef KD
  }
  FENCE();
  u32x4 d1[8];
  if (w == 0) {
#pragma unroll
    for (int i = 0; i < 8; ++i) d1[i] = ld128<SC1>(rd1, hg * 8192 + l * 16, i * 1024);
  }
  FENCE();
  {
    const int uc0 = u >= 0 ? u : 0;
    const int rec = aoff(xg * 32 + (2 * wpp + (w >> 2)) * 8 + (l & 7));
    X.aA = rec + ((w & 3) * 32 + 8 * g) * 2; X.aAml = rec + 256;
    X.aApub = vsel((l & 3) == 0, aoff(cu) + l * 4, vsel(l == 1, aoff(cu) + 256, OOB));
    X.aB0 = WS_B + (l >> 3) * 1024 + w * 128 + (l & 7) * 16;
    X.aB2 = WS_B + (l >> 3) * 1024 + s * 128 + (l & 7) * 16;
    X.aBpub = vsel((l & 7) == 0, WS_B + ((2 * xg + wpp) >> 3) * 16384 + ((2 * xg + wpp) & 7) * 1024 + wj * 128 + l * 2, OOB);
    X.o_bw = w * 16384 + 8192 + (l >> 3) * 144 + (l & 7) * 16; X.o_br = w * 16384 + 8192 + c * 144 + g * 16;
    pin(X.o_bw); pin(X.o_br);
    const int cc = WS_C + w * 16384 + (l >> 3) * 1024 + s * 128 + (l & 7) * 16;
    X.aC0 = cc; X.aC1 = vsel(64 + 8 * w + (l >> 3) < NU, cc + 8 * 16384, OOB);
    X.aCpub = vsel((l & 7) == 0, WS_C + (uc0 >> 3) * 16384 + (uc0 & 7) * 1024 + w * 128 + l * 2, OOB);
    X.aPr = doff(cu) + 1408;
    X.pl = wsa + doff(cu) + 1408;
    if (w == 3) {
      unsigned d0;
      asm volatile("s_load_dword %0, %1, 0x0\n\ts_waitcnt lgkmcnt(0)" : "=s"(d0) : "s"(wsa + aoff(cu) + 384) : "memory");
    }
    X.aDg = doff(hg * 8 + w) + vsel(l < 16, (l >> 1) * 128 + (l & 1) * 16, vsel(l < 32, 768 + l * 16, 1280));
    X.aDq = vsel((l & 55) == 0, doff(cu) + w * 128 + l * 2, OOB);
    X.aDx = vsel(w == 0 ? (l & 3) == 0 : l == 0, doff(cu) + (w == 0 ? 1024 : 1280) + l * 4, OOB);
    pin(X.aA); pin(X.aAml); pin(X.aApub); pin(X.aB0); pin(X.aB2); pin(X.aBpub); pin(X.aC0); pin(X.aC1); pin(X.aCpub); pin(X.aDg); pin(X.aDq); pin(X.aDx);
    X.o_ob = (w * 128 + 8 * c + 4 * g) * 4; X.o_l8 = l * 8; X.o_ml = (l & 7) * 8;
    X.o_yw = ((16 * (g & 1) + c) * 8 + ((w + c) & 7)) * 4; X.o_c32 = c * 32;
    X.o_sw = ((4 * g + ((l >> 3) & 1) * 2 + ((l >> 2) & 1)) * 8 + ((w + 2 * g) & 7)) * 4;
    X.o_cw = ((8 * (l & 7) + (l >> 3)) * 8 + ((w + l) & 7)) * 4; X.o_l32 = l * 32; X.o_l4 = l * 4;
    X.o_xw = w * 256 + l * 2; X.o_xr = w * 256 + (c & 1) * 128 + g * 16;
    X.o_qr = (l & 7) * 288 + g * 16; X.o_st = (l & 7) * 16;
    X.o_dd = vsel(l < 16, (int)__builtin_offsetof(Lds, qrec) + w * 288 + l * 16, vsel(l < 32, (int)__builtin_offsetof(Lds, xfull) + w * 256 + (l - 16) * 16, (int)__builtin_offsetof(Lds, st2) + w * 16));
    X.o_xs = (8 * (l & 7) + (l >> 3)) * 4;
    pin(X.o_ob); pin(X.o_yw); pin(X.o_sw); pin(X.o_cw); pin(X.o_xr); pin(X.o_qr); pin(X.o_dd); pin(X.o_xs);
  }
  LWAIT();
  __builtin_amdgcn_s_barrier();
  FENCE();
#pragma unroll
  for (int i = 0; i < 16; ++i) vr[i] = ld128<KV_AUX>(rV, w * 16384 + g * 4096 + c * 16, i * 256);
  FENCE();
  const u64 epoch = (((u64)ep.y << 32) | ep.x) + 1;
  asm volatile("s_waitcnt vmcnt(16)" ::: "memory");
  {
    const unsigned char* st = L.big[w];
#pragma unroll
    for (int T = 0; T < 4; ++T)
#pragma unroll
      for (int i = 0; i < 4; ++i) kf[4 * T + i] = *(const u32x4*)(st + (16 * T + c) * 256 + (((4 * i + g) ^ c) * 16));
  }
  if (w == 0) {
    const unsigned tag = (unsigned)epoch;
    unsigned bad = 0;
#pragma unroll
    for (int i = 0; i < 8; ++i) bad |= (d1[i].y ^ tag) | (d1[i].w ^ tag);
    if (UNLIKELY(!__all(bad == 0u))) {
      for (unsigned sp_ = 0; sp_ < SPIN_MAX; ++sp_) {
        asm volatile("" ::: "memory");
        bad = 0;
#pragma unroll
        for (int i = 0; i < 8; ++i) { d1[i] = ld128<SC1>(rd1, hg * 8192 + l * 16, i * 1024); bad |= (d1[i].y ^ tag) | (d1[i].w ^ tag); }
        if (__all(bad == 0u)) break;
      }
    }
    float qa = 0.f, qc = 0.f, ca = 0.f, cb = 0.f;
#pragma unroll
    for (int i = 0; i < 8; ++i) { qa += lo(d1[i].x); ca += hi(d1[i].x); qc += lo(d1[i].z); cb += hi(d1[i].z); }
    L.qb[l] = pk2(qa, qc);
    L.csb[l] = pk2(ca, cb);
    LWAIT();
    if (l == 0) __hip_atomic_store(&L.qflag, 1u, __ATOMIC_RELAXED, __HIP_MEMORY_SCOPE_WORKGROUP);
  } else {
    for (unsigned sp_ = 0; sp_ < (SPIN_MAX << 4); ++sp_) {
      if (__hip_atomic_load(&L.qflag, __ATOMIC_RELAXED, __HIP_MEMORY_SCOPE_WORKGROUP) != 0u) break;
      __builtin_amdgcn_s_sleep(1);
    }
  }
  asm volatile("" ::: "memory");
  float kcs;
  u32x4 rA[4], wA[4], wqB[2], xf[2];
  unsigned rB[8], b1r, xres_r, b2_r;
  float cs1 = 0.f, b1v = 0.f, b2v = 0.f, xres1 = 0.f;
  const int uc = u >= 0 ? u : 0;
  const int xcol = 8 * (l & 7) + (l >> 3);
  {
    f32x4 sc[4];
#pragma unroll
    for (int T = 0; T < 4; ++T) sc[T] = f32x4{0.f, 0.f, 0.f, 0.f};
#pragma unroll
    for (int i = 0; i < 4; ++i) {
      const u32x4 qf = *(const u32x4*)&L.qb[16 * i + 4 * g];
#pragma unroll
      for (int T = 0; T < 4; ++T) sc[T] = mfma(qf, kf[4 * T + i], sc[T]);
    }
    const float tmax = max16(fmx(fmx3(sc[0][0], sc[1][0], sc[2][0]), sc[3][0]));
    const float tsel = sel4(g, sc[0][0], sc[1][0], sc[2][0], sc[3][0]);
    u32x4 fl = {0u, 0u, 0u, 0u};
    if (w >= 6) fl = ld128<SC1>(rws, WS_FLAGS + (t & 127) * 16);
    FENCE();
    pv_publish<0>(L, X, tsel, tmax, QS, vr, msk, [&]() {
      if (wocu) {
#pragma unroll
        for (int i = 0; i < 4; ++i) rA[i] = ld128<W_AUX>(rwo, ((4 * xg + 2 * wpp + (w >> 2)) * 128 + 32 * (w & 3) + 8 * i + (l >> 3)) * 1024 + wj * 128 + (l & 7) * 16);
      }
      const unsigned e0 = (unsigned)epoch, e1 = (unsigned)(epoch >> 32);
      if (UNLIKELY(w >= 6 && !__all(((fl.x ^ e0) | (fl.y ^ e1) | (fl.z ^ e0) | (fl.w ^ e1)) == 0u))) {
        for (unsigned sp_ = 0; sp_ < SPIN_MAX; ++sp_) {
          __builtin_amdgcn_s_sleep(16);
          asm volatile("" ::: "memory");
          fl = ld128<SC1>(rws, WS_FLAGS + (t & 127) * 16);
          if (__all(((fl.x ^ e0) | (fl.y ^ e1) | (fl.z ^ e0) | (fl.w ^ e1)) == 0u)) break;
        }
      }
    });
  }
  u64 tref = rtime();
  b1r = ld16<0>(rb1, (16 * uc + c) * 2);
  xres_r = ld16<0>(rx, (64 * s + xcol) * 2); b2_r = ld16<0>(rb2, (64 * s + xcol) * 2);
#pragma unroll
  for (int ci = 0; ci < 2; ++ci) xf[ci] = ld128<0>(rx, c == 0 ? (64 * w + 32 * ci + 8 * g) * 2 : 1024);
  if (!wocu) {
#pragma unroll
    for (int i = 0; i < 2; ++i) rA[i] = ld128<W_AUX>(rw1, (64 * w + 32 * i + (l >> 1)) * (FF * 2) + uc * 32 + (l & 1) * 16);
#pragma unroll
    for (int f = 0; f < 8; ++f) rA[2 + (f >> 2)][f & 3] = ld32<W_AUX>(rw2, (16 * uc + f) * 1024 + (t >> 1) * 4);
#pragma unroll
    for (int f = 0; f < 8; ++f) rB[f] = ld32<W_AUX>(rw2, (16 * uc + 8 + f) * 1024 + (t >> 1) * 4);
  }
  if (wocu) {
    wo_stage<0>(L, X, wA, [&]() {
      {
        unsigned char* st = L.big[w];
#pragma unroll
        for (int i = 0; i < 4; ++i) *(u32x4*)(st + (8 * i + (l >> 3)) * 128 + (l & 7) * 16) = rA[i];
        LWAIT();
#pragma unroll
        for (int n = 0; n < 4; ++n) {
          typedef short s16x4 __attribute__((ext_vector_type(4)));
          const unsigned base = (unsigned)(size_t)(__attribute__((address_space(3))) unsigned char*)st + (8 * g + (c >> 2)) * 128 + (16 * n + 4 * (c & 3)) * 2;
          const s16x4 lo4 = __builtin_amdgcn_ds_read_tr16_b64_v4i16((__attribute__((address_space(3))) s16x4*)(size_t)base), hi4 = __builtin_amdgcn_ds_read_tr16_b64_v4i16((__attribute__((address_space(3))) s16x4*)(size_t)(base + 512));
          const u32x2 l2 = __builtin_bit_cast(u32x2, lo4), h2 = __builtin_bit_cast(u32x2, hi4);
          wA[n] = u32x4{l2.x, l2.y, h2.x, h2.y}; pin(wA[n]);
        }
      }
      pin(wA[0]); pin(wA[1]); pin(wA[2]); pin(wA[3]);
    });
  }
  const u64 tpubB = rtime();
  b2v = lo(b2_r); xres1 = lo(xres_r) + b2v; pin(b2v); pin(xres1);
  if (!wocu) {
    {
      const u32x4 w1r[2] = {rA[0], rA[1]};
      u32x4 fr[2];
      make_frags(L, X, w1r, fr, 2048);
      wA[0] = fr[0]; wA[1] = fr[1];
    }
#pragma unroll
    for (int f = 0; f < 8; ++f) {
      const unsigned r0 = f < 4 ? rA[2 + (f >> 1)][(2 * f) & 3] : rB[2 * f - 8], r1 = f < 4 ? rA[2 + (f >> 1)][(2 * f + 1) & 3] : rB[2 * f - 7];
      wA[2 + (f >> 2)][f & 3] = (t & 1) ? __builtin_amdgcn_perm(r1, r0, 0x07060302u) : __builtin_amdgcn_perm(r1, r0, 0x05040100u);
    }
    pin(wA[0]); pin(wA[1]); pin(wA[2]); pin(wA[3]);
    b1v = lo(b1r); pin(b1v);
    const u32x4 ones = {ONE2, ONE2, ONE2, ONE2};
    f32x4 S = mfma(wA[0], ones, f32x4{0.f, 0.f, 0.f, 0.f});
    S = mfma(wA[1], ones, S);
    if (c == 0) {
#pragma unroll
      for (int i = 0; i < 4; ++i) L.cs1p[4 * g + i][w] = S[i];
    }
  }
  if (w == 6) {
    unsigned pa, pb;
    {
      LWAIT();
      for (unsigned sp_ = 0; sp_ < SPIN_MAX && __hip_atomic_load(&L.prb[2], __ATOMIC_RELAXED, __HIP_MEMORY_SCOPE_WORKGROUP) == 0u; ++sp_) __builtin_amdgcn_s_sleep(1);
      const unsigned p0 = L.prb[0], p1 = L.prb[1];
      const bool near0 = p0 > 250u && p0 < PCLEAN && p1 > p0 + 100u, near1 = p1 > 250u && p1 < PCLEAN && p0 > p1 + 100u;
      pa = near0 ? 1u : 2u; pb = near0 ? 2u : 1u;
      if (near0 || near1) goto probed;
      pa = p0; pb = p1;
      if (wocu) goto probed;
    }
    for (int tries = 0; tries < 8; ++tries) {
      u64 t0, t1, t2;
      st128x2(rws, l == 0 ? X.aPr : OOB, 0, u32x4{SENT, SENT, SENT, SENT});
      asm volatile("s_waitcnt vmcnt(0)\n\ts_memtime %0\n\ts_waitcnt lgkmcnt(0)" : "=s"(t0) :: "memory");
      const unsigned v0 = ld32<SC1>(rws, X.aPr, 0);
      asm volatile("s_waitcnt vmcnt(0)\n\ts_memtime %0\n\ts_waitcnt lgkmcnt(0)" : "=s"(t1) : "v"(v0) : "memory");
      const unsigned v1 = ld32<SC1>(rws, X.aPr, CPY);
      asm volatile("s_waitcnt vmcnt(0)\n\ts_memtime %0\n\ts_waitcnt lgkmcnt(0)" : "=s"(t2) : "v"(v1) : "memory");
      pa = (unsigned)(t1 - t0); pb = (unsigned)(t2 - t1);
      const unsigned mn = pa < pb ? pa : pb, mxp = pa < pb ? pb : pa;
      if ((mn < 600u && mxp > mn + 90u && mxp < 1000u) || (long long)(rtime() - (tref + PDL)) > 0) break;
    }
  probed:
    if (l == 0) { L.prb[3] = pa < pb ? 0u : (unsigned)CPY; }
  }
  mlp_stage<0>(L, X, tref, tpubB, wocu, xf, wA, cs1, b1v, xres1);
  {
    const float x2 = c_gather<0>(L, X, [&]() {
      const unsigned char* st = (const unsigned char*)&L.wqs[0][0];
#pragma unroll
      for (int ci = 0; ci < 2; ++ci)
#pragma unroll
        for (int j2 = 0; j2 < 4; ++j2) {
          const int d0 = 32 * ci + 8 * g + 2 * j2;
          const unsigned a = *(const u16*)(st + (d0 >> 2) * 1024 + (d0 & 3) * 256 + (16 * w + c) * 2), bb = *(const u16*)(st + ((d0 + 1) >> 2) * 1024 + ((d0 + 1) & 3) * 256 + (16 * w + c) * 2);
          wqB[ci][j2] = a | (bb << 16);
        }
      pin(wqB[0]); pin(wqB[1]);
    });
    const unsigned hh = pk2(x2, x2);
    const unsigned ll = pk2(x2 - lo(hh), 0.f);
    LP(u16, xhl, X.o_xw) = (u16)hh; LP(u16, xhl[0][1], X.o_xw) = (u16)ll;
    LWAIT();
    const u32x4 a0 = LP(const u32x4, xhl, X.o_xr), a1 = LP(const u32x4, xhl[0][0][32], X.o_xr);
    f32x4 d = mfma(a0, wqB[0], f32x4{0.f, 0.f, 0.f, 0.f});
    d = mfma(a1, wqB[1], d);
    const float q1 = d[0] + d[1];
    const unsigned pk = sanu(pk2(q1, dpp<XOR1>(q1)));
    const u32x4 v = {pk, dppu<SHL + 2>(pk), dppu<SHL + 4>(pk), dppu<SHL + 6>(pk)};
    st128x2(rws, X.aDq, 0, v);
    if (w == 0) {
      const unsigned xu = san(x2);
      const u32x4 vx = {xu, dppu<0x55>(xu), dppu<0xAA>(xu), dppu<0xFF>(xu)};
      st128x2(rws, X.aDx, 0, vx);
    } else if (w == 1) {
      const u32x4 ones = {ONE2, ONE2, ONE2, ONE2};
      const f32x4 z = {0.f, 0.f, 0.f, 0.f};
      f32x4 G = mfma(a0, a0, z), s1 = mfma(a0, ones, z);
      G = mfma(a1, a1, G); s1 = mfma(a1, ones, s1);
      float s2 = G[0] + G[1]; s2 += dpp<XOR1>(s2);
      st128x2(rws, X.aDx, 0, u32x4{san(s1[0] + s1[1]), san(s2), 0u, 0u});
    }
  }
  {
    u32x4 r4 = {0u, 0u, 0u, 0u};
    pin(r4);
    BAR();
    __builtin_amdgcn_s_sleep(SLP_D);
    gather_4(r4, X.aDg, rws, X.sd, 0x1FFFFFFFFull);
    FENCE();
    {
      f32x4 sk[4];
#pragma unroll
      for (int T = 0; T < 4; ++T) sk[T] = f32x4{0.f, 0.f, 0.f, 0.f};
#pragma unroll
      for (int i = 0; i < 4; ++i) {
        const u32x4 cf = *(const u32x4*)&L.csb[16 * i + 4 * g];
#pragma unroll
        for (int T = 0; T < 4; ++T) sk[T] = mfma(cf, kf[4 * T + i], sk[T]);
      }
      kcs = sel4(g, sk[0][0], sk[1][0], sk[2][0], sk[3][0]); pin(kcs);
    }
    FENCE();
    VMWAIT(0, "+v"(r4));
    {
      bool bad = mx4(r4) == SENT;
      if (UNLIKELY(__any(bad))) {
        for (unsigned sp_ = 0; sp_ < SPIN_MAX; ++sp_) {
          gather_4(r4, X.aDg, rws, X.sd, __ballot(bad));
          VMWAIT(0, "+v"(r4));
          bad = mx4(r4) == SENT;
          if (!__any(bad)) break;
        }
      }
    }
    if (l <= 32) *(u32x4*)((unsigned char*)&L + X.o_dd) = r4;
    BAR();
    u32x4 qf[4];
#pragma unroll
    for (int i = 0; i < 4; ++i) qf[i] = LP(const u32x4, qrec[0][16 * i], X.o_qr);
    const f32x2 st2 = LP(const f32x2, st2, X.o_st);
    f32x4 sc[4];
#pragma unroll
    for (int T = 0; T < 4; ++T) sc[T] = f32x4{0.f, 0.f, 0.f, 0.f};
#pragma unroll
    for (int i = 0; i < 4; ++i)
#pragma unroll
      for (int T = 0; T < 4; ++T) sc[T] = mfma(qf[i], kf[4 * T + i], sc[T]);
    const float mean = sum8l(st2[0]) * (1.f / 512);
    const float ms = sum8l(st2[1]) * (1.f / 512);
    const float scale = __builtin_amdgcn_rsqf(fmx(ms - mean * mean, 0.f) + 1e-5f) * QS;
    const float ta = tr16(sum4(sc[0]), sum4(sc[1])), tb = tr16(sum4(sc[2]), sum4(sc[3]));
    const float tsel = ((g & 2) ? tb : ta) - mean * kcs;
    const float tmax = mx32r(mx16r(max16(tsel)));
    pv_publish<1>(L, X, tsel, tmax, scale, vr, msk, []() {});
  }
  tref = rtime();
  float xres2 = 0.f;
  auto shadow2 = [&]() {
    xres2 = L.xfull[64 * s + xcol] + b2v; pin(xres2);
    if (!wocu) {
#pragma unroll
      for (int ci = 0; ci < 2; ++ci) {
        const f32x4 p0 = *(const f32x4*)&L.xfull[64 * w + 32 * ci + 8 * g], p1 = *(const f32x4*)&L.xfull[64 * w + 32 * ci + 8 * g + 4];
        const float xv8[8] = {p0[0], p0[1], p0[2], p0[3], p1[0], p1[1], p1[2], p1[3]};
#pragma unroll
        for (int j = 0; j < 4; ++j) {
          const unsigned hh = pk2(xv8[2 * j], xv8[2 * j + 1]);
          const unsigned ll = pk2(xv8[2 * j] - lo(hh), xv8[2 * j + 1] - hi(hh));
          xf[ci][j] = c == 0 ? hh : c == 1 ? ll : 0u;
        }
      }
      pin(xf[0]); pin(xf[1]);
    }
  };
  if (wocu) wo_stage<1>(L, X, wA, shadow2); else shadow2();
  const u64 tpubB2 = rtime();
  const bool fin = (xg == 0 && idx >= 24);
  if (u < 0 && !fin) return;
  mlp_stage<1>(L, X, tref, tpubB2, wocu, xf, wA, cs1, b1v, xres2);
  if (!fin) return;
  const float o = c_gather<1>(L, X, []() {});
  if (w == 0) {
    p.out[64 * s + l] = (u16)(pk2(o, o) & 0xffffu);
    if (s == 0 && l == 0) *(u64*)p.ws = epoch;
  }
}

extern "C" void kernel_launch(void* const* d_in, const int* in_sizes, int n_in, void* d_out, int out_size, void* d_ws, size_t ws_size, hipStream_t stream) {
  Params p;
  p.x = (const u16*)d_in[0]; p.K = (const u16*)d_in[1]; p.V = (const u16*)d_in[2]; p.wq = (const u16*)d_in[3]; p.wo = (const u16*)d_in[4];
  p.w1 = (const u16*)d_in[6]; p.b1 = (const u16*)d_in[7]; p.w2 = (const u16*)d_in[8]; p.b2 = (const u16*)d_in[9];
  p.out = (u16*)d_out; p.ws = (unsigned char*)d_ws;
  hipLaunchKernelGGL(fused_kernel, dim3(NCU), dim3(NTHR), 0, stream, p);
}
```
